```python
import torch

def _softmax(x, axis=-1):
    return torch.softmax(x, dim=axis)
def _gelu(x):
    return torch.nn.functional.gelu(x, approximate="tanh")
def _silu(x):
    return torch.nn.functional.silu(x)
def _sigmoid(x):
    return torch.sigmoid(x)
def _layernorm(x, axis=-1, eps=1e-5):
    m = x.mean(dim=axis, keepdim=True)
    v = x.var(dim=axis, keepdim=True, unbiased=False)
    return (x - m) / torch.sqrt(v + eps)
def _scatter_add(tgt, idx, src, axis=0):
    return tgt.clone().index_add_(axis, idx, src)
def _segment_sum(src, seg, n):
    out = torch.zeros((n,) + src.shape[1:], dtype=src.dtype)
    return out.index_add_(0, seg, src)
def _pad(x, pw):
    flat = tuple(v for pair in reversed(pw) for v in pair)
    return torch.nn.functional.pad(x, flat)

kernel_name = 'gated_mix__sliding_block__mixer_token_mlp__prompt'

def setup_inputs():

    _w = (lambda a: (lambda t: t.to(torch.bfloat16) if t.is_floating_point() else t)(torch.as_tensor(a)))
    def _A_setup():
        B, S, D = 1, 2048, 512
        return {
            "x": _w((lambda *s: torch.randn(*s, dtype=torch.bfloat16) * 0.05)(B, S, D)),
            "wqkv": _w((lambda *s: torch.randn(*s, dtype=torch.bfloat16) * 0.8)(D, 3 * D)),
            "wo": _w((lambda *s: torch.randn(*s, dtype=torch.bfloat16) * 0.05)(D, D)),
        }
    _A_in = _A_setup()

    def _B_setup():
        B, S, D, F = 1, 2048, 512, 1376
        return {
            "x": _w((lambda *s: torch.randn(*s, dtype=torch.bfloat16) * 0.05)(B, S, D)),
            "w1": _w((lambda *s: torch.randn(*s, dtype=torch.bfloat16) * 3.2)(S, F) * (S ** -0.5)),
            "b1": _w((lambda *s: torch.randn(*s, dtype=torch.bfloat16) * 3.2)(F) * 0.02),
            "w2": _w((lambda *s: torch.randn(*s, dtype=torch.bfloat16) * 0.1)(F, S) * (F ** -0.5)),
            "b2": _w((lambda *s: torch.randn(*s, dtype=torch.bfloat16) * 0.1)(S) * 0.02),
        }
    _B_in = _B_setup()

    return {
        "A_x": _A_in["x"],
        "A_wqkv": _A_in["wqkv"],
        "A_wo": _A_in["wo"],
        "B_x": _B_in["x"],
        "B_w1": _B_in["w1"],
        "B_b1": _B_in["b1"],
        "B_w2": _B_in["w2"],
        "B_b2": _B_in["b2"],
        "gate_w": _w((lambda *s: torch.randn(*s, dtype=torch.bfloat16) * 0.05)(512) * 16.0),
    }

def reference(A_x, A_wqkv, A_wo, B_x, B_w1, B_b1, B_w2, B_b2, gate_w):
    def _A_ref(x, wqkv, wo):
        B, S, D = x.shape
        H = 32; Dh = D // H; W = 256
        qkv = (lambda x, *d: x.permute(*d))((x @ wqkv).reshape(B, S, 3, H, Dh), 2, 0, 3, 1, 4)
        q, k, v = qkv[0], qkv[1], qkv[2]
        scores = torch.einsum("bhqd,bhkd->bhqk", q, k) * (Dh ** -0.5)
        idx = torch.arange(S)
        mask = (idx[:, None] - idx[None, :] >= 0) & (idx[:, None] - idx[None, :] < W)
        scores = torch.where(mask[None, None], scores, -1e9)
        p = _softmax(scores, axis=-1)
        o = torch.einsum("bhqk,bhkd->bhqd", p, v)
        return (lambda x, *d: x.permute(*d))(o, 0, 2, 1, 3).reshape(B, S, D) @ wo

    def _B_ref(x, w1, b1, w2, b2):
        B, S, D = x.shape
        h = _layernorm(x, axis=-1)
        u = torch.einsum("bsd,sf->bdf", h, w1) + b1
        u = _gelu(u)
        y = torch.einsum("bdf,fs->bds", u, w2) + b2
        return (lambda x, *d: x.permute(*d))(y, 0, 2, 1)

    x = A_x
    g = _sigmoid((x * gate_w).sum(-1, keepdims=True))
    a = _A_ref(x, A_wqkv, A_wo)
    b = _B_ref(x, B_w1, B_b1, B_w2, B_b2)
    return g * a + (1 - g) * b
```

```cpp
#include <hip/hip_runtime.h>
#include <hip/hip_bf16.h>
typedef __hip_bfloat16 bf16;
#define S 2048
#define D 512
#define F 1376
#define H 32
#define DH 16
#define W 256

__device__ __forceinline__ float b2f(bf16 x) { return __bfloat162float(x); }
__device__ __forceinline__ bf16 f2b(float x) { return __float2bfloat16(x); }

__global__ void k_ln(const bf16* x, const bf16* gw, bf16* h, float* g) {
  int row = blockIdx.x; int lane = threadIdx.x;
  float v[8]; float s = 0, gs = 0;
  for (int j = 0; j < 8; ++j) { v[j] = b2f(x[row * D + lane * 8 + j]); s += v[j]; gs += v[j] * b2f(gw[lane * 8 + j]); }
  for (int o = 32; o; o >>= 1) { s += __shfl_xor(s, o); gs += __shfl_xor(gs, o); }
  float m = s / D; float q = 0;
  for (int j = 0; j < 8; ++j) { float d = v[j] - m; q += d * d; }
  for (int o = 32; o; o >>= 1) q += __shfl_xor(q, o);
  float r = rsqrtf(q / D + 1e-5f);
  for (int j = 0; j < 8; ++j) h[row * D + lane * 8 + j] = f2b((v[j] - m) * r);
  if (lane == 0) g[row] = 1.f / (1.f + __expf(-gs));
}
template <int EPI>
__global__ void k_gemm(const bf16* A, int sam, int sak, const bf16* B, int sbk, int sbn, int M, int N, int K,
                       bf16* C, int ldc, const bf16* bias, float* Cf) {
  int n = blockIdx.x * 64 + (threadIdx.x & 63); int m = blockIdx.y * 4 + (threadIdx.x >> 6);
  if (n >= N || m >= M) return;
  float acc = 0;
  for (int k = 0; k < K; ++k) acc += b2f(A[(long)m * sam + (long)k * sak]) * b2f(B[(long)k * sbk + (long)n * sbn]);
  if (EPI == 0) C[(long)m * ldc + n] = f2b(acc);
  if (EPI == 1) { float u = acc + b2f(bias[n]); float t = tanhf(0.7978845608028654f * (u + 0.044715f * u * u * u)); C[(long)m * ldc + n] = f2b(0.5f * u * (1.f + t)); }
  if (EPI == 2) Cf[(long)m * ldc + n] = acc + b2f(bias[m]);
  if (EPI == 3) Cf[(long)m * ldc + n] = acc;
}
__global__ void k_attn(const bf16* qkv, bf16* o) {
  int q = blockIdx.x * 64 + (threadIdx.x & 63); int hd = blockIdx.y;
  float qv[DH]; for (int j = 0; j < DH; ++j) qv[j] = b2f(qkv[q * 3 * D + hd * DH + j]);
  int k0 = q - (W - 1); if (k0 < 0) k0 = 0;
  float mx = -1e30f;
  for (int k = k0; k <= q; ++k) { float s = 0; for (int j = 0; j < DH; ++j) s += qv[j] * b2f(qkv[k * 3 * D + D + hd * DH + j]); s *= 0.25f; mx = fmaxf(mx, s); }
  float l = 0, acc[DH]; for (int j = 0; j < DH; ++j) acc[j] = 0;
  for (int k = k0; k <= q; ++k) { float s = 0; for (int j = 0; j < DH; ++j) s += qv[j] * b2f(qkv[k * 3 * D + D + hd * DH + j]); s *= 0.25f; float p = __expf(s - mx); l += p;
    for (int j = 0; j < DH; ++j) acc[j] += p * b2f(qkv[k * 3 * D + 2 * D + hd * DH + j]); }
  for (int j = 0; j < DH; ++j) o[q * D + hd * DH + j] = f2b(acc[j] / l);
}
__global__ void k_comb(const float* a, const float* b, const float* g, bf16* out) {
  int i = blockIdx.x * 256 + threadIdx.x; int s = i / D; float gg = g[s];
  out[i] = f2b(gg * a[i] + (1.f - gg) * b[i]);
}
extern "C" void kernel_launch(void* const* d_in, const int* in_sizes, int n_in, void* d_out, int out_size, void* d_ws, size_t ws_size, hipStream_t stream) {
  const bf16* x = (const bf16*)d_in[0]; const bf16* wqkv = (const bf16*)d_in[1]; const bf16* wo = (const bf16*)d_in[2];
  const bf16* w1 = (const bf16*)d_in[4]; const bf16* b1 = (const bf16*)d_in[5]; const bf16* w2 = (const bf16*)d_in[6]; const bf16* b2 = (const bf16*)d_in[7]; const bf16* gw = (const bf16*)d_in[8];
  char* ws = (char*)d_ws;
  bf16* h = (bf16*)ws; ws += S * D * 2;
  bf16* qkv = (bf16*)ws; ws += S * 3 * D * 2;
  bf16* o = (bf16*)ws; ws += S * D * 2;
  bf16* u = (bf16*)ws; ws += D * F * 2;
  float* g = (float*)ws; ws += S * 4;
  float* a = (float*)ws; ws += S * D * 4;
  float* b = (float*)ws; ws += S * D * 4;
  k_ln<<<S, 64, 0, stream>>>(x, gw, h, g);
  k_gemm<0><<<dim3(3 * D / 64, S / 4), 256, 0, stream>>>(x, D, 1, wqkv, 3 * D, 1, S, 3 * D, D, qkv, 3 * D, nullptr, nullptr);
  k_attn<<<dim3(S / 64, H), 64, 0, stream>>>(qkv, o);
  k_gemm<3><<<dim3(D / 64, S / 4), 256, 0, stream>>>(o, D, 1, wo, D, 1, S, D, D, nullptr, D, nullptr, a);
  k_gemm<1><<<dim3((F + 63) / 64, D / 4), 256, 0, stream>>>(h, 1, D, w1, F, 1, D, F, S, u, F, b1, nullptr);
  k_gemm<2><<<dim3(D / 64, S / 4), 256, 0, stream>>>(w2, 1, S, u, 1, F, S, D, F, nullptr, D, b2, b);
  k_comb<<<S * D / 256, 256, 0, stream>>>(a, b, g, (bf16*)d_out);
}
```

```cpp
#include <hip/hip_runtime.h>
#include <hip/hip_bf16.h>
#include <stdint.h>

#define LAS __attribute__((address_space(3)))
#define DI __device__ __forceinline__
typedef __attribute__((ext_vector_type(8))) __bf16 bf16x8;
typedef __attribute__((ext_vector_type(2))) __bf16 bf16x2;
typedef __attribute__((ext_vector_type(4))) short s16x4;
typedef __attribute__((ext_vector_type(8))) short s16x8;
typedef __attribute__((ext_vector_type(2))) float f32x2;
typedef __attribute__((ext_vector_type(4))) float f32x4;
typedef __attribute__((ext_vector_type(16))) float f32x16;
typedef __attribute__((ext_vector_type(2))) unsigned u32x2;
typedef __attribute__((ext_vector_type(4))) unsigned u32x4;
typedef LAS char* lptr;

constexpr int S_ = 2048, D_ = 512, F_ = 1376, NH = 32;
constexpr size_t WS_Q = 0, WS_K = 2u << 20, WS_V = 4u << 20, WS_H = 6u << 20, WS_O = 8u << 20, WS_U = 10u << 20, WS_G = 12u << 20;
constexpr float QSCALE = 0.25f * 1.4426950408889634f;

#define MFMA32(a, b, c) __builtin_amdgcn_mfma_f32_32x32x16_bf16(a, b, c, 0, 0, 0)
#define MFMA16(a, b, c) __builtin_amdgcn_mfma_f32_16x16x32_bf16(a, b, c, 0, 0, 0)
#define WAIT_BAR(N) asm volatile("s_waitcnt vmcnt(" #N ") lgkmcnt(0)\n\ts_barrier" ::: "memory")

DI int rfl(int x) { return __builtin_amdgcn_readfirstlane(x); }
DI void glds16(const void* sbase, unsigned voff, unsigned lds_dst) {
  unsigned sv;
  asm volatile("s_mov_b32 %0, m0\n\ts_mov_b32 m0, %3\n\ts_nop 0\n\tglobal_load_lds_dwordx4 %1, %2\n\ts_mov_b32 m0, %0"
               : "=&s"(sv) : "v"(voff), "s"(sbase), "s"(lds_dst) : "memory");
}
DI bf16x8 ldsr128(lptr p) { return *(const LAS bf16x8*)p; }
DI s16x4 ldstr(lptr p) { return __builtin_amdgcn_ds_read_tr16_b64_v4i16((LAS s16x4*)p); }
DI bf16x8 cat8(s16x4 a, s16x4 b) { return __builtin_bit_cast(bf16x8, __builtin_shufflevector(a, b, 0, 1, 2, 3, 4, 5, 6, 7)); }
DI unsigned pk2(float a, float b) { f32x2 f = {a, b}; return __builtin_bit_cast(unsigned, __builtin_convertvector(f, bf16x2)); }
DI float bflo(unsigned u) { return __uint_as_float(u << 16); }
DI float bfhi(unsigned u) { return __uint_as_float(u & 0xffff0000u); }
DI int crow(int i, int h) { return (i & 3) + 8 * (i >> 2) + 4 * h; }

template <int PER>
DI void wait_bar_n(int n) {
  if (PER == 2) {
    switch (n) { case 0: WAIT_BAR(0); break; case 1: WAIT_BAR(2); break; case 2: WAIT_BAR(4); break; case 3: WAIT_BAR(6); break;
                 case 4: WAIT_BAR(8); break; case 5: WAIT_BAR(10); break; case 6: WAIT_BAR(12); break; default: WAIT_BAR(14); break; }
  } else {
    switch (n) { case 0: WAIT_BAR(0); break; case 1: WAIT_BAR(4); break; case 2: WAIT_BAR(8); break; default: WAIT_BAR(12); break; }
  }
}

template <bool ISV>
DI void qkv_body(const char* __restrict__ x, const char* __restrict__ wqkv, char* __restrict__ ws, int tm, int tn, char* smem) {
  constexpr int NS = 4, PD = 3, NC = 8, SLOT = 32768;
  const int lane = threadIdx.x & 63, w = rfl(threadIdx.x >> 6);
  const int r = lane & 31, h = lane >> 5;
  const unsigned lds0 = (unsigned)(uintptr_t)smem;
  lptr L = (lptr)smem;
  const unsigned xv0 = (lane >> 3) * 1024 + (((lane & 7) ^ (lane >> 4)) << 4);
  const unsigned xv1 = (8 + (lane >> 3)) * 1024 + (((lane & 7) ^ (4 + (lane >> 4))) << 4);
  const unsigned wv = (lane >> 4) * 3072 + (((lane & 15) ^ ((lane >> 4) << 2)) << 4);
  const char* xb = x + (size_t)(tm * 128 + w * 16) * 1024;
  const char* wb = wqkv + (size_t)(w * 8) * 3072 + tn * 256;
  auto issue = [&](int c) {
    const unsigned sl = lds0 + (c & (NS - 1)) * SLOT;
    glds16(xb + c * 128, xv0, sl + w * 2048);
    glds16(xb + c * 128, xv1, sl + w * 2048 + 1024);
    glds16(wb + (size_t)c * 64 * 3072, wv, sl + 16384 + w * 2048);
    glds16(wb + (size_t)c * 64 * 3072 + 4 * 3072, wv, sl + 16384 + w * 2048 + 1024);
  };
#pragma unroll
  for (int c = 0; c < PD; ++c) issue(c);
  const int wm = w >> 2, wn = w & 3;
  int xa[4];
#pragma unroll
  for (int s = 0; s < 4; ++s) xa[s] = (wm * 64 + r) * 128 + (((2 * s + h) ^ ((r >> 1) & 7)) << 4);
  const int q = (lane & 15) >> 2, p = lane & 3, blk = (lane >> 4) & 1;
  const int wa = 16384 + (8 * h + q) * 256 + (((wn * 4 + blk * 2 + (p >> 1)) ^ (q << 2)) << 4) + (p & 1) * 8;
  f32x16 acc[2] = {};
  for (int c = 0; c < NC; ++c) {
    wait_bar_n<4>(min(NC - 1 - c, PD - 1));
    if (c + PD < NC) issue(c + PD);
    lptr sl = L + (c & (NS - 1)) * SLOT;
#pragma unroll
    for (int s = 0; s < 4; ++s) {
      bf16x8 wf = cat8(ldstr(sl + wa + s * 4096), ldstr(sl + wa + s * 4096 + 1024));
      bf16x8 x0 = ldsr128(sl + xa[s]), x1 = ldsr128(sl + xa[s] + 4096);
      if (ISV) { acc[0] = MFMA32(x0, wf, acc[0]); acc[1] = MFMA32(x1, wf, acc[1]); }
      else     { acc[0] = MFMA32(wf, x0, acc[0]); acc[1] = MFMA32(wf, x1, acc[1]); }
    }
  }
  const int hd0 = (tn & 3) * 8 + wn * 2;
#pragma unroll
  for (int mi = 0; mi < 2; ++mi) {
    const int tb = tm * 4 + wm * 2 + mi;
    if (ISV) {
      char* dst = ws + WS_V + ((size_t)((hd0 + (r >> 4)) * 64 + tb) * 64 + h * 16 + (r & 15)) * 16;
#pragma unroll
      for (int s = 0; s < 2; ++s) {
        u32x4 v;
#pragma unroll
        for (int j = 0; j < 4; ++j) v[j] = pk2(acc[mi][8 * s + 2 * j], acc[mi][8 * s + 2 * j + 1]);
        *(u32x4*)(dst + s * 512) = v;
      }
    } else {
      const float sc = tn < 4 ? QSCALE : 1.0f;
      char* dst = ws + (tn < 4 ? WS_Q : WS_K) + ((size_t)(hd0 * 64 + tb) * 64 + lane) * 16;
#pragma unroll
      for (int s = 0; s < 2; ++s) {
        u32x4 v;
#pragma unroll
        for (int j = 0; j < 4; ++j) v[j] = pk2(acc[mi][8 * s + 2 * j] * sc, acc[mi][8 * s + 2 * j + 1] * sc);
        *(u32x4*)(dst + s * 65536) = v;
      }
    }
  }
}

DI void ln_body(const char* __restrict__ x, const char* __restrict__ gw, char* __restrict__ ws, int ks, char* smem) {
  constexpr int RS = 1056;
  const int lane = threadIdx.x & 63, w = rfl(threadIdx.x >> 6);
  lptr L = (lptr)smem;
  u32x4 xv[4];
#pragma unroll
  for (int i = 0; i < 4; ++i) xv[i] = *(const u32x4*)(x + (size_t)(ks * 32 + w * 4 + i) * 1024 + lane * 16);
  const u32x4 gv = *(const u32x4*)(gw + lane * 16);
#pragma unroll
  for (int i = 0; i < 4; ++i) {
    float v[8], s = 0.f, gs = 0.f;
#pragma unroll
    for (int j = 0; j < 4; ++j) {
      v[2 * j] = bflo(xv[i][j]); v[2 * j + 1] = bfhi(xv[i][j]);
      s += v[2 * j] + v[2 * j + 1];
      gs += v[2 * j] * bflo(gv[j]) + v[2 * j + 1] * bfhi(gv[j]);
    }
#pragma unroll
    for (int o = 32; o; o >>= 1) { s += __shfl_xor(s, o); gs += __shfl_xor(gs, o); }
    const float m = s * (1.0f / D_);
    float qq = 0.f;
#pragma unroll
    for (int j = 0; j < 8; ++j) { v[j] -= m; qq += v[j] * v[j]; }
#pragma unroll
    for (int o = 32; o; o >>= 1) qq += __shfl_xor(qq, o);
    const float rs = __builtin_amdgcn_rsqf(qq * (1.0f / D_) + 1e-5f);
    u32x4 o;
#pragma unroll
    for (int j = 0; j < 4; ++j) o[j] = pk2(v[2 * j] * rs, v[2 * j + 1] * rs);
    *(LAS u32x4*)(L + (w * 4 + i) * RS + lane * 16) = o;
    if (lane == 0) ((float*)(ws + WS_G))[ks * 32 + w * 4 + i] = 1.0f / (1.0f + __expf(-gs));
  }
  __syncthreads();
  const int g = lane >> 4, q = (lane & 15) >> 2, p = lane & 3;
#pragma unroll
  for (int i = 0; i < 4; ++i) {
    const int db = w * 4 + i;
    lptr a = L + (4 * g + q) * RS + db * 32 + p * 8;
    bf16x8 f = cat8(ldstr(a), ldstr(a + 16 * RS));
    *(bf16x8*)(ws + WS_H + ((size_t)(w * 64 + ks) * 4 + i) * 1024 + lane * 16) = f;
  }
}

__global__ __launch_bounds__(512, 2) void k1(const char* __restrict__ x, const char* __restrict__ wqkv, const char* __restrict__ gw, char* __restrict__ ws) {
  extern __shared__ __attribute__((aligned(16))) char smem[];
  const int xcd = blockIdx.x & 7, idx = blockIdx.x >> 3;
  if (idx < 24) {
    const int tm = (xcd >> 1) * 4 + idx / 6, tn = (xcd & 1) * 6 + idx % 6;
    if (tn >= 8) qkv_body<true>(x, wqkv, ws, tm, tn, smem);
    else qkv_body<false>(x, wqkv, ws, tm, tn, smem);
  } else {
    ln_body(x, gw, ws, xcd * 8 + (idx - 24), smem);
  }
}

DI float gelu_tanh(float u) {
  const float z = u * (0.7978845608028654f + 0.7978845608028654f * 0.044715f * u * u);
  const float e = __builtin_amdgcn_exp2f(z * (-2.0f * 1.4426950408889634f));
  return u * __builtin_amdgcn_rcpf(1.0f + e);
}
DI void g1_body(const char* __restrict__ w1, const char* __restrict__ b1, char* __restrict__ ws, int dt, int ft, char* smem) {
  constexpr int NS = 5, PD = 4, NC = 32, SLOT = 14336;
  const int lane = threadIdx.x & 63, w = rfl(threadIdx.x >> 6);
  const unsigned lds0 = (unsigned)(uintptr_t)smem;
  lptr L = (lptr)smem;
  const int lrow = lane / 6; int lch = lane - lrow * 6;
  if (ft == 28 && lch > 3) lch = 3;
  const unsigned wv = lrow * 2752 + lch * 16;
  const unsigned hv = lane * 16;
  const char* hb = ws + WS_H + (size_t)dt * 64 * 4096 + w * 1024;
  const char* wb = w1 + (size_t)(w * 8) * 2752 + ft * 96;
  int islot = 0;
  auto issue = [&](int c) {
    const unsigned sl = lds0 + islot * SLOT;
    glds16(hb + (size_t)c * 8192, hv, sl + w * 1024);
    if (lane < 48) glds16(wb + (size_t)c * 64 * 2752, wv, sl + 8192 + w * 768);
    islot = islot == NS - 1 ? 0 : islot + 1;
  };
#pragma unroll
  for (int c = 0; c < PD; ++c) issue(c);
  const int kh = w >> 2, db = w & 3;
  const int g = lane >> 4, q = (lane & 15) >> 2, p = lane & 3;
  const int ha = w * 1024 + lane * 16;
  const int wa = 8192 + (32 * kh + 4 * g + q) * 96 + p * 8;
  f32x4 acc[3] = {};
  int rslot = 0;
  for (int c = 0; c < NC; ++c) {
    wait_bar_n<2>(min(NC - 1 - c, PD - 1));
    if (c + PD < NC) issue(c + PD);
    lptr sl = L + rslot * SLOT;
    rslot = rslot == NS - 1 ? 0 : rslot + 1;
    bf16x8 hf = ldsr128(sl + ha);
#pragma unroll
    for (int fb = 0; fb < 3; ++fb) {
      bf16x8 wf = cat8(ldstr(sl + wa + fb * 32), ldstr(sl + wa + fb * 32 + 16 * 96));
      acc[fb] = MFMA16(wf, hf, acc[fb]);
    }
  }
  WAIT_BAR(0);
  if (kh == 1) {
#pragma unroll
    for (int fb = 0; fb < 3; ++fb) *(LAS f32x4*)(L + ((db * 3 + fb) * 64 + lane) * 16) = acc[fb];
  }
  WAIT_BAR(0);
  if (kh == 0) {
    const int nfb = ft == 28 ? 2 : 3;
#pragma unroll
    for (int fb = 0; fb < 3; ++fb) {
      if (fb < nfb) {
        const f32x4 o = *(const LAS f32x4*)(L + ((db * 3 + fb) * 64 + lane) * 16);
        const u32x2 bb = *(const u32x2*)(b1 + (ft * 48 + fb * 16 + 4 * g) * 2);
        const float u0 = gelu_tanh(acc[fb][0] + o[0] + bflo(bb[0])), u1 = gelu_tanh(acc[fb][1] + o[1] + bfhi(bb[0]));
        const float u2 = gelu_tanh(acc[fb][2] + o[2] + bflo(bb[1])), u3 = gelu_tanh(acc[fb][3] + o[3] + bfhi(bb[1]));
        u32x2 v = {pk2(u0, u1), pk2(u2, u3)};
        const int fblk = ft * 3 + fb;
        *(u32x2*)(ws + WS_U + ((size_t)((dt * 86 + fblk) * 2 + (db >> 1)) * 64 + (g >> 1) * 32 + (db & 1) * 16 + (lane & 15)) * 16 + (g & 1) * 8) = v;
      }
    }
  }
}

DI float max16(const f32x16& s, float m) {
#pragma unroll
  for (int i = 0; i < 16; i += 2) m = __builtin_fmaxf(__builtin_fmaxf(s[i], s[i + 1]), m);
  return m;
}
DI void attn_body(char* __restrict__ ws, int head, int qb, char* smem) {
  constexpr int LK = 0, LV = 16384, LC = 32768;
  const int lane = threadIdx.x & 63, w = rfl(threadIdx.x >> 6);
  const int r = lane & 31, h = lane >> 5;
  const unsigned lds0 = (unsigned)(uintptr_t)smem;
  lptr L = (lptr)smem;
  const unsigned lv = lane * 16;
  const int T0 = qb * 8 - 8;
  const char* kb = ws + WS_K + (size_t)(head * 64 + T0 + w) * 1024;
  const char* vb = ws + WS_V + (size_t)(head * 64 + T0 + w) * 1024;
  glds16(kb + 8192, lv, lds0 + LK + (w + 8) * 1024);
  if (qb > 0) glds16(kb, lv, lds0 + LK + w * 1024);
  const bf16x8 qf = *(const bf16x8*)(ws + WS_Q + (size_t)(head * 64 + qb * 8 + w) * 1024 + lane * 16);
  glds16(vb + 8192, lv, lds0 + LV + (w + 8) * 1024);
  if (qb > 0) glds16(vb, lv, lds0 + LV + w * 1024);
  if (threadIdx.x < 4) {
    const unsigned cv = (threadIdx.x & 1) ? 0u : 0x3f803f80u;
    *(LAS u32x4*)(L + LC + (threadIdx.x >> 1) * 512 + (threadIdx.x & 1) * 16) = u32x4{cv, cv, cv, cv};
  }
  WAIT_BAR(0);
  const int t_lo = qb > 0 ? w + 1 : 8, t_hi = w + 8;
  lptr kp = L + LK + lane * 16;
  const f32x16 zero = {};
  f32x16 sb;
  {
    f32x16 sd = MFMA32(ldsr128(kp + (w + 8) * 1024), qf, zero);
    f32x16 sf;
    if (qb > 0) sf = MFMA32(ldsr128(kp + w * 1024), qf, zero);
    else {
#pragma unroll
      for (int i = 0; i < 16; ++i) sf[i] = -INFINITY;
    }
#pragma unroll
    for (int i = 0; i < 16; ++i) sb[i] = (crow(i, h) <= r) ? sd[i] : sf[i];
  }
  float m = max16(sb, -INFINITY);
  for (int t = t_lo; t < t_hi; ++t) {
    f32x16 s = MFMA32(ldsr128(kp + t * 1024), qf, zero);
    m = max16(s, m);
  }
  {
    auto rr = __builtin_amdgcn_permlane32_swap(__float_as_uint(m), __float_as_uint(m), false, false);
    m = __builtin_fmaxf(__uint_as_float(rr[0]), __uint_as_float(rr[1]));
  }
  f32x16 cin;
#pragma unroll
  for (int i = 0; i < 16; ++i) cin[i] = -m;
  const int vbase = r < 16 ? LV + h * 256 + r * 16 : (LC + ((r == 16 || r == 20) ? 0 : 16));
  const int vstr = r < 16 ? 1024 : 0;
  f32x16 o = {};
  {
    u32x4 pd[2], pf[2];
#pragma unroll
    for (int i = 0; i < 16; i += 2) {
      const float p0 = __builtin_amdgcn_exp2f(sb[i] - m), p1 = __builtin_amdgcn_exp2f(sb[i + 1] - m);
      const bool v0 = crow(i, h) <= r, v1 = crow(i + 1, h) <= r;
      pd[i >> 3][(i >> 1) & 3] = pk2(v0 ? p0 : 0.f, v1 ? p1 : 0.f);
      pf[i >> 3][(i >> 1) & 3] = pk2(v0 ? 0.f : p0, v1 ? 0.f : p1);
    }
    lptr vp = L + vbase + (w + 8) * vstr;
    o = MFMA32(ldsr128(vp), __builtin_bit_cast(bf16x8, pd[0]), o);
    o = MFMA32(ldsr128(vp + 512), __builtin_bit_cast(bf16x8, pd[1]), o);
    if (qb > 0) {
      lptr vq = L + vbase + w * vstr;
      o = MFMA32(ldsr128(vq), __builtin_bit_cast(bf16x8, pf[0]), o);
      o = MFMA32(ldsr128(vq + 512), __builtin_bit_cast(bf16x8, pf[1]), o);
    }
  }
  for (int t = t_lo; t < t_hi; ++t) {
    f32x16 s = MFMA32(ldsr128(kp + t * 1024), qf, cin);
    u32x4 pp[2];
#pragma unroll
    for (int i = 0; i < 16; i += 2) pp[i >> 3][(i >> 1) & 3] = pk2(__builtin_amdgcn_exp2f(s[i]), __builtin_amdgcn_exp2f(s[i + 1]));
    lptr vp = L + vbase + t * vstr;
    o = MFMA32(ldsr128(vp), __builtin_bit_cast(bf16x8, pp[0]), o);
    o = MFMA32(ldsr128(vp + 512), __builtin_bit_cast(bf16x8, pp[1]), o);
  }
  const float il = 1.0f / o[8];
  u32x4 ov;
#pragma unroll
  for (int j = 0; j < 4; ++j) ov[j] = pk2(o[2 * j] * il, o[2 * j + 1] * il);
  const int B = qb * 8 + w;
  *(u32x4*)(ws + WS_O + ((size_t)((B >> 1) * 32 + head) * 2 + (B & 1)) * 1024 + lane * 16) = ov;
}

__global__ __launch_bounds__(512, 4) void k2(const char* __restrict__ w1, const char* __restrict__ b1, char* __restrict__ ws) {
  extern __shared__ __attribute__((aligned(16))) char smem[];
  const int xcd = blockIdx.x & 7, idx = blockIdx.x >> 3;
  if (idx < 29) {
    const int Lx = xcd * 29 + idx;
    const int dh = Lx / 116, rem = Lx - dh * 116;
    g1_body(w1, b1, ws, dh * 4 + (rem & 3), rem >> 2, smem);
  } else {
    const int i2 = idx - 29;
    attn_body(ws, xcd * 4 + (i2 & 3), i2 >> 2, smem);
  }
}

__global__ __launch_bounds__(512, 2) void k3(const char* __restrict__ w2, const char* __restrict__ b2, const char* __restrict__ wo,
                                             const char* __restrict__ ws, char* __restrict__ out) {
  extern __shared__ __attribute__((aligned(16))) char smem[];
  constexpr int NS = 8, PD = 7, NC = 30, SLOT = 16384;
  const int xcd = blockIdx.x & 7, idx = blockIdx.x >> 3;
  const int st = (xcd >> 1) * 8 + (idx >> 2), dt = (xcd & 1) * 4 + (idx & 3);
  const int lane = threadIdx.x & 63, w = rfl(threadIdx.x >> 6);
  const int r = lane & 31, h = lane >> 5;
  const unsigned lds0 = (unsigned)(uintptr_t)smem;
  lptr L = (lptr)smem;
  const unsigned swz = ((lane & 7) ^ (((lane >> 4) & 1) << 2)) << 4;
  const unsigned v2 = (lane >> 3) * 4096 + swz, vo = (lane >> 3) * 1024 + swz, vl = lane * 16;
  const char* w2b = w2 + (size_t)(w * 8) * 4096 + st * 128;
  const char* wob = wo + (size_t)(w * 8) * 1024 + dt * 128;
  const char* ub = ws + WS_U + (size_t)dt * 86 * 2048 + w * 1024;
  const char* ob = ws + WS_O + (size_t)st * 32 * 2048 + w * 1024;
  auto issue = [&](int c) {
    const unsigned sl = lds0 + (c & (NS - 1)) * SLOT + w * 1024;
    if (c < 22) {
      const int kc = c == 0 ? 21 : c - 1;
      glds16(w2b + (size_t)kc * 64 * 4096, v2, sl);
      glds16(ub + (size_t)kc * 8192, vl, sl + 8192);
    } else {
      glds16(wob + (size_t)(c - 22) * 64 * 1024, vo, sl);
      glds16(ob + (size_t)(c - 22) * 8192, vl, sl + 8192);
    }
  };
  if (w < 4) issue(0);
#pragma unroll
  for (int c = 1; c < PD; ++c) issue(c);
  const int kh = w >> 2, mt = (w >> 1) & 1, nt = w & 1;
  const int q = (lane & 15) >> 2, p = lane & 3, blk = (lane >> 4) & 1;
  const int tok = st * 64 + nt * 32 + r;
  const float g = ((const float*)(ws + WS_G))[tok];
  const float bb2 = bflo((unsigned)((const unsigned short*)b2)[tok]);
  const int ta2 = (32 * kh + 8 * h + q) * 128 + (((nt * 4 + blk * 2 + (p >> 1)) ^ ((q >> 1) << 2)) << 4) + (p & 1) * 8;
  const int tao = (32 * kh + 4 * h + q) * 128 + (((mt * 4 + blk * 2 + (p >> 1)) ^ ((q >> 1) << 2)) << 4) + (p & 1) * 8;
  const int fa2 = 8192 + (4 * kh + mt) * 1024 + lane * 16;
  const int fao = 8192 + (4 * kh + nt) * 1024 + lane * 16;
  f32x16 ay = {}, aa = {};
  for (int c = 0; c < 22; ++c) {
    wait_bar_n<2>(PD - 1);
    issue(c + PD);
    lptr sl = L + (c & (NS - 1)) * SLOT;
    if (c > 0 || kh == 0) {
#pragma unroll
      for (int e = 0; e < 2; ++e) {
        bf16x8 bf = cat8(ldstr(sl + ta2 + e * 2048), ldstr(sl + ta2 + e * 2048 + 512));
        bf16x8 af = ldsr128(sl + fa2 + e * 2048);
        ay = MFMA32(af, bf, ay);
      }
    }
  }
  for (int c = 22; c < NC; ++c) {
    wait_bar_n<2>(min(NC - 1 - c, PD - 1));
    if (c + PD < NC) issue(c + PD);
    lptr sl = L + (c & (NS - 1)) * SLOT;
#pragma unroll
    for (int e = 0; e < 2; ++e) {
      bf16x8 af = cat8(ldstr(sl + tao + e * 2048), ldstr(sl + tao + e * 2048 + 1024));
      bf16x8 bf = ldsr128(sl + fao + e * 2048);
      aa = MFMA32(af, bf, aa);
    }
  }
  const float og = 1.0f - g;
  float v[16];
#pragma unroll
  for (int i = 0; i < 16; ++i) v[i] = ay[i] * og + aa[i] * g;
  WAIT_BAR(0);
  {
    lptr ex = L + w * 2048 + lane * 16;
    const int so = kh ? 0 : 8;
    f32x4 s0, s1;
#pragma unroll
    for (int j = 0; j < 4; ++j) { s0[j] = kh ? v[j] : v[8 + j]; s1[j] = kh ? v[4 + j] : v[12 + j]; }
    (void)so;
    *(LAS f32x4*)(ex) = s0; *(LAS f32x4*)(ex + 1024) = s1;
  }
  WAIT_BAR(0);
  {
    lptr ex = L + (w ^ 4) * 2048 + lane * 16;
    const f32x4 r0 = *(const LAS f32x4*)(ex), r1 = *(const LAS f32x4*)(ex + 1024);
    const float bo = bb2 * og;
    float f[8];
#pragma unroll
    for (int j = 0; j < 4; ++j) { f[j] = (kh ? v[8 + j] : v[j]) + r0[j] + bo; f[4 + j] = (kh ? v[12 + j] : v[4 + j]) + r1[j] + bo; }
    unsigned a0 = pk2(f[0], f[1]), a1 = pk2(f[2], f[3]), b0 = pk2(f[4], f[5]), b1 = pk2(f[6], f[7]);
    auto x0 = __builtin_amdgcn_permlane32_swap(a0, b0, false, false);
    auto x1 = __builtin_amdgcn_permlane32_swap(a1, b1, false, false);
    u32x4 ov = {x0[0], x1[0], x0[1], x1[1]};
    *(u32x4*)(out + (size_t)tok * 1024 + (dt * 64 + mt * 32 + kh * 16 + h * 8) * 2) = ov;
  }
}

extern "C" void kernel_launch(void* const* d_in, const int* in_sizes, int n_in, void* d_out, int out_size, void* d_ws, size_t ws_size,
                              hipStream_t stream) {
  const char* x = (const char*)d_in[0];
  const char* wqkv = (const char*)d_in[1];
  const char* wo = (const char*)d_in[2];
  const char* w1 = (const char*)d_in[4];
  const char* b1 = (const char*)d_in[5];
  const char* w2 = (const char*)d_in[6];
  const char* b2 = (const char*)d_in[7];
  const char* gw = (const char*)d_in[8];
  char* ws = (char*)d_ws;
  hipFuncSetAttribute((const void*)k1, hipFuncAttributeMaxDynamicSharedMemorySize, 131072);
  hipFuncSetAttribute((const void*)k2, hipFuncAttributeMaxDynamicSharedMemorySize, 71680);
  hipFuncSetAttribute((const void*)k3, hipFuncAttributeMaxDynamicSharedMemorySize, 131072);
  k1<<<256, 512, 131072, stream>>>(x, wqkv, gw, ws);
  k2<<<488, 512, 71680, stream>>>(w1, b1, ws);
  k3<<<256, 512, 131072, stream>>>(w2, b2, wo, ws, (char*)d_out);
}
```

```cpp
#include <hip/hip_runtime.h>
#include <hip/hip_bf16.h>
#include <stdint.h>

#define LAS __attribute__((address_space(3)))
#define GAS __attribute__((address_space(1)))
#define DI __device__ __forceinline__
typedef __attribute__((ext_vector_type(8))) __bf16 bf16x8;
typedef __attribute__((ext_vector_type(2))) __bf16 bf16x2;
typedef __attribute__((ext_vector_type(4))) short s16x4;
typedef __attribute__((ext_vector_type(2))) float f32x2;
typedef __attribute__((ext_vector_type(4))) float f32x4;
typedef __attribute__((ext_vector_type(16))) float f32x16;
typedef __attribute__((ext_vector_type(2))) unsigned u32x2;
typedef __attribute__((ext_vector_type(4))) unsigned u32x4;
typedef LAS char* lptr;
typedef __amdgpu_buffer_rsrc_t rsrc_t;

constexpr unsigned WS_Q = 0, WS_K = 2u << 20, WS_V = 4u << 20, WS_H = 6u << 20, WS_O = 8u << 20, WS_U = 10u << 20, WS_W1T = 12u << 20,
                   WS_G = 18u << 20, WS_FL = 19u << 20, WS_END = 20u << 20;
constexpr int FL_HW = 0;
constexpr int FL_Q = 512;
constexpr int FL_U = 1024;
constexpr int FL_O = 1536;
constexpr int FL_EPOCH = 2048;
constexpr float QSCALE = 0.25f * 1.4426950408889634f;
constexpr int NS = 7, SLOT = 16384, RING = NS * SLOT;
constexpr int L_K = RING, L_V = RING + 16384, L_X = RING + 32768  , L_C = L_X + 12288  ;
constexpr int LDS_BYTES = 163840;
constexpr unsigned SPIN_CAP = 200000;

#define MFMA32(a, b, c) __builtin_amdgcn_mfma_f32_32x32x16_bf16(a, b, c, 0, 0, 0)
#define MFMA16(a, b, c) __builtin_amdgcn_mfma_f32_16x16x32_bf16(a, b, c, 0, 0, 0)
#define SB() __builtin_amdgcn_sched_barrier(0)
#define LBAR() do { asm volatile("s_waitcnt lgkmcnt(0)\n\ts_barrier" ::: "memory"); SB(); } while (0)
#define DBAR() do { asm volatile("s_waitcnt vmcnt(0) lgkmcnt(0)\n\ts_barrier" ::: "memory"); SB(); } while (0)
#define VBAR(N) asm volatile("s_waitcnt vmcnt(" #N ")\n\ts_barrier" ::: "memory")
#define BAR() asm volatile("s_barrier" ::: "memory")

#define STAMP(i) do {} while (0)
#define STAMP_OUT(ws, slot) do {} while (0)

DI int rfl(int x) { return __builtin_amdgcn_readfirstlane(x); }
DI bf16x8 ldsr128(lptr p) { return *(const LAS bf16x8*)p; }
DI s16x4 ldstr(lptr p) { return __builtin_amdgcn_ds_read_tr16_b64_v4i16((LAS s16x4*)p); }
DI bf16x8 cat8(s16x4 a, s16x4 b) { return __builtin_bit_cast(bf16x8, __builtin_shufflevector(a, b, 0, 1, 2, 3, 4, 5, 6, 7)); }
DI unsigned pk2(float a, float b) { f32x2 f = {a, b}; return __builtin_bit_cast(unsigned, __builtin_convertvector(f, bf16x2)); }
DI float bflo(unsigned u) { return __uint_as_float(u << 16); }
DI float bfhi(unsigned u) { return __uint_as_float(u & 0xffff0000u); }
DI int crow(int i, int h) { return (i & 3) + 8 * (i >> 2) + 4 * h; }
DI void st128(rsrc_t r, unsigned off, u32x4 v) { __builtin_amdgcn_raw_buffer_store_b128(v, r, off, 0, 16); }
DI void st64(rsrc_t r, unsigned off, u32x2 v) { __builtin_amdgcn_raw_buffer_store_b64(v, r, off, 0, 16); }
DI void st32(rsrc_t r, unsigned off, unsigned v) { __builtin_amdgcn_raw_buffer_store_b32(v, r, off, 0, 16); }
DI u32x4 ld128(rsrc_t r, unsigned off) { return __builtin_amdgcn_raw_buffer_load_b128(r, off, 0, 16); }
DI unsigned ld32(rsrc_t r, unsigned off) { return __builtin_amdgcn_raw_buffer_load_b32(r, off, 0, 16); }
DI unsigned flag_ld(const char* ws, int idx) { return __hip_atomic_load((const GAS unsigned*)(ws + WS_FL) + idx, __ATOMIC_RELAXED, __HIP_MEMORY_SCOPE_AGENT); }
DI void flag_st(char* ws, int idx, unsigned v) { __hip_atomic_store((GAS unsigned*)(ws + WS_FL) + idx, v, __ATOMIC_RELAXED, __HIP_MEMORY_SCOPE_AGENT); }

#define M0_IN "s_mov_b32 %0, m0\n\ts_mov_b32 m0, %1\n\ts_nop 0\n\t"
#define M0_OUT "s_mov_b32 m0, %0"
DI void dma_4v(unsigned m0v, const void* b, unsigned v0, unsigned v1, unsigned v2, unsigned v3) {
  unsigned sv;
  asm volatile(M0_IN "global_load_lds_dwordx4 %3, %2\n\tglobal_load_lds_dwordx4 %4, %2 offset:1024\n\t"
               "global_load_lds_dwordx4 %5, %2 offset:2048\n\tglobal_load_lds_dwordx4 %6, %2 offset:3072\n\t" M0_OUT
               : "=&s"(sv) : "s"(m0v), "s"(b), "v"(v0), "v"(v1), "v"(v2), "v"(v3) : "memory");
}
DI void dma_lin4_sc1(unsigned m0v, const void* b, unsigned vl) {
  unsigned sv;
  asm volatile(M0_IN "global_load_lds_dwordx4 %3, %2 sc1\n\tglobal_load_lds_dwordx4 %3, %2 offset:1024 sc1\n\t"
               "global_load_lds_dwordx4 %3, %2 offset:2048 sc1\n\tglobal_load_lds_dwordx4 %3, %2 offset:3072 sc1\n\t" M0_OUT
               : "=&s"(sv) : "s"(m0v), "s"(b), "v"(vl) : "memory");
}
DI void dma_lin4(unsigned m0v, const void* b, unsigned vl) {
  unsigned sv;
  asm volatile(M0_IN "global_load_lds_dwordx4 %3, %2\n\tglobal_load_lds_dwordx4 %3, %2 offset:1024\n\t"
               "global_load_lds_dwordx4 %3, %2 offset:2048\n\tglobal_load_lds_dwordx4 %3, %2 offset:3072\n\t" M0_OUT
               : "=&s"(sv) : "s"(m0v), "s"(b), "v"(vl) : "memory");
}
DI void dma_g1(unsigned m0v, const void* hb, const void* wb_m2048, unsigned vl) {
  unsigned sv;
  asm volatile(M0_IN "global_load_lds_dwordx4 %4, %2 sc1\n\tglobal_load_lds_dwordx4 %4, %2 offset:1024 sc1\n\t"
               "global_load_lds_dwordx4 %4, %3 offset:2048 sc1\n\t"
               "s_mov_b32 exec_hi, 0\n\tglobal_load_lds_dwordx4 %4, %3 offset:3072 sc1\n\ts_mov_b32 exec_hi, -1\n\t" M0_OUT
               : "=&s"(sv) : "s"(m0v), "s"(hb), "s"(wb_m2048), "v"(vl) : "memory");
}
DI void dma_wh(unsigned m0v, const void* wb, unsigned v0, unsigned v1) {
  unsigned sv;
  asm volatile(M0_IN "global_load_lds_dwordx4 %3, %2\n\tglobal_load_lds_dwordx4 %4, %2 offset:1024\n\t" M0_OUT
               : "=&s"(sv) : "s"(m0v), "s"(wb), "v"(v0), "v"(v1) : "memory");
}
DI void dma_fh(unsigned m0v, const void* fb_m2048, unsigned vl) {
  unsigned sv;
  asm volatile(M0_IN "global_load_lds_dwordx4 %3, %2 offset:2048 sc1\n\tglobal_load_lds_dwordx4 %3, %2 offset:3072 sc1\n\t" M0_OUT
               : "=&s"(sv) : "s"(m0v), "s"(fb_m2048), "v"(vl) : "memory");
}

template <int NW>
DI void poll_issue(const char* ws, int idx, unsigned token, unsigned (&v)[NW]) {
#pragma unroll
  for (int k = 0; k < NW; ++k) v[k] = idx >= 0 ? flag_ld(ws, idx + 64 * k) : token;
}
template <int NW>
DI void poll_wait(const char* ws, int idx, unsigned token, unsigned (&v)[NW]) {
  for (unsigned spins = 0; spins < SPIN_CAP; ++spins) {
    bool ok = true;
#pragma unroll
    for (int k = 0; k < NW; ++k) ok &= v[k] == token;
    if (__all(ok)) break;
    __builtin_amdgcn_s_sleep(2);
    poll_issue<NW>(ws, idx, token, v);
  }
  __builtin_amdgcn_fence(__ATOMIC_ACQUIRE, "wavefront");
}

DI float wave_sum(float v) {
#define DPPADD(ctrl, rm) v += __int_as_float(__builtin_amdgcn_update_dpp(0, __float_as_int(v), ctrl, rm, 0xf, false))
  DPPADD(0x111, 0xf); DPPADD(0x112, 0xf); DPPADD(0x114, 0xf); DPPADD(0x118, 0xf); DPPADD(0x142, 0xa); DPPADD(0x143, 0xc);
#undef DPPADD
  return __int_as_float(__builtin_amdgcn_readlane(__float_as_int(v), 63));
}
DI float gelu_tanh(float u) {
  const float z = u * (0.7978845608028654f + 0.7978845608028654f * 0.044715f * u * u);
  const float e = __builtin_amdgcn_exp2f(z * (-2.0f * 1.4426950408889634f));
  return u * __builtin_amdgcn_rcpf(1.0f + e);
}
DI float max16(const f32x16& s, float m) {
#pragma unroll
  for (int i = 0; i < 16; i += 2) m = __builtin_fmaxf(__builtin_fmaxf(s[i], s[i + 1]), m);
  return m;
}

#define NEXT_SO() so = so == (NS - 1) * SLOT ? 0 : so + SLOT

template <bool ISV, class Hook>
DI void qkv_consume(lptr L, int& so, int w, int lane, f32x16 (&acc)[2], Hook hook) {
  const int r = lane & 31, h = lane >> 5, wm = w >> 2, wn = w & 3;
  int xa[4];
#pragma unroll
  for (int s = 0; s < 4; ++s) xa[s] = (wm * 64 + r) * 128 + (((2 * s + h) ^ ((r >> 1) & 7)) << 4);
  const int q = (lane & 15) >> 2, p = lane & 3, blk = (lane >> 4) & 1;
  const int wa = (8 * h + q) * 256 + (((wn * 4 + blk * 2 + (p >> 1)) ^ (q << 2)) << 4) + (p & 1) * 8;
  bf16x8 xA[4][2], xB[4][2], wA[4], wB[4];
#define QMM(X, W, s) do { if (ISV) { acc[0] = MFMA32(X[s][0], W[s], acc[0]); acc[1] = MFMA32(X[s][1], W[s], acc[1]); } \
                          else { acc[0] = MFMA32(W[s], X[s][0], acc[0]); acc[1] = MFMA32(W[s], X[s][1], acc[1]); } } while (0)
#define QRX(X, s) do { X[s][0] = ldsr128(sl + xa[s]); X[s][1] = ldsr128(sl + xa[s] + 4096); } while (0)
#define QRW(W, s) do { W[s] = cat8(ldstr(sl + wa + s * 4096), ldstr(sl + wa + s * 4096 + 1024)); } while (0)
#define QSTEPX(XN, XP, WP, MM, st) do { hook(st); lptr sl = L + so; NEXT_SO(); \
    QRX(XN, 0); QRX(XN, 1); SB(); if (MM) QMM(XP, WP, 0); SB(); QRX(XN, 2); QRX(XN, 3); SB(); if (MM) QMM(XP, WP, 1); SB(); } while (0)
#define QSTEPW(WN, XP, WP, MM, st) do { hook(st); lptr sl = L + so; NEXT_SO(); \
    QRW(WN, 0); QRW(WN, 1); SB(); if (MM) QMM(XP, WP, 2); SB(); QRW(WN, 2); QRW(WN, 3); SB(); if (MM) QMM(XP, WP, 3); SB(); } while (0)
  QSTEPX(xA, xB, wB, false, 0); QSTEPW(wA, xB, wB, false, 1);
#pragma unroll 1
  for (int c = 1; c < 7; c += 2) {
    QSTEPX(xB, xA, wA, true, 2 * c);     QSTEPW(wB, xA, wA, true, 2 * c + 1);
    QSTEPX(xA, xB, wB, true, 2 * c + 2); QSTEPW(wA, xB, wB, true, 2 * c + 3);
  }
  QSTEPX(xB, xA, wA, true, 14); QSTEPW(wB, xA, wA, true, 15);
  QMM(xB, wB, 0); QMM(xB, wB, 1); QMM(xB, wB, 2); QMM(xB, wB, 3);
}

__global__ __launch_bounds__(768, 3) void fused_kernel(const char* __restrict__ x, const char* __restrict__ wqkv, const char* __restrict__ wo,
                                                       const char* __restrict__ w1, const char* __restrict__ b1, const char* __restrict__ w2,
                                                       const char* __restrict__ b2, const char* __restrict__ gw, char* __restrict__ ws,
                                                       char* __restrict__ out) {
  extern __shared__ __attribute__((aligned(16))) char smem[];
  STAMP(0);
  const int b = blockIdx.x, xcd = b & 7, idx = b >> 3;
  const int lane = threadIdx.x & 63, w = rfl(threadIdx.x >> 6);
  const unsigned lds0 = (unsigned)(uintptr_t)smem;
  lptr L = (lptr)smem;
  const bool hasQ = idx < 24, hasG1 = idx >= 3;
  const int tm = (xcd >> 1) * 4 + idx / 6, tn = (xcd & 1) * 6 + idx % 6;
  const int rt = xcd * 8 + (idx - 24);
  const int Lx = xcd * 29 + (idx - 3), dh_ = Lx / 116, rem_ = Lx - dh_ * 116;
  const int g1dt = dh_ * 4 + (rem_ & 3), g1ft = rem_ >> 2;
  const int head = xcd * 4 + (idx & 3), qb = idx >> 2;
  const int st = (xcd >> 1) * 8 + (idx >> 2), dt = (xcd & 1) * 4 + (idx & 3);
  const unsigned epoch = *(const unsigned*)(ws + WS_FL + FL_EPOCH * 4);
  const unsigned token = epoch * 1664525u + 1013904223u;
  int fq_idx = -1;
  if (lane < 12) { const int wh = lane >> 2, tmi = lane & 3, t_ = 2 * qb - 2 + tmi; if (t_ >= 0 && (wh > 0 || tmi >= 2)) fq_idx = FL_Q + (wh * 4 + (head >> 3)) * 16 + t_; }
  const int fu_idx = lane < 29 ? FL_U + dt * 32 + lane : -1;
  const int fo_idx = lane < 32 ? FL_O + (st >> 2) * 32 + lane : -1;

  if (w >= 8) {
    const int j = w - 8;
    const unsigned m0b = lds0 + j * 4096;
    const unsigned vl = lane * 16;
    unsigned so = 0;
    const char* hp = ws + WS_H + (size_t)g1dt * 64 * 4096 + j * 2048;
    const char* w1p = ws + WS_W1T + ((size_t)g1ft * 2048 + j * 16) * 96 - 2048;
    auto fill_g1 = [&]() { dma_g1(m0b + so, hp, w1p, vl); hp += 8192; w1p += 64 * 96; NEXT_SO(); };
    const unsigned swz = ((lane & 7) ^ (((lane >> 4) & 1) << 2)) << 4;
    unsigned vw0 = (lane >> 3) * 4096 + swz, vw1 = vw0 + 8 * 4096 - 1024;
    const char* w2p = w2 + (size_t)(j * 16) * 4096 + st * 128;
    const char* up = ws + WS_U + (size_t)dt * 86 * 2048 + j * 2048 - 2048;
    const size_t w2half = (size_t)(j < 2 ? 21 * 64 : 20 * 64) * 4096;
    const int uhalf = j < 2 ? 21 * 8192 : 20 * 8192;
    auto fill_kv = [&]() {
      int t0 = qb * 8 - 8 + 4 * j; t0 = t0 < 0 ? t0 + 8 : t0;
      dma_lin4_sc1(lds0 + L_K + j * 4096, ws + WS_K + (size_t)(head * 64 + t0) * 1024, vl);
      dma_lin4_sc1(lds0 + L_V + j * 4096, ws + WS_V + (size_t)(head * 64 + t0) * 1024, vl);
    };
    int nwh = 0;
    auto fill_wh = [&]() { dma_wh(m0b + so, nwh == 0 ? w2p + w2half : w2p + (size_t)(nwh - 1) * 64 * 4096, vw0, vw1); ++nwh; NEXT_SO(); };
    unsigned so_g2 = 0;

    if (hasQ) {
      const unsigned x0 = (lane >> 3) * 1024 + (((lane & 7) ^ (lane >> 4)) << 4), x1 = (8 + (lane >> 3)) * 1024 + (((lane & 7) ^ (4 + (lane >> 4))) << 4) - 1024;
      const unsigned x2 = x0 + 16 * 1024 - 2048, x3 = x1 + 16 * 1024 - 2048;
      const unsigned q0 = (lane >> 4) * 3072 + (((lane & 15) ^ ((lane >> 4) << 2)) << 4);
      const unsigned q1 = q0 + 4 * 3072 - 1024, q2 = q0 + 8 * 3072 - 2048, q3 = q0 + 12 * 3072 - 3072;
      const char* xp = x + (size_t)(tm * 128 + j * 32) * 1024;
      const char* qp = wqkv + (size_t)(j * 16) * 3072 + tn * 256;
      auto fill_x = [&]() { dma_4v(m0b + so, xp, x0, x1, x2, x3); xp += 128; NEXT_SO(); };
      auto fill_w = [&]() { dma_4v(m0b + so, qp, q0, q1, q2, q3); qp += 64 * 3072; NEXT_SO(); };
#pragma unroll 1
      for (int c = 0; c < 3; ++c) { fill_x(); fill_w(); }
#pragma unroll 1
      for (int c = 0; c < 5; ++c) { VBAR(20); fill_x(); VBAR(20); fill_w(); }
      if (hasG1) {
#pragma unroll 1
        for (int s = 10; s < 16; ++s) { VBAR(20); fill_g1(); }
      } else {
        VBAR(20); VBAR(16); VBAR(12); VBAR(8); VBAR(4); VBAR(0);
      }
    } else {
      const char* sp = w1 + (size_t)rt * 32 * 2752;
#pragma unroll 1
      for (int i = 0; i < 5; ++i) dma_lin4(lds0 + i * 16384 + j * 4096, sp + i * 16384 + j * 4096, vl);
      { const int o = 5 * 16384 + (j < 1 ? 0 : 2048); dma_lin4(lds0 + o, sp + o, vl); }
      VBAR(0);
      BAR();
      BAR();
      unsigned pv[5];
      poll_issue<5>(ws, FL_HW + lane, token, pv);
      poll_wait<5>(ws, FL_HW + lane, token, pv);
#pragma unroll 1
      for (int s = 0; s < 6; ++s) fill_g1();
    }
    STAMP(1);
    if (hasG1) {
#pragma unroll 1
      for (int s = 0; s < 26; ++s) { VBAR(20); fill_g1(); if (s == 14) fill_kv(); }
      so_g2 = so;
      VBAR(20); fill_wh(); VBAR(18); fill_wh(); VBAR(16); fill_wh(); VBAR(14); fill_wh(); VBAR(12); fill_wh(); VBAR(10); fill_wh();
      BAR(); fill_wh();
      BAR();
      BAR();
    } else {
      BAR();
      so_g2 = so;
#pragma unroll 1
      for (int i = 0; i < 7; ++i) fill_wh();
      unsigned pv[1];
      poll_issue<1>(ws, fq_idx, token, pv);
      poll_wait<1>(ws, fq_idx, token, pv);
      fill_kv();
      VBAR(0);
    }
    STAMP(2);
    {
      unsigned pv[1];
      poll_issue<1>(ws, fu_idx, token, pv);
      poll_wait<1>(ws, fu_idx, token, pv);
      STAMP(3);
      so = so_g2;
      dma_fh(m0b + so, up + uhalf, vl); NEXT_SO();
#pragma unroll 1
      for (int i = 0; i < 6; ++i) { dma_fh(m0b + so, up, vl); up += 8192; NEXT_SO(); }
    }
    w2p += (size_t)6 * 64 * 4096;
    unsigned wstr = 64 * 4096;
    int ci = 7;
    auto fill_g2 = [&]() {
      if (ci == 22) {
        w2p = wo + (size_t)(j * 16) * 1024 + dt * 128; up = ws + WS_O + (size_t)st * 32 * 2048 + j * 2048 - 2048; wstr = 64 * 1024;
        vw0 = (lane >> 3) * 1024 + swz; vw1 = vw0 + 8 * 1024 - 1024;
      }
      dma_wh(m0b + so, w2p, vw0, vw1); dma_fh(m0b + so, up, vl);
      w2p += wstr; up += 8192; NEXT_SO(); ++ci;
    };
    VBAR(12);
    VBAR(10); fill_g2(); VBAR(12); fill_g2(); VBAR(14); fill_g2(); VBAR(16); fill_g2(); VBAR(18); fill_g2();
#pragma unroll 1
    for (int s = 6; s < 24; ++s) { VBAR(20); fill_g2(); }
    VBAR(20); VBAR(16); VBAR(12); VBAR(8); VBAR(4); VBAR(0);
    BAR(); BAR();
    STAMP(4);
    if (j == 0) STAMP_OUT(ws, 1);
    return;
  }

  const rsrc_t rws = __builtin_amdgcn_make_buffer_rsrc(ws, 0, WS_END, 0x00020000);
  const int r = lane & 31, h = lane >> 5;
  const int g4 = lane >> 4, q4 = (lane & 15) >> 2, p4 = lane & 3, blk = (lane >> 4) & 1;
  int so = 0;
  {
    const int row = 32 * (b >> 2) + 16 * (w >> 2) + 4 * (b & 3) + (w & 3);
    const u32x4 xv = *(const u32x4*)(x + (size_t)row * 1024 + lane * 16);
    const u32x4 gv = *(const u32x4*)(gw + lane * 16);
    if (threadIdx.x < 4) {
      const unsigned cv = (threadIdx.x & 1) ? 0u : 0x3f803f80u;
      *(LAS u32x4*)(L + L_C + (threadIdx.x >> 1) * 512 + (threadIdx.x & 1) * 16) = u32x4{cv, cv, cv, cv};
    }
    float v[8], s = 0.f, gs = 0.f;
#pragma unroll
    for (int jj = 0; jj < 4; ++jj) {
      v[2 * jj] = bflo(xv[jj]); v[2 * jj + 1] = bfhi(xv[jj]);
      s += v[2 * jj] + v[2 * jj + 1];
      gs += v[2 * jj] * bflo(gv[jj]) + v[2 * jj + 1] * bfhi(gv[jj]);
    }
    s = wave_sum(s); gs = wave_sum(gs);
    const float m = s * (1.0f / 512);
    float qq = 0.f;
#pragma unroll
    for (int jj = 0; jj < 8; ++jj) { v[jj] -= m; qq += v[jj] * v[jj]; }
    qq = wave_sum(qq);
    const float rs = __builtin_amdgcn_rsqf(qq * (1.0f / 512) + 1e-5f);
    u32x4 o;
#pragma unroll
    for (int jj = 0; jj < 4; ++jj) o[jj] = pk2(v[2 * jj] * rs, v[2 * jj + 1] * rs);
    *(LAS u32x4*)(L + L_X + w * 1056 + lane * 16) = o;
    if (lane == 0) st32(rws, WS_G + row * 4, __float_as_uint(1.0f / (1.0f + __expf(-gs))));
  }
  auto ln_part2 = [&]() {
    lptr a = L + L_X + q4 * 1056 + (w * 64 + g4 * 16) * 2 + p4 * 8;
    const bf16x8 f = cat8(ldstr(a), ldstr(a + 4 * 1056));
    st128(rws, WS_H + (((w * 64 + (b >> 2)) * 4 + g4) * 64 + (b & 3) * 16 + (lane & 15)) * 16, __builtin_bit_cast(u32x4, f));
  };
  asm volatile("" :: "s"(out), "s"(b1), "s"(b2));
  STAMP(1);

  if (hasQ) {
    f32x16 acc[2] = {};
    unsigned pv[5];
    auto hook = [&](int s) {
      if (s == 4) DBAR(); else if (s == 10 && w == 7 && hasG1) { poll_wait<5>(ws, FL_HW + lane, token, pv); LBAR(); } else LBAR();
      if (s == 0) ln_part2();
      if (s == 4 && threadIdx.x == 0) flag_st(ws, FL_HW + b, token);
      if (s == 6 && w == 7 && hasG1) poll_issue<5>(ws, FL_HW + lane, token, pv);
    };
    if (tn >= 8) qkv_consume<true>(L, so, w, lane, acc, hook);
    else qkv_consume<false>(L, so, w, lane, acc, hook);
    STAMP(2);
    const int wm = w >> 2, wn = w & 3;
    const int hd0 = (tn & 3) * 8 + wn * 2;
#pragma unroll
    for (int mi = 0; mi < 2; ++mi) {
      const int tb = tm * 4 + wm * 2 + mi;
      const float sc = tn < 4 ? QSCALE : 1.0f;
      const unsigned dst = tn >= 8 ? WS_V + (((hd0 + (r >> 4)) * 64 + tb) * 64 + h * 16 + (r & 15)) * 16
                                   : (tn < 4 ? WS_Q : WS_K) + ((hd0 * 64 + tb) * 64 + lane) * 16;
      const unsigned dstep = tn >= 8 ? 512 : 65536;
#pragma unroll
      for (int s = 0; s < 2; ++s) {
        u32x4 v;
#pragma unroll
        for (int jj = 0; jj < 4; ++jj) v[jj] = pk2(acc[mi][8 * s + 2 * jj] * sc, acc[mi][8 * s + 2 * jj + 1] * sc);
        st128(rws, dst + s * dstep, v);
      }
    }
  } else {
    LBAR();
    ln_part2();
    unsigned so3[3];
#pragma unroll
    for (int k = 0; k < 3; ++k) { const int e = k * 64 + lane, rr = e / 6; so3[k] = rr * 2752 + (e - rr * 6) * 16; }
#pragma unroll 1
    for (int ft = w; ft < 29; ft += 8) {
#pragma unroll
      for (int k = 0; k < 3; ++k) {
        const u32x4 v = *(const LAS u32x4*)(L + so3[k] + ft * 96);
        st128(rws, WS_W1T + (ft * 2048 + rt * 32) * 96 + k * 1024 + lane * 16, v);
      }
    }
    LBAR();
    DBAR();
    if (threadIdx.x == 0) { flag_st(ws, FL_HW + b, token); flag_st(ws, FL_HW + 256 + rt, token); }
    STAMP(2);
  }

  bf16x8 qf;
  if (hasG1) {
    const int kh = w >> 2, db = w & 3;
    const int ha = (kh * 2 + (db >> 1)) * 4096 + (db & 1) * 1024 + lane * 16;
    const int wa = kh * 8192 + 2048 + (4 * g4 + q4) * 96 + p4 * 8;
    f32x4 acc[3] = {};
    bf16x8 hA, wA[3], hB, wB[3];
    unsigned pv[1];
    u32x2 bb[3];
#define G1RD(H, W) do { lptr sl = L + so; NEXT_SO(); H = ldsr128(sl + ha); \
    _Pragma("unroll") for (int fb = 0; fb < 3; ++fb) W[fb] = cat8(ldstr(sl + wa + fb * 32), ldstr(sl + wa + fb * 32 + 4096)); SB(); } while (0)
#define G1MM(H, W) do { _Pragma("unroll") for (int fb = 0; fb < 3; ++fb) acc[fb] = MFMA16(W[fb], H, acc[fb]); SB(); } while (0)
    auto hook = [&](int s) {
      if (s == 6) DBAR(); else if (s == 14 && w == 7) { poll_wait<1>(ws, fq_idx, token, pv); LBAR(); } else LBAR();
      if (s == 6 && hasQ && threadIdx.x == 0) flag_st(ws, FL_Q + tn * 16 + tm, token);
      if (s == 10 && w == 7) poll_issue<1>(ws, fq_idx, token, pv);
    };
    hook(0); G1RD(hA, wA);
#pragma unroll 1
    for (int s = 1; s < 31; s += 2) {
      hook(s);     G1RD(hB, wB); G1MM(hA, wA);
      hook(s + 1); G1RD(hA, wA); G1MM(hB, wB);
    }
    qf = __builtin_bit_cast(bf16x8, ld128(rws, WS_Q + (head * 64 + qb * 8 + w) * 1024 + lane * 16));
#pragma unroll
    for (int fb = 0; fb < 3; ++fb) { const int f0 = min(g1ft * 48 + fb * 16 + 4 * g4, 1372); bb[fb] = *(const u32x2*)(b1 + f0 * 2); }
    LBAR(); G1RD(hB, wB); G1MM(hA, wA);
    LBAR();
    G1MM(hB, wB);
    STAMP(3);
    if (kh == 1) {
#pragma unroll
      for (int fb = 0; fb < 3; ++fb) *(LAS f32x4*)(L + L_X + ((db * 3 + fb) * 64 + lane) * 16) = acc[fb];
    }
    LBAR();
    if (kh == 0) {
      const int nfb = g1ft == 28 ? 2 : 3;
#pragma unroll
      for (int fb = 0; fb < 3; ++fb) {
        if (fb < nfb) {
          const f32x4 o = *(const LAS f32x4*)(L + L_X + ((db * 3 + fb) * 64 + lane) * 16);
          const float u0 = gelu_tanh(acc[fb][0] + o[0] + bflo(bb[fb][0])), u1 = gelu_tanh(acc[fb][1] + o[1] + bfhi(bb[fb][0]));
          const float u2 = gelu_tanh(acc[fb][2] + o[2] + bflo(bb[fb][1])), u3 = gelu_tanh(acc[fb][3] + o[3] + bfhi(bb[fb][1]));
          const u32x2 v = {pk2(u0, u1), pk2(u2, u3)};
          const int fblk = g1ft * 3 + fb;
          st64(rws, WS_U + (((g1dt * 86 + fblk) * 2 + (db >> 1)) * 64 + (g4 >> 1) * 32 + (db & 1) * 16 + (lane & 15)) * 16 + (g4 & 1) * 8, v);
        }
      }
    }
  } else {
    DBAR();
    if (threadIdx.x == 0) flag_st(ws, FL_Q + tn * 16 + tm, token);
    LBAR();
    qf = __builtin_bit_cast(bf16x8, ld128(rws, WS_Q + (head * 64 + qb * 8 + w) * 1024 + lane * 16));
    STAMP(3);
  }

  {
    const int t_lo = qb > 0 ? w + 1 : 8, t_hi = w + 8;
    lptr kp = L + L_K + lane * 16;
    const f32x16 zero = {};
    f32x16 sb;
    {
      const f32x16 sd = MFMA32(ldsr128(kp + (w + 8) * 1024), qf, zero);
      const f32x16 sf = MFMA32(ldsr128(kp + w * 1024), qf, zero);
#pragma unroll
      for (int i = 0; i < 16; ++i) sb[i] = (crow(i, h) <= r) ? sd[i] : (qb > 0 ? sf[i] : -INFINITY);
    }
    float m = max16(sb, -INFINITY);
#pragma unroll 1
    for (int t = t_lo; t < t_hi; ++t) {
      const f32x16 s = MFMA32(ldsr128(kp + t * 1024), qf, zero);
      m = max16(s, m);
    }
    {
      auto rr = __builtin_amdgcn_permlane32_swap(__float_as_uint(m), __float_as_uint(m), false, false);
      m = __builtin_fmaxf(__uint_as_float(rr[0]), __uint_as_float(rr[1]));
    }
    if (hasG1) {
      DBAR();
      if (threadIdx.x == 0) flag_st(ws, FL_U + g1dt * 32 + g1ft, token);
    }
    STAMP(4);
    f32x16 cin;
#pragma unroll
    for (int i = 0; i < 16; ++i) cin[i] = -m;
    const int vbase = r < 16 ? L_V + h * 256 + r * 16 : (L_C + ((r == 16 || r == 20) ? 0 : 16));
    const int vstr = r < 16 ? 1024 : 0;
    f32x16 o = {};
    {
      u32x4 pd[2], pf[2];
#pragma unroll
      for (int i = 0; i < 16; i += 2) {
        const float p0 = __builtin_amdgcn_exp2f(sb[i] - m), p1 = __builtin_amdgcn_exp2f(sb[i + 1] - m);
        const bool v0 = crow(i, h) <= r, v1 = crow(i + 1, h) <= r;
        pd[i >> 3][(i >> 1) & 3] = pk2(v0 ? p0 : 0.f, v1 ? p1 : 0.f);
        pf[i >> 3][(i >> 1) & 3] = pk2(v0 ? 0.f : p0, v1 ? 0.f : p1);
      }
      lptr vp = L + vbase + (w + 8) * vstr;
      o = MFMA32(ldsr128(vp), __builtin_bit_cast(bf16x8, pd[0]), o);
      o = MFMA32(ldsr128(vp + 512), __builtin_bit_cast(bf16x8, pd[1]), o);
      if (qb > 0) {
        lptr vq = L + vbase + w * vstr;
        o = MFMA32(ldsr128(vq), __builtin_bit_cast(bf16x8, pf[0]), o);
        o = MFMA32(ldsr128(vq + 512), __builtin_bit_cast(bf16x8, pf[1]), o);
      }
    }
#pragma unroll 1
    for (int t = t_lo; t < t_hi; ++t) {
      const f32x16 s = MFMA32(ldsr128(kp + t * 1024), qf, cin);
      u32x4 pp[2];
#pragma unroll
      for (int i = 0; i < 16; i += 2) pp[i >> 3][(i >> 1) & 3] = pk2(__builtin_amdgcn_exp2f(s[i]), __builtin_amdgcn_exp2f(s[i + 1]));
      lptr vp = L + vbase + t * vstr;
      o = MFMA32(ldsr128(vp), __builtin_bit_cast(bf16x8, pp[0]), o);
      o = MFMA32(ldsr128(vp + 512), __builtin_bit_cast(bf16x8, pp[1]), o);
    }
    const float il = 1.0f / o[8];
    u32x4 ov;
#pragma unroll
    for (int jj = 0; jj < 4; ++jj) ov[jj] = pk2(o[2 * jj] * il, o[2 * jj + 1] * il);
    const int B = qb * 8 + w;
    st128(rws, WS_O + (((B >> 1) * 32 + head) * 2 + (B & 1)) * 1024 + lane * 16, ov);
  }
  STAMP(5);

  {
    const int kh = w >> 2, mt = (w >> 1) & 1, nt = w & 1;
    const int tok = st * 64 + nt * 32 + r;
    const int ta2 = kh * 8192 + (8 * h + q4) * 128 + (((nt * 4 + blk * 2 + (p4 >> 1)) ^ ((q4 >> 1) << 2)) << 4) + (p4 & 1) * 8;
    const int tao = kh * 8192 + (4 * h + q4) * 128 + (((mt * 4 + blk * 2 + (p4 >> 1)) ^ ((q4 >> 1) << 2)) << 4) + (p4 & 1) * 8;
    const int fa2 = kh * 8192 + 2048 + mt * 1024 + lane * 16;
    const int fao = kh * 8192 + 2048 + nt * 1024 + lane * 16;
    f32x16 ay = {}, aa = {};
    bf16x8 pa0, pb0, pa1, pb1, qa0, qb0, qa1, qb1;
    unsigned pv[1];
    float g = 0.f; unsigned b2v = 0;
#define RD2(A0, B0, A1, B1) do { lptr sl = L + so; NEXT_SO(); B0 = cat8(ldstr(sl + ta2), ldstr(sl + ta2 + 512)); A0 = ldsr128(sl + fa2); \
    B1 = cat8(ldstr(sl + ta2 + 4096), ldstr(sl + ta2 + 4096 + 512)); A1 = ldsr128(sl + fa2 + 4096); SB(); } while (0)
#define RDO(A0, B0, A1, B1) do { lptr sl = L + so; NEXT_SO(); A0 = cat8(ldstr(sl + tao), ldstr(sl + tao + 1024)); B0 = ldsr128(sl + fao); \
    A1 = cat8(ldstr(sl + tao + 4096), ldstr(sl + tao + 4096 + 1024)); B1 = ldsr128(sl + fao + 4096); SB(); } while (0)
#define MM2(ACC, A0, B0, A1, B1) do { ACC = MFMA32(A0, B0, ACC); ACC = MFMA32(A1, B1, ACC); SB(); } while (0)
    auto hook = [&](int s) {
      if (s == 4) DBAR(); else if (s == 16 && w == 7) { poll_wait<1>(ws, fo_idx, token, pv); LBAR(); } else LBAR();
      if (s == 4 && threadIdx.x == 0) flag_st(ws, FL_O + qb * 32 + head, token);
      if (s == 12 && w == 7) poll_issue<1>(ws, fo_idx, token, pv);
    };
    LBAR(); STAMP(6);
    RD2(pa0, pb0, pa1, pb1);
    if (kh) { pa0 = bf16x8{}; pa1 = bf16x8{}; pb0 = bf16x8{}; pb1 = bf16x8{}; }
#pragma unroll 1
    for (int s = 1; s < 21; s += 2) {
      hook(s);     RD2(qa0, qb0, qa1, qb1); MM2(ay, pa0, pb0, pa1, pb1);
      hook(s + 1); RD2(pa0, pb0, pa1, pb1); MM2(ay, qa0, qb0, qa1, qb1);
    }
    g = __uint_as_float(ld32(rws, WS_G + tok * 4));
    b2v = ((const unsigned short*)b2)[tok];
    LBAR(); RD2(qa0, qb0, qa1, qb1); MM2(ay, pa0, pb0, pa1, pb1);
    STAMP(7);
    LBAR(); RDO(pa0, pb0, pa1, pb1); MM2(ay, qa0, qb0, qa1, qb1);
#pragma unroll 1
    for (int s = 23; s < 29; s += 2) {
      LBAR(); RDO(qa0, qb0, qa1, qb1); MM2(aa, pa0, pb0, pa1, pb1);
      LBAR(); RDO(pa0, pb0, pa1, pb1); MM2(aa, qa0, qb0, qa1, qb1);
    }
    LBAR(); RDO(qa0, qb0, qa1, qb1); MM2(aa, pa0, pb0, pa1, pb1);
    MM2(aa, qa0, qb0, qa1, qb1);
    STAMP(8);
    const float og = 1.0f - g;
    float v[16];
#pragma unroll
    for (int i = 0; i < 16; ++i) v[i] = ay[i] * og + aa[i] * g;
    LBAR();
    {
      lptr ex = L + w * 2048 + lane * 16;
      f32x4 s0, s1;
#pragma unroll
      for (int jj = 0; jj < 4; ++jj) { s0[jj] = kh ? v[jj] : v[8 + jj]; s1[jj] = kh ? v[4 + jj] : v[12 + jj]; }
      *(LAS f32x4*)(ex) = s0; *(LAS f32x4*)(ex + 1024) = s1;
    }
    LBAR();
    {
      lptr ex = L + (w ^ 4) * 2048 + lane * 16;
      const f32x4 r0 = *(const LAS f32x4*)(ex), r1 = *(const LAS f32x4*)(ex + 1024);
      const float bo = bflo(b2v) * og;
      float f[8];
#pragma unroll
      for (int jj = 0; jj < 4; ++jj) { f[jj] = (kh ? v[8 + jj] : v[jj]) + r0[jj] + bo; f[4 + jj] = (kh ? v[12 + jj] : v[4 + jj]) + r1[jj] + bo; }
      const unsigned a0 = pk2(f[0], f[1]), a1 = pk2(f[2], f[3]), c0 = pk2(f[4], f[5]), c1 = pk2(f[6], f[7]);
      auto x0 = __builtin_amdgcn_permlane32_swap(a0, c0, false, false);
      auto x1 = __builtin_amdgcn_permlane32_swap(a1, c1, false, false);
      const u32x4 ov = {x0[0], x1[0], x0[1], x1[1]};
      *(u32x4*)(out + (size_t)tok * 1024 + (dt * 64 + mt * 32 + kh * 16 + h * 8) * 2) = ov;
    }
    if (b == 0 && threadIdx.x == 0) *(unsigned*)(ws + WS_FL + FL_EPOCH * 4) = token;
  }
  STAMP(9);
  if (w == 0) STAMP_OUT(ws, 0);
}

extern "C" void kernel_launch(void* const* d_in, const int* in_sizes, int n_in, void* d_out, int out_size, void* d_ws, size_t ws_size,
                              hipStream_t stream) {
  (void)hipFuncSetAttribute((const void*)fused_kernel, hipFuncAttributeMaxDynamicSharedMemorySize, LDS_BYTES);
  fused_kernel<<<256, 768, LDS_BYTES, stream>>>((const char*)d_in[0], (const char*)d_in[1], (const char*)d_in[2], (const char*)d_in[4],
                                                (const char*)d_in[5], (const char*)d_in[6], (const char*)d_in[7], (const char*)d_in[8],
                                                (char*)d_ws, (char*)d_out);
}
```

```cpp
#include <hip/hip_runtime.h>
#include <hip/hip_bf16.h>
#include <stdint.h>

#define LAS __attribute__((address_space(3)))
#define GAS __attribute__((address_space(1)))
#define DI __device__ __forceinline__
typedef __attribute__((ext_vector_type(8))) __bf16 bf16x8;
typedef __attribute__((ext_vector_type(2))) __bf16 bf16x2;
typedef __attribute__((ext_vector_type(4))) short s16x4;
typedef __attribute__((ext_vector_type(2))) float f32x2;
typedef __attribute__((ext_vector_type(4))) float f32x4;
typedef __attribute__((ext_vector_type(16))) float f32x16;
typedef __attribute__((ext_vector_type(2))) unsigned u32x2;
typedef __attribute__((ext_vector_type(4))) unsigned u32x4;
typedef LAS char* lptr;
typedef __amdgpu_buffer_rsrc_t rsrc_t;

constexpr unsigned WS_Q = 0, WS_K = 2u << 20, WS_V = 4u << 20, WS_O = 6u << 20, WS_U = 8u << 20, WS_H = 10u << 20,
                   WS_G = 13u << 20, WS_FL = 14u << 20, WS_END = 15u << 20;
constexpr int FL_H = 0;
constexpr int FL_Q = 64 * 32;
constexpr int FL_U = FL_Q + 192 * 32;
constexpr int FL_O = FL_U + 352 * 32;
constexpr int FL_EPOCH = FL_O + 256 * 32;
constexpr float QSCALE = 0.25f * 1.4426950408889634f;
constexpr int NS = 7, SLOT = 16384, RING = NS * SLOT;
constexpr int L_K = RING, L_V = RING + 16384, L_X = RING + 32768  , L_C = L_X + 12288  , L_P = L_C + 1024  ;
constexpr int LDS_BYTES = 163840;
constexpr unsigned SPIN_CAP = 200000;
#define QROT 1

#define MFMA32(a, b, c) __builtin_amdgcn_mfma_f32_32x32x16_bf16(a, b, c, 0, 0, 0)
#define MFMA16(a, b, c) __builtin_amdgcn_mfma_f32_16x16x32_bf16(a, b, c, 0, 0, 0)
#define SB() __builtin_amdgcn_sched_barrier(0)
#define LBAR() do { asm volatile("s_waitcnt lgkmcnt(0)\n\ts_barrier" ::: "memory"); SB(); } while (0)
#define DBAR() do { asm volatile("s_waitcnt vmcnt(0) lgkmcnt(0)\n\ts_barrier" ::: "memory"); SB(); } while (0)
#define VBAR(N) asm volatile("s_waitcnt vmcnt(" #N ")\n\ts_barrier" ::: "memory")
#define BAR() asm volatile("s_barrier" ::: "memory")

#define STAMP(i) do {} while (0)
#define STAMP_OUT(ws, slot) do {} while (0)

DI int rfl(int x) { return __builtin_amdgcn_readfirstlane(x); }
DI bf16x8 ldsr128(lptr p) { return *(const LAS bf16x8*)p; }
DI s16x4 ldstr(lptr p) { return __builtin_amdgcn_ds_read_tr16_b64_v4i16((LAS s16x4*)p); }
DI bf16x8 cat8(s16x4 a, s16x4 b) { return __builtin_bit_cast(bf16x8, __builtin_shufflevector(a, b, 0, 1, 2, 3, 4, 5, 6, 7)); }
DI unsigned pk2(float a, float b) { f32x2 f = {a, b}; return __builtin_bit_cast(unsigned, __builtin_convertvector(f, bf16x2)); }
DI float bflo(unsigned u) { return __uint_as_float(u << 16); }
DI float bfhi(unsigned u) { return __uint_as_float(u & 0xffff0000u); }
DI int crow(int i, int h) { return (i & 3) + 8 * (i >> 2) + 4 * h; }
DI void st128(rsrc_t r, unsigned off, u32x4 v) { __builtin_amdgcn_raw_buffer_store_b128(v, r, off, 0, 16); }
DI void st64(rsrc_t r, unsigned off, u32x2 v) { __builtin_amdgcn_raw_buffer_store_b64(v, r, off, 0, 16); }
DI void st32(rsrc_t r, unsigned off, unsigned v) { __builtin_amdgcn_raw_buffer_store_b32(v, r, off, 0, 16); }
DI u32x4 ld128(rsrc_t r, unsigned off) { return __builtin_amdgcn_raw_buffer_load_b128(r, off, 0, 16); }
DI unsigned ld32(rsrc_t r, unsigned off) { return __builtin_amdgcn_raw_buffer_load_b32(r, off, 0, 16); }
DI unsigned flag_ld(const char* ws, int idx) { return __hip_atomic_load((const GAS unsigned*)(ws + WS_FL) + idx, __ATOMIC_RELAXED, __HIP_MEMORY_SCOPE_AGENT); }
DI void flag_st(char* ws, int idx, unsigned v) { __hip_atomic_store((GAS unsigned*)(ws + WS_FL) + idx, v, __ATOMIC_RELAXED, __HIP_MEMORY_SCOPE_AGENT); }

#define M0_IN "s_mov_b32 %0, m0\n\ts_mov_b32 m0, %1\n\ts_nop 0\n\t"
#define M0_OUT "s_mov_b32 m0, %0"
DI void dma_2x2(unsigned m0v, const void* b0, unsigned v0, unsigned v1, const void* b1, unsigned v2, unsigned v3) {
  unsigned sv;
  asm volatile(M0_IN "global_load_lds_dwordx4 %4, %2\n\tglobal_load_lds_dwordx4 %5, %2 offset:1024\n\t"
               "global_load_lds_dwordx4 %6, %3 offset:2048\n\tglobal_load_lds_dwordx4 %7, %3 offset:3072\n\t" M0_OUT
               : "=&s"(sv) : "s"(m0v), "s"(b0), "s"(b1), "v"(v0), "v"(v1), "v"(v2), "v"(v3) : "memory");
}
DI void dma_lin4_sc1(unsigned m0v, const void* b, unsigned vl) {
  unsigned sv;
  asm volatile(M0_IN "global_load_lds_dwordx4 %3, %2 sc1\n\tglobal_load_lds_dwordx4 %3, %2 offset:1024 sc1\n\t"
               "global_load_lds_dwordx4 %3, %2 offset:2048 sc1\n\tglobal_load_lds_dwordx4 %3, %2 offset:3072 sc1\n\t" M0_OUT
               : "=&s"(sv) : "s"(m0v), "s"(b), "v"(vl) : "memory");
}
DI void dma_g1(unsigned m0v, const void* wb, unsigned v0, unsigned v1, const void* hb_m2048, unsigned vl) {
  unsigned sv;
  asm volatile(M0_IN "global_load_lds_dwordx4 %4, %2\n\tglobal_load_lds_dwordx4 %5, %2 offset:1024\n\t"
               "global_load_lds_dwordx4 %6, %3 offset:2048 sc1\n\t"
               "s_mov_b32 exec_hi, 0\n\tglobal_load_lds_dwordx4 %6, %3 offset:3072 sc1\n\ts_mov_b32 exec_hi, -1\n\t" M0_OUT
               : "=&s"(sv) : "s"(m0v), "s"(wb), "s"(hb_m2048), "v"(v0), "v"(v1), "v"(vl) : "memory");
}
DI void dma_wh(unsigned m0v, const void* wb, unsigned v0, unsigned v1) {
  unsigned sv;
  asm volatile(M0_IN "global_load_lds_dwordx4 %3, %2\n\tglobal_load_lds_dwordx4 %4, %2 offset:1024\n\t" M0_OUT
               : "=&s"(sv) : "s"(m0v), "s"(wb), "v"(v0), "v"(v1) : "memory");
}
DI void dma_fh(unsigned m0v, const void* fb_m2048, unsigned vl) {
  unsigned sv;
  asm volatile(M0_IN "global_load_lds_dwordx4 %3, %2 offset:2048 sc1\n\tglobal_load_lds_dwordx4 %3, %2 offset:3072 sc1\n\t" M0_OUT
               : "=&s"(sv) : "s"(m0v), "s"(fb_m2048), "v"(vl) : "memory");
}

DI void spin1(rsrc_t r, unsigned off, unsigned tok) {
  unsigned n = 0;
#pragma nounroll
  do { const unsigned v = ld32(r, off); if (__all(v == tok)) break; __builtin_amdgcn_s_sleep(4); asm volatile("" ::: "memory"); } while (++n < SPIN_CAP);
  __builtin_amdgcn_fence(__ATOMIC_ACQUIRE, "wavefront");
}
DI void peek_issue(unsigned m0v, const void* ws, unsigned off) {
  unsigned sv;
  asm volatile(M0_IN "global_load_lds_dwordx4 %3, %2 sc1\n\t" M0_OUT : "=&s"(sv) : "s"(m0v), "s"(ws), "v"(off) : "memory");
}
DI void peek_check(lptr p, rsrc_t r, unsigned off, unsigned tok) {
  if (__all(*(const volatile LAS unsigned*)p == tok)) return;
  asm volatile("s_waitcnt vmcnt(0)" ::: "memory");
  if (__all(*(const volatile LAS unsigned*)p == tok)) return;
  spin1(r, off, tok);
}
#define RARE(c) __builtin_expect((c) != 0, 0)

DI float wave_sum(float v) {
#define DPPADD(ctrl, rm) v += __int_as_float(__builtin_amdgcn_update_dpp(0, __float_as_int(v), ctrl, rm, 0xf, false))
  DPPADD(0x111, 0xf); DPPADD(0x112, 0xf); DPPADD(0x114, 0xf); DPPADD(0x118, 0xf); DPPADD(0x142, 0xa); DPPADD(0x143, 0xc);
#undef DPPADD
  return __int_as_float(__builtin_amdgcn_readlane(__float_as_int(v), 63));
}
DI float gelu_tanh(float u) {
  const float z = u * (0.7978845608028654f + 0.7978845608028654f * 0.044715f * u * u);
  const float e = __builtin_amdgcn_exp2f(z * (-2.0f * 1.4426950408889634f));
  return u * __builtin_amdgcn_rcpf(1.0f + e);
}
DI float max16(const f32x16& s, float m) {
#pragma unroll
  for (int i = 0; i < 16; i += 2) m = __builtin_fmaxf(__builtin_fmaxf(s[i], s[i + 1]), m);
  return m;
}

#define NEXT_SO() so = so == (NS - 1) * SLOT ? 0 : so + SLOT

template <bool ISV, class Hook>
DI void qkv_consume(lptr L, int& so, int w, int lane, f32x16 (&acc)[2], Hook hook) {
  const int r = lane & 31, h = lane >> 5, wm = w >> 2, wn = w & 3;
  int xa[4];
#pragma unroll
  for (int s = 0; s < 4; ++s) xa[s] = (wm * 2 + (r >> 4)) * 4096 + (r & 15) * 128 + (((2 * s + h) ^ ((r >> 1) & 7)) << 4);
  const int q = (lane & 15) >> 2, p = lane & 3, blk = (lane >> 4) & 1;
  const int wa = h * 4096 + 2048 + q * 256 + (((wn * 4 + blk * 2 + (p >> 1)) ^ (q << 2)) << 4) + (p & 1) * 8;
  bf16x8 xA[2][4], xB[2][4], wA[4], wB[4];
#define QM(X, W, mi, s) do { if (ISV) acc[mi] = MFMA32(X[mi][s], W[s], acc[mi]); else acc[mi] = MFMA32(W[s], X[mi][s], acc[mi]); SB(); } while (0)
#define QRX(X, mi, s) X[mi][s] = ldsr128(sl + xa[s])
#define QRW(W, mi, e) W[2 * mi + e] = cat8(ldstr(sl + wa + e * 8192), ldstr(sl + wa + e * 8192 + 1024))
#define QSTEP(mi, XN, WN, XP, WP, MM) do { lptr sl = L + so; NEXT_SO(); \
    QRX(XN, mi, 0); QRX(XN, mi, 1); QRX(XN, mi, 2); SB(); if (MM) QM(XP, WP, 0, 2 * mi); \
    QRX(XN, mi, 3); QRW(WN, mi, 0); SB(); if (MM) QM(XP, WP, 1, 2 * mi); \
    QRW(WN, mi, 1); SB(); if (MM) { QM(XP, WP, 0, 2 * mi + 1); QM(XP, WP, 1, 2 * mi + 1); } } while (0)
  LBAR(); QSTEP(0, xA, wA, xB, wB, false); LBAR(); QSTEP(1, xA, wA, xB, wB, false);
#pragma unroll 1
  for (int s = 2; s < 14; s += 4) {
    hook(s);     QSTEP(0, xB, wB, xA, wA, true); LBAR(); QSTEP(1, xB, wB, xA, wA, true);
    hook(s + 2); QSTEP(0, xA, wA, xB, wB, true); LBAR(); QSTEP(1, xA, wA, xB, wB, true);
  }
  LBAR(); QSTEP(0, xB, wB, xA, wA, true); LBAR(); QSTEP(1, xB, wB, xA, wA, true);
#pragma unroll
  for (int s = 0; s < 4; ++s) { QM(xB, wB, 0, s); QM(xB, wB, 1, s); }
}

__global__ __launch_bounds__(768, 3) void fused_kernel(const char* __restrict__ x, const char* __restrict__ wqkv, const char* __restrict__ wo,
                                                       const char* __restrict__ w1, const char* __restrict__ b1, const char* __restrict__ w2,
                                                       const char* __restrict__ b2, const char* __restrict__ gw, char* __restrict__ ws,
                                                       char* __restrict__ out) {
  extern __shared__ __attribute__((aligned(16))) char smem[];
  asm volatile("" :: "s"(x), "s"(wqkv), "s"(wo), "s"(w1), "s"(b1), "s"(w2), "s"(b2), "s"(gw), "s"(ws), "s"(out));
  STAMP(0);
  const int b = blockIdx.x, xcd = b & 7, idx = b >> 3;
  const int lane = threadIdx.x & 63, w = rfl(threadIdx.x >> 6);
  const unsigned lds0 = (unsigned)(uintptr_t)smem;
  lptr L = (lptr)smem;
  const int Lx = xcd * 31 + (31 - idx);
  const bool hasQ = idx < 24, hasG1 = idx >= 1 && Lx < 242;
  const int tm = (xcd >> 1) * 4 + idx / 6, tn = (xcd & 1) * 6 + idx % 6;
  const int lnb = xcd * 8 + (idx - 24);
  const int g1ft = Lx < 132 ? Lx / 6 : (Lx - 132) / 5, g1dt = Lx < 132 ? Lx % 6 : 6 + (Lx - 132) % 5;
  const int head = xcd * 4 + (idx & 3), qb = idx >> 2;
  const int st = (xcd >> 1) * 8 + (idx >> 2), dt = (xcd & 1) * 4 + (idx & 3);
  unsigned fq_off;
  { const int l = lane < 12 ? lane : 3, wh = l >> 2; int tmi = l & 3; if (wh == 0 || qb == 0) tmi |= 2;
    fq_off = WS_FL + (FL_Q + ((wh * 4 + (head >> 3)) * 16 + 2 * qb - 2 + tmi) * 32) * 4; }
  const unsigned fu_off = WS_FL + (FL_U + (((lane & 32) ? (4 * dt + 3) / 3 : (4 * dt) / 3) * 32 + min(lane & 31, 21)) * 32) * 4;
  const unsigned fo_off = WS_FL + (FL_O + ((st >> 2) * 32 + (lane & 31)) * 32) * 4;
  const unsigned fh_off = WS_FL + FL_H * 4 + lane * 128;
  const rsrc_t rws = __builtin_amdgcn_make_buffer_rsrc(ws, 0, WS_END, 0x00020000);
  volatile LAS unsigned* relay = (volatile LAS unsigned*)(L + L_C + 64);
  unsigned eoff = WS_FL + FL_EPOCH * 4;
  asm volatile("" : "+v"(eoff));
  const unsigned epoch = ld32(rws, eoff);
#define TOKEN_OF(e) ((e) * 1664525u + 1013904223u)

  if (w >= 8) {
    const int j = w - 8;
#define token TOKEN_OF(epoch)
    const unsigned m0b = lds0 + j * 4096;
    const unsigned vl = lane * 16;
    unsigned so = 0;
    const char* hp = ws + WS_H + (size_t)g1dt * 64 * 3072 + j * 1536 - 2048;
    const char* w1p = w1 + (size_t)(j * 16) * 2752 + g1ft * 128;
    const unsigned u0 = (lane >> 3) * 2752 + ((((lane & 7) ^ (((lane >> 4) & 3) << 1)) & (g1ft == 21 ? 3 : 7)) << 4), u1 = u0 + 8 * 2752 - 1024;
    auto fill_g1 = [&]() { dma_g1(m0b + so, w1p, u0, u1, hp, vl); hp += 6144; w1p += 64 * 2752; NEXT_SO(); };
    const unsigned swz = ((lane & 7) ^ (((lane >> 4) & 1) << 2)) << 4;
    unsigned vw0 = (lane >> 3) * 4096 + swz, vw1 = vw0 + 8 * 4096 - 1024;
    const char* w2p = w2 + (size_t)(j * 16) * 4096 + st * 128;
    const char* up = ws + WS_U + (size_t)dt * 86 * 2048 + j * 2048 - 2048;
    const size_t w2half = (size_t)(j < 2 ? 21 * 64 : 20 * 64) * 4096;
    const int uhalf = j < 2 ? 21 * 8192 : 20 * 8192;
    auto fill_kv = [&]() {
      int t0 = qb * 8 - 8 + 4 * j; t0 = t0 < 0 ? t0 + 8 : t0;
      dma_lin4_sc1(lds0 + L_K + j * 4096, ws + WS_K + (size_t)(head * 64 + t0) * 1024, vl);
      dma_lin4_sc1(lds0 + L_V + j * 4096, ws + WS_V + (size_t)(head * 64 + t0) * 1024, vl);
    };
    int nwh = 0;
    auto fill_wh = [&]() { dma_wh(m0b + so, nwh == 0 ? w2p + w2half : w2p + (size_t)(nwh - 1) * 64 * 4096, vw0, vw1); ++nwh; NEXT_SO(); };
    unsigned so_g2 = 0;

    if (hasQ) {
      const unsigned x0 = (lane >> 3) * 1024 + (((lane & 7) ^ (lane >> 4)) << 4), x1 = (8 + (lane >> 3)) * 1024 + (((lane & 7) ^ (4 + (lane >> 4))) << 4) - 1024;
      const unsigned q0 = (lane >> 4) * 3072 + (((lane & 15) ^ ((lane >> 4) << 2)) << 4), q1 = q0 + 4 * 3072 - 1024;
      const char* xp = x + (size_t)(tm * 128 + (j >> 1) * 64 + (j & 1) * 16) * 1024;
      const char* qp = wqkv + (size_t)(j * 8) * 3072 + tn * 256 - 2048;
      int kc = QROT ? ((idx / 6 + idx % 6) % 3) * 3 : 0;
      auto fill_a = [&]() { dma_2x2(m0b + so, xp + kc * 128, x0, x1, qp + (size_t)kc * 64 * 3072, q0, q1); NEXT_SO(); };
      auto fill_b = [&]() { dma_2x2(m0b + so, xp + kc * 128 + 32 * 1024, x0, x1, qp + (size_t)kc * 64 * 3072 + 32 * 3072, q0, q1); kc = (kc + 1) & 7; NEXT_SO(); };
#pragma unroll 1
      for (int c = 0; c < 3; ++c) { fill_a(); fill_b(); }
#pragma unroll 1
      for (int c = 0; c < 5; ++c) { VBAR(20); fill_a(); VBAR(20); fill_b(); }
      if (hasG1) {
#pragma unroll 1
        for (int s = 10; s < 16; ++s) { VBAR(20); fill_g1(); }
      } else {
        VBAR(20); VBAR(16); VBAR(12); VBAR(8); VBAR(4); VBAR(0);
      }
    } else {
      BAR();
      VBAR(0);
      if (j == 0) spin1(rws, fh_off, token);
      BAR();
#pragma unroll 1
      for (int s = 0; s < 6; ++s) fill_g1();
    }
    STAMP(1);
    if (hasG1) {
#pragma unroll 1
      for (int s = 0; s < 19; ++s) { VBAR(20); fill_g1(); }
      fill_kv();
#pragma unroll 1
      for (int s = 19; s < 26; ++s) { VBAR(20); fill_g1(); }
      so_g2 = so;
      VBAR(20); fill_wh(); VBAR(18); fill_wh(); VBAR(16); fill_wh(); VBAR(14); fill_wh(); VBAR(12); fill_wh(); VBAR(10); fill_wh();
      BAR(); fill_wh();
      BAR();
      BAR();
    } else {
      BAR();
      so_g2 = so;
#pragma unroll 1
      for (int i = 0; i < 7; ++i) fill_wh();
      if (j == 0) spin1(rws, fq_off, token);
      BAR();
      fill_kv();
      VBAR(0);
    }
    STAMP(2);
    {
      if (j == 0) { spin1(rws, fu_off, token); *relay = 1u; }
      else { for (unsigned spins = 0; spins < 64 * SPIN_CAP && *relay == 0u; ++spins) __builtin_amdgcn_s_sleep(1); }
      STAMP(3);
      so = so_g2;
      dma_fh(m0b + so, up + uhalf, vl); NEXT_SO();
#pragma unroll 1
      for (int i = 0; i < 6; ++i) { dma_fh(m0b + so, up, vl); up += 8192; NEXT_SO(); }
    }
    w2p += (size_t)6 * 64 * 4096;
    unsigned wstr = 64 * 4096;
    int ci = 7;
    auto fill_g2 = [&]() {
      if (ci == 22) {
        w2p = wo + (size_t)(j * 16) * 1024 + dt * 128; up = ws + WS_O + (size_t)st * 32 * 2048 + j * 2048 - 2048; wstr = 64 * 1024;
        vw0 = (lane >> 3) * 1024 + swz; vw1 = vw0 + 8 * 1024 - 1024;
      }
      dma_wh(m0b + so, w2p, vw0, vw1); dma_fh(m0b + so, up, vl);
      w2p += wstr; up += 8192; NEXT_SO(); ++ci;
    };
    VBAR(12);
    VBAR(10); fill_g2(); VBAR(12); fill_g2(); VBAR(14); fill_g2(); VBAR(16); fill_g2(); VBAR(18); fill_g2();
#pragma unroll 1
    for (int s = 6; s < 24; ++s) { VBAR(20); fill_g2(); }
    VBAR(20); VBAR(16); VBAR(12); VBAR(8); VBAR(4); VBAR(0);
    BAR(); BAR();
    if (b == 0 && j == 0) {
#pragma unroll 1
      for (int k = 0; k < 4; ++k) spin1(rws, WS_FL + FL_O * 4 + (k * 64 + lane) * 128, token);
      if (lane == 0) st32(rws, WS_FL + FL_EPOCH * 4, token);
    }
    STAMP(4);
    if (j == 0) STAMP_OUT(ws, 1);
    return;
  }

#undef token
  unsigned token;
  const int r = lane & 31, h = lane >> 5;
  const int g4 = lane >> 4, q4 = (lane & 15) >> 2, p4 = lane & 3, blk = (lane >> 4) & 1;
  int so = 0;
  if (threadIdx.x == 4) *relay = 0u;
  if (threadIdx.x < 4) {
    const unsigned cv = (threadIdx.x & 1) ? 0u : 0x3f803f80u;
    *(LAS u32x4*)(L + L_C + (threadIdx.x >> 1) * 512 + (threadIdx.x & 1) * 16) = u32x4{cv, cv, cv, cv};
  }
  lptr pk = L + L_P + lane * 16;

  if (hasQ) {
    token = TOKEN_OF(epoch);
    asm volatile("" : "+v"(token));
    if (w == 7) *(LAS unsigned*)pk = ~token;
    STAMP(1);
    f32x16 acc[2] = {};
    const bool poller = w == 7 && hasG1;
    const unsigned pre = poller ? 1u << 10 : 0u, post = poller ? (1u << 4) | (1u << 6) | (1u << 8) : 0u;
    auto hook = [&](int s) {
      if (RARE((pre >> s) & 1)) peek_check(pk, rws, fh_off, token);
      LBAR();
      if (RARE((post >> s) & 1)) peek_issue(lds0 + L_P, ws, fh_off);
      SB();
    };
    if (tn >= 8) qkv_consume<true>(L, so, w, lane, acc, hook);
    else qkv_consume<false>(L, so, w, lane, acc, hook);
    STAMP(2);
    const int wm = w >> 2, wn = w & 3;
    const int hd0 = (tn & 3) * 8 + wn * 2;
#pragma unroll
    for (int mi = 0; mi < 2; ++mi) {
      const int tb = tm * 4 + wm * 2 + mi;
      const float sc = tn < 4 ? QSCALE : 1.0f;
      const unsigned dst = tn >= 8 ? WS_V + (((hd0 + (r >> 4)) * 64 + tb) * 64 + h * 16 + (r & 15)) * 16
                                   : (tn < 4 ? WS_Q : WS_K) + ((hd0 * 64 + tb) * 64 + lane) * 16;
      const unsigned dstep = tn >= 8 ? 512 : 65536;
#pragma unroll
      for (int s = 0; s < 2; ++s) {
        u32x4 v;
#pragma unroll
        for (int jj = 0; jj < 4; ++jj) v[jj] = pk2(acc[mi][8 * s + 2 * jj] * sc, acc[mi][8 * s + 2 * jj + 1] * sc);
        st128(rws, dst + s * dstep, v);
      }
    }
  } else {
    constexpr int RS = 1056;
    u32x4 xv[4];
#pragma unroll
    for (int i = 0; i < 4; ++i) xv[i] = *(const u32x4*)(x + (size_t)(lnb * 32 + w * 4 + i) * 1024 + lane * 16);
    const u32x4 gv = *(const u32x4*)(gw + lane * 16);
    float gsel = 0.f;
#pragma unroll
    for (int i = 0; i < 4; ++i) {
      float v[8], s = 0.f, gs = 0.f;
#pragma unroll
      for (int jj = 0; jj < 4; ++jj) {
        v[2 * jj] = bflo(xv[i][jj]); v[2 * jj + 1] = bfhi(xv[i][jj]);
        s += v[2 * jj] + v[2 * jj + 1];
        gs += v[2 * jj] * bflo(gv[jj]) + v[2 * jj + 1] * bfhi(gv[jj]);
      }
      s = wave_sum(s); gs = wave_sum(gs);
      const float m = s * (1.0f / 512);
      float qq = 0.f;
#pragma unroll
      for (int jj = 0; jj < 8; ++jj) { v[jj] -= m; qq += v[jj] * v[jj]; }
      qq = wave_sum(qq);
      const float rs = __builtin_amdgcn_rsqf(qq * (1.0f / 512) + 1e-5f);
      u32x4 o;
#pragma unroll
      for (int jj = 0; jj < 4; ++jj) o[jj] = pk2(v[2 * jj] * rs, v[2 * jj + 1] * rs);
      *(LAS u32x4*)(L + (w * 4 + i) * RS + lane * 16) = o;
      gsel = lane == i ? gs : gsel;
    }
    token = TOKEN_OF(epoch);
    asm volatile("" : "+v"(token));
    if (lane < 4) st32(rws, WS_G + (lnb * 32 + w * 4 + lane) * 4, __float_as_uint(1.0f / (1.0f + __expf(-gsel))));
    STAMP(1);
    LBAR();
#pragma unroll
    for (int i = 0; i < 4; ++i) {
      const int D = w * 4 + i, d48 = D / 3;
      lptr a = L + (4 * g4 + q4) * RS + D * 32 + p4 * 8;
      const bf16x8 f = cat8(ldstr(a), ldstr(a + 16 * RS));
      st128(rws, WS_H + ((d48 * 64 + lnb) * 3 + (D - d48 * 3)) * 1024 + lane * 16, __builtin_bit_cast(u32x4, f));
    }
    DBAR();
    if (threadIdx.x == 0) flag_st(ws, FL_H + lnb * 32, token);
    STAMP(2);
    LBAR();
  }

  bf16x8 qf;
  if (hasG1) {
    const int kh = w >> 2, fb = w & 3;
    int ha[3];
#pragma unroll
    for (int db = 0; db < 3; ++db) { const int o = (kh * 3 + db) * 1024 + lane * 16, rg = o / 1536; ha[db] = rg * 4096 + 2048 + (o - rg * 1536); }
    const int wa = kh * 8192 + (4 * g4 + q4) * 128 + ((fb ^ ((2 * g4 + (q4 >> 1)) & 3)) << 5) + p4 * 8;
    f32x4 acc[3] = {};
    bf16x8 wA, hA[3], wB, hB[3];
#define G1RD(W, H) do { lptr sl = L + so; NEXT_SO(); W = cat8(ldstr(sl + wa), ldstr(sl + wa + 4096)); \
    _Pragma("unroll") for (int db = 0; db < 3; ++db) H[db] = ldsr128(sl + ha[db]); SB(); } while (0)
#define G1MM(W, H) do { _Pragma("unroll") for (int db = 0; db < 3; ++db) acc[db] = MFMA16(W, H[db], acc[db]); SB(); } while (0)
    const unsigned pre = (1u << 6) | (w == 7 ? 1u << 18 : 0u);
    const unsigned post = (w == 0 && hasQ ? 1u << 6 : 0u) | (w == 7 ? (1u << 6) | (1u << 12) | (1u << 14) | (1u << 16) : 0u);
    auto hook = [&](int s) {
      if (RARE((pre >> s) & 1)) { if (s == 6) asm volatile("s_waitcnt vmcnt(0)" ::: "memory"); else peek_check(pk, rws, fq_off, token); }
      LBAR();
      if (RARE((post >> s) & 1)) {
        if (s == 6) { if (w == 7) *(LAS unsigned*)pk = ~token; else if (lane == 0) flag_st(ws, FL_Q + (tn * 16 + tm) * 32, token); }
        else peek_issue(lds0 + L_P, ws, fq_off);
      }
      SB();
    };
    LBAR(); G1RD(wA, hA);
#pragma unroll 1
    for (int s = 2; s < 20; s += 2) {
      LBAR();  G1RD(wB, hB); G1MM(wA, hA);
      hook(s); G1RD(wA, hA); G1MM(wB, hB);
    }
    STAMP(10);
    qf = __builtin_bit_cast(bf16x8, ld128(rws, WS_Q + (head * 64 + qb * 8 + w) * 1024 + lane * 16));
    const int fblk = g1ft * 4 + fb;
    u32x2 bb = *(const u32x2*)(b1 + min(fblk * 16 + 4 * g4, 1372) * 2);
#pragma unroll 1
    for (int s = 20; s < 32; s += 2) {
      LBAR(); G1RD(wB, hB); G1MM(wA, hA);
      LBAR(); G1RD(wA, hA); G1MM(wB, hB);
    }
    asm volatile("" : "+v"(qf), "+v"(bb));
    LBAR(); G1RD(wB, hB); G1MM(wA, hA);
    LBAR();
    G1MM(wB, hB);
    STAMP(3);
    if (kh == 1) { *(LAS f32x4*)(L + L_X + ((fb * 3 + 0) * 64 + lane) * 16) = acc[0]; *(LAS f32x4*)(L + L_X + ((fb * 3 + 1) * 64 + lane) * 16) = acc[1]; }
    else *(LAS f32x4*)(L + L_X + ((fb * 3 + 2) * 64 + lane) * 16) = acc[2];
    LBAR();
    if (fblk < 86) {
#pragma unroll
      for (int db = 0; db < 3; ++db) {
        const int D = g1dt * 3 + db;
        if ((db == 2) == (kh == 1) && D < 32) {
          const f32x4 o = *(const LAS f32x4*)(L + L_X + ((fb * 3 + db) * 64 + lane) * 16);
          const float u0 = gelu_tanh(acc[db][0] + o[0] + bflo(bb[0])), u1 = gelu_tanh(acc[db][1] + o[1] + bfhi(bb[0]));
          const float u2 = gelu_tanh(acc[db][2] + o[2] + bflo(bb[1])), u3 = gelu_tanh(acc[db][3] + o[3] + bfhi(bb[1]));
          const u32x2 v = {pk2(u0, u1), pk2(u2, u3)};
          st64(rws, WS_U + ((((D >> 2) * 86 + fblk) * 2 + ((D >> 1) & 1)) * 64 + (g4 >> 1) * 32 + (D & 1) * 16 + (lane & 15)) * 16 + (g4 & 1) * 8, v);
        }
      }
    }
  } else {
    DBAR();
    if (threadIdx.x == 0) flag_st(ws, FL_Q + (tn * 16 + tm) * 32, token);
    LBAR();
    qf = __builtin_bit_cast(bf16x8, ld128(rws, WS_Q + (head * 64 + qb * 8 + w) * 1024 + lane * 16));
    asm volatile("" : "+v"(qf));
    LBAR();
    STAMP(3);
  }

  {
    const int t_lo = qb > 0 ? w + 1 : 8, t_hi = w + 8;
    lptr kp = L + L_K + lane * 16;
    const f32x16 zero = {};
    f32x16 sb;
    {
      const f32x16 sd = MFMA32(ldsr128(kp + (w + 8) * 1024), qf, zero);
      const f32x16 sf = MFMA32(ldsr128(kp + w * 1024), qf, zero);
#pragma unroll
      for (int i = 0; i < 16; ++i) sb[i] = (crow(i, h) <= r) ? sd[i] : (qb > 0 ? sf[i] : -INFINITY);
    }
    float m = max16(sb, -INFINITY);
#pragma unroll 1
    for (int t = t_lo; t < t_hi; ++t) {
      const f32x16 s = MFMA32(ldsr128(kp + t * 1024), qf, zero);
      m = max16(s, m);
    }
    {
      auto rr = __builtin_amdgcn_permlane32_swap(__float_as_uint(m), __float_as_uint(m), false, false);
      m = __builtin_fmaxf(__uint_as_float(rr[0]), __uint_as_float(rr[1]));
    }
    if (hasG1) {
      DBAR();
      if (threadIdx.x == 0) flag_st(ws, FL_U + (g1dt * 32 + g1ft) * 32, token);
    }
    STAMP(4);
    f32x16 cin;
#pragma unroll
    for (int i = 0; i < 16; ++i) cin[i] = -m;
    const int vbase = r < 16 ? L_V + h * 256 + r * 16 : (L_C + ((r == 16 || r == 20) ? 0 : 16));
    const int vstr = r < 16 ? 1024 : 0;
    f32x16 o = {};
    {
      u32x4 pd[2], pf[2];
#pragma unroll
      for (int i = 0; i < 16; i += 2) {
        const float p0 = __builtin_amdgcn_exp2f(sb[i] - m), p1 = __builtin_amdgcn_exp2f(sb[i + 1] - m);
        const bool v0 = crow(i, h) <= r, v1 = crow(i + 1, h) <= r;
        pd[i >> 3][(i >> 1) & 3] = pk2(v0 ? p0 : 0.f, v1 ? p1 : 0.f);
        pf[i >> 3][(i >> 1) & 3] = pk2(v0 ? 0.f : p0, v1 ? 0.f : p1);
      }
      lptr vp = L + vbase + (w + 8) * vstr;
      o = MFMA32(ldsr128(vp), __builtin_bit_cast(bf16x8, pd[0]), o);
      o = MFMA32(ldsr128(vp + 512), __builtin_bit_cast(bf16x8, pd[1]), o);
      if (qb > 0) {
        lptr vq = L + vbase + w * vstr;
        o = MFMA32(ldsr128(vq), __builtin_bit_cast(bf16x8, pf[0]), o);
        o = MFMA32(ldsr128(vq + 512), __builtin_bit_cast(bf16x8, pf[1]), o);
      }
    }
#pragma unroll 1
    for (int t = t_lo; t < t_hi; ++t) {
      const f32x16 s = MFMA32(ldsr128(kp + t * 1024), qf, cin);
      u32x4 pp[2];
#pragma unroll
      for (int i = 0; i < 16; i += 2) pp[i >> 3][(i >> 1) & 3] = pk2(__builtin_amdgcn_exp2f(s[i]), __builtin_amdgcn_exp2f(s[i + 1]));
      lptr vp = L + vbase + t * vstr;
      o = MFMA32(ldsr128(vp), __builtin_bit_cast(bf16x8, pp[0]), o);
      o = MFMA32(ldsr128(vp + 512), __builtin_bit_cast(bf16x8, pp[1]), o);
    }
    const float il = 1.0f / o[8];
    u32x4 ov;
#pragma unroll
    for (int jj = 0; jj < 4; ++jj) ov[jj] = pk2(o[2 * jj] * il, o[2 * jj + 1] * il);
    const int B = qb * 8 + w;
    st128(rws, WS_O + (((B >> 1) * 32 + head) * 2 + (B & 1)) * 1024 + lane * 16, ov);
  }
  STAMP(5);

  {
    const int kh = w >> 2, mt = (w >> 1) & 1, nt = w & 1;
    const int tok = st * 64 + nt * 32 + r;
    const int ta2 = kh * 8192 + (8 * h + q4) * 128 + (((nt * 4 + blk * 2 + (p4 >> 1)) ^ ((q4 >> 1) << 2)) << 4) + (p4 & 1) * 8;
    const int tao = kh * 8192 + (4 * h + q4) * 128 + (((mt * 4 + blk * 2 + (p4 >> 1)) ^ ((q4 >> 1) << 2)) << 4) + (p4 & 1) * 8;
    const int fa2 = kh * 8192 + 2048 + mt * 1024 + lane * 16;
    const int fao = kh * 8192 + 2048 + nt * 1024 + lane * 16;
    f32x16 ay = {}, aa = {};
    bf16x8 pa0, pb0, pa1, pb1, qa0, qb0, qa1, qb1;
    float g = 0.f; unsigned b2v = 0;
#define RD2(A0, B0, A1, B1) do { lptr sl = L + so; NEXT_SO(); B0 = cat8(ldstr(sl + ta2), ldstr(sl + ta2 + 512)); A0 = ldsr128(sl + fa2); \
    B1 = cat8(ldstr(sl + ta2 + 4096), ldstr(sl + ta2 + 4096 + 512)); A1 = ldsr128(sl + fa2 + 4096); SB(); } while (0)
#define RDO(A0, B0, A1, B1) do { lptr sl = L + so; NEXT_SO(); A0 = cat8(ldstr(sl + tao), ldstr(sl + tao + 1024)); B0 = ldsr128(sl + fao); \
    A1 = cat8(ldstr(sl + tao + 4096), ldstr(sl + tao + 4096 + 1024)); B1 = ldsr128(sl + fao + 4096); SB(); } while (0)
#define MM2(ACC, A0, B0, A1, B1) do { ACC = MFMA32(A0, B0, ACC); ACC = MFMA32(A1, B1, ACC); SB(); } while (0)
    const unsigned pre = (1u << 4) | (w == 7 ? 1u << 16 : 0u);
    const unsigned post = (w == 0 ? 1u << 4 : 0u) | (w == 7 ? (1u << 4) | (1u << 10) | (1u << 12) | (1u << 14) : 0u);
    auto hook = [&](int s) {
      if (RARE((pre >> s) & 1)) { if (s == 4) asm volatile("s_waitcnt vmcnt(0)" ::: "memory"); else peek_check(pk, rws, fo_off, token); }
      LBAR();
      if (RARE((post >> s) & 1)) {
        if (s == 4) { if (w == 7) *(LAS unsigned*)pk = ~token; else if (lane == 0) flag_st(ws, FL_O + (qb * 32 + head) * 32, token); }
        else peek_issue(lds0 + L_P, ws, fo_off);
      }
      SB();
    };
    LBAR(); STAMP(6);
    RD2(pa0, pb0, pa1, pb1);
    if (kh) { pa0 = bf16x8{}; pa1 = bf16x8{}; pb0 = bf16x8{}; pb1 = bf16x8{}; }
#pragma unroll 1
    for (int s = 2; s < 22; s += 2) {
      LBAR();  RD2(qa0, qb0, qa1, qb1); MM2(ay, pa0, pb0, pa1, pb1);
      hook(s); RD2(pa0, pb0, pa1, pb1); MM2(ay, qa0, qb0, qa1, qb1);
    }
    g = __uint_as_float(ld32(rws, WS_G + tok * 4));
    b2v = ((const unsigned short*)b2)[tok];
    LBAR(); RD2(qa0, qb0, qa1, qb1); MM2(ay, pa0, pb0, pa1, pb1);
    STAMP(7);
    LBAR(); RDO(pa0, pb0, pa1, pb1); MM2(ay, qa0, qb0, qa1, qb1);
#pragma unroll 1
    for (int s = 23; s < 29; s += 2) {
      LBAR(); RDO(qa0, qb0, qa1, qb1); MM2(aa, pa0, pb0, pa1, pb1);
      LBAR(); RDO(pa0, pb0, pa1, pb1); MM2(aa, qa0, qb0, qa1, qb1);
    }
    LBAR(); RDO(qa0, qb0, qa1, qb1); MM2(aa, pa0, pb0, pa1, pb1);
    MM2(aa, qa0, qb0, qa1, qb1);
    STAMP(8);
    const float og = 1.0f - g;
    float v[16];
#pragma unroll
    for (int i = 0; i < 16; ++i) v[i] = ay[i] * og + aa[i] * g;
    LBAR();
    {
      lptr ex = L + w * 2048 + lane * 16;
      f32x4 s0, s1;
#pragma unroll
      for (int jj = 0; jj < 4; ++jj) { s0[jj] = kh ? v[jj] : v[8 + jj]; s1[jj] = kh ? v[4 + jj] : v[12 + jj]; }
      *(LAS f32x4*)(ex) = s0; *(LAS f32x4*)(ex + 1024) = s1;
    }
    LBAR();
    {
      lptr ex = L + (w ^ 4) * 2048 + lane * 16;
      const f32x4 r0 = *(const LAS f32x4*)(ex), r1 = *(const LAS f32x4*)(ex + 1024);
      const float bo = bflo(b2v) * og;
      float f[8];
#pragma unroll
      for (int jj = 0; jj < 4; ++jj) { f[jj] = (kh ? v[8 + jj] : v[jj]) + r0[jj] + bo; f[4 + jj] = (kh ? v[12 + jj] : v[4 + jj]) + r1[jj] + bo; }
      const unsigned a0 = pk2(f[0], f[1]), a1 = pk2(f[2], f[3]), c0 = pk2(f[4], f[5]), c1 = pk2(f[6], f[7]);
      auto x0 = __builtin_amdgcn_permlane32_swap(a0, c0, false, false);
      auto x1 = __builtin_amdgcn_permlane32_swap(a1, c1, false, false);
      const u32x4 ov = {x0[0], x1[0], x0[1], x1[1]};
      *(u32x4*)(out + (size_t)tok * 1024 + (dt * 64 + mt * 32 + kh * 16 + h * 8) * 2) = ov;
    }
  }
  STAMP(9);
  if (w == 0) STAMP_OUT(ws, 0);
}

extern "C" void kernel_launch(void* const* d_in, const int* in_sizes, int n_in, void* d_out, int out_size, void* d_ws, size_t ws_size,
                              hipStream_t stream) {
  (void)hipFuncSetAttribute((const void*)fused_kernel, hipFuncAttributeMaxDynamicSharedMemorySize, LDS_BYTES);
  fused_kernel<<<256, 768, LDS_BYTES, stream>>>((const char*)d_in[0], (const char*)d_in[1], (const char*)d_in[2], (const char*)d_in[4],
                                                (const char*)d_in[5], (const char*)d_in[6], (const char*)d_in[7], (const char*)d_in[8],
                                                (char*)d_ws, (char*)d_out);
}
```

```cpp
#include <hip/hip_runtime.h>
#include <hip/hip_bf16.h>
#include <stdint.h>

#define LAS __attribute__((address_space(3)))
#define GAS __attribute__((address_space(1)))
#define DI __device__ __forceinline__
typedef __attribute__((ext_vector_type(8))) __bf16 bf16x8;
typedef __attribute__((ext_vector_type(2))) __bf16 bf16x2;
typedef __attribute__((ext_vector_type(4))) short s16x4;
typedef __attribute__((ext_vector_type(2))) float f32x2;
typedef __attribute__((ext_vector_type(4))) float f32x4;
typedef __attribute__((ext_vector_type(16))) float f32x16;
typedef __attribute__((ext_vector_type(2))) unsigned u32x2;
typedef __attribute__((ext_vector_type(4))) unsigned u32x4;
typedef LAS char* lptr;
typedef __amdgpu_buffer_rsrc_t rsrc_t;

constexpr unsigned WS_Q = 0, WS_K = 2u << 20, WS_V = 4u << 20, WS_O = 6u << 20, WS_U = 8u << 20, WS_H = 10u << 20,
                   WS_G = 13u << 20, WS_FL = 14u << 20, WS_END = 15u << 20;
constexpr int FL_H = 0;
constexpr int FL_Q = 64 * 32;
constexpr int FL_U = FL_Q + 192 * 32;
constexpr int FL_O = FL_U + 352 * 32;
constexpr int FL_EPOCH = FL_O + 256 * 32;
constexpr float QSCALE = 0.25f * 1.4426950408889634f;
constexpr int NS = 7, SLOT = 16384, RING = NS * SLOT;
constexpr int L_K = RING, L_V = RING + 16384, L_X = RING + 32768  , L_C = L_X + 12288  , L_P = L_C + 1024  ;
constexpr int LDS_BYTES = 163840;
constexpr unsigned SPIN_CAP = 200000;
#define PREFETCH 0
#define QROT 1

#define MFMA32(a, b, c) __builtin_amdgcn_mfma_f32_32x32x16_bf16(a, b, c, 0, 0, 0)
#define MFMA16(a, b, c) __builtin_amdgcn_mfma_f32_16x16x32_bf16(a, b, c, 0, 0, 0)
#define SB() __builtin_amdgcn_sched_barrier(0)
#define LBAR() do { asm volatile("s_waitcnt lgkmcnt(0)\n\ts_barrier" ::: "memory"); SB(); } while (0)
#define DBAR() do { asm volatile("s_waitcnt vmcnt(0) lgkmcnt(0)\n\ts_barrier" ::: "memory"); SB(); } while (0)
#define VBAR(N) asm volatile("s_waitcnt vmcnt(" #N ")\n\ts_barrier" ::: "memory")
#define BAR() asm volatile("s_barrier" ::: "memory")

#define STAMP(i) do {} while (0)
#define STAMP_OUT(ws, slot) do {} while (0)

DI int rfl(int x) { return __builtin_amdgcn_readfirstlane(x); }
DI bf16x8 ldsr128(lptr p) { return *(const LAS bf16x8*)p; }
DI s16x4 ldstr(lptr p) { return __builtin_amdgcn_ds_read_tr16_b64_v4i16((LAS s16x4*)p); }
DI bf16x8 cat8(s16x4 a, s16x4 b) { return __builtin_bit_cast(bf16x8, __builtin_shufflevector(a, b, 0, 1, 2, 3, 4, 5, 6, 7)); }
DI unsigned pk2(float a, float b) { f32x2 f = {a, b}; return __builtin_bit_cast(unsigned, __builtin_convertvector(f, bf16x2)); }
DI float bflo(unsigned u) { return __uint_as_float(u << 16); }
DI float bfhi(unsigned u) { return __uint_as_float(u & 0xffff0000u); }
DI int crow(int i, int h) { return (i & 3) + 8 * (i >> 2) + 4 * h; }
DI void st128(rsrc_t r, unsigned off, u32x4 v) { __builtin_amdgcn_raw_buffer_store_b128(v, r, off, 0, 16); }
DI void st64(rsrc_t r, unsigned off, u32x2 v) { __builtin_amdgcn_raw_buffer_store_b64(v, r, off, 0, 16); }
DI void st32(rsrc_t r, unsigned off, unsigned v) { __builtin_amdgcn_raw_buffer_store_b32(v, r, off, 0, 16); }
DI u32x4 ld128(rsrc_t r, unsigned off) { return __builtin_amdgcn_raw_buffer_load_b128(r, off, 0, 16); }
DI unsigned ld32(rsrc_t r, unsigned off) { return __builtin_amdgcn_raw_buffer_load_b32(r, off, 0, 16); }
DI unsigned flag_ld(const char* ws, int idx) { return __hip_atomic_load((const GAS unsigned*)(ws + WS_FL) + idx, __ATOMIC_RELAXED, __HIP_MEMORY_SCOPE_AGENT); }
DI void flag_st(char* ws, int idx, unsigned v) { __hip_atomic_store((GAS unsigned*)(ws + WS_FL) + idx, v, __ATOMIC_RELAXED, __HIP_MEMORY_SCOPE_AGENT); }

#define M0_IN "s_mov_b32 %0, m0\n\ts_mov_b32 m0, %1\n\ts_nop 0\n\t"
#define M0_OUT "s_mov_b32 m0, %0"
DI void dma_2x2(unsigned m0v, const void* b0, unsigned v0, unsigned v1, const void* b1, unsigned v2, unsigned v3) {
  unsigned sv;
  asm volatile(M0_IN "global_load_lds_dwordx4 %4, %2\n\tglobal_load_lds_dwordx4 %5, %2 offset:1024\n\t"
               "global_load_lds_dwordx4 %6, %3 offset:2048\n\tglobal_load_lds_dwordx4 %7, %3 offset:3072\n\t" M0_OUT
               : "=&s"(sv) : "s"(m0v), "s"(b0), "s"(b1), "v"(v0), "v"(v1), "v"(v2), "v"(v3) : "memory");
}
DI void dma_lin4_sc1(unsigned m0v, const void* b, unsigned vl) {
  unsigned sv;
  asm volatile(M0_IN "global_load_lds_dwordx4 %3, %2 sc1\n\tglobal_load_lds_dwordx4 %3, %2 offset:1024 sc1\n\t"
               "global_load_lds_dwordx4 %3, %2 offset:2048 sc1\n\tglobal_load_lds_dwordx4 %3, %2 offset:3072 sc1\n\t" M0_OUT
               : "=&s"(sv) : "s"(m0v), "s"(b), "v"(vl) : "memory");
}
DI void dma_g1(unsigned m0v, const void* wb, unsigned v0, unsigned v1, const void* hb_m2048, unsigned vl) {
  unsigned sv;
  asm volatile(M0_IN "global_load_lds_dwordx4 %4, %2\n\tglobal_load_lds_dwordx4 %5, %2 offset:1024\n\t"
               "global_load_lds_dwordx4 %6, %3 offset:2048 sc1\n\t"
               "s_mov_b32 exec_hi, 0\n\tglobal_load_lds_dwordx4 %6, %3 offset:3072 sc1\n\ts_mov_b32 exec_hi, -1\n\t" M0_OUT
               : "=&s"(sv) : "s"(m0v), "s"(wb), "s"(hb_m2048), "v"(v0), "v"(v1), "v"(vl) : "memory");
}
DI void dma_hh(unsigned m0v, const void* hb_m2048, unsigned vl) {
  unsigned sv;
  asm volatile(M0_IN "global_load_lds_dwordx4 %3, %2 offset:2048 sc1\n\t"
               "s_mov_b32 exec_hi, 0\n\tglobal_load_lds_dwordx4 %3, %2 offset:3072 sc1\n\ts_mov_b32 exec_hi, -1\n\t" M0_OUT
               : "=&s"(sv) : "s"(m0v), "s"(hb_m2048), "v"(vl) : "memory");
}
DI void dma_lin4(unsigned m0v, const void* b, unsigned vl) {
  unsigned sv;
  asm volatile(M0_IN "global_load_lds_dwordx4 %3, %2\n\tglobal_load_lds_dwordx4 %3, %2 offset:1024\n\t"
               "global_load_lds_dwordx4 %3, %2 offset:2048\n\tglobal_load_lds_dwordx4 %3, %2 offset:3072\n\t" M0_OUT
               : "=&s"(sv) : "s"(m0v), "s"(b), "v"(vl) : "memory");
}
DI void dma_wh(unsigned m0v, const void* wb, unsigned v0, unsigned v1) {
  unsigned sv;
  asm volatile(M0_IN "global_load_lds_dwordx4 %3, %2\n\tglobal_load_lds_dwordx4 %4, %2 offset:1024\n\t" M0_OUT
               : "=&s"(sv) : "s"(m0v), "s"(wb), "v"(v0), "v"(v1) : "memory");
}
DI void dma_fh(unsigned m0v, const void* fb_m2048, unsigned vl) {
  unsigned sv;
  asm volatile(M0_IN "global_load_lds_dwordx4 %3, %2 offset:2048 sc1\n\tglobal_load_lds_dwordx4 %3, %2 offset:3072 sc1\n\t" M0_OUT
               : "=&s"(sv) : "s"(m0v), "s"(fb_m2048), "v"(vl) : "memory");
}

DI void spin1(rsrc_t r, unsigned off, unsigned tok) {
  unsigned n = 0;
#pragma nounroll
  do { const unsigned v = ld32(r, off); if (__all(v == tok)) break; __builtin_amdgcn_s_sleep(4); asm volatile("" ::: "memory"); } while (++n < SPIN_CAP);
  __builtin_amdgcn_fence(__ATOMIC_ACQUIRE, "wavefront");
}
DI void peek_issue(unsigned m0v, const void* ws, unsigned off) {
  unsigned sv;
  asm volatile(M0_IN "global_load_lds_dwordx4 %3, %2 sc1\n\t" M0_OUT : "=&s"(sv) : "s"(m0v), "s"(ws), "v"(off) : "memory");
}
DI void peek_check(lptr p, rsrc_t r, unsigned off, unsigned tok) {
  if (__all(*(const volatile LAS unsigned*)p == tok)) return;
  asm volatile("s_waitcnt vmcnt(0)" ::: "memory");
  if (__all(*(const volatile LAS unsigned*)p == tok)) return;
  spin1(r, off, tok);
}
#define RARE(c) __builtin_expect((c) != 0, 0)

DI float wave_sum(float v) {
#define DPPADD(ctrl, rm) v += __int_as_float(__builtin_amdgcn_update_dpp(0, __float_as_int(v), ctrl, rm, 0xf, false))
  DPPADD(0x111, 0xf); DPPADD(0x112, 0xf); DPPADD(0x114, 0xf); DPPADD(0x118, 0xf); DPPADD(0x142, 0xa); DPPADD(0x143, 0xc);
#undef DPPADD
  return __int_as_float(__builtin_amdgcn_readlane(__float_as_int(v), 63));
}
DI float gelu_tanh(float u) {
  const float z = u * (0.7978845608028654f + 0.7978845608028654f * 0.044715f * u * u);
  const float e = __builtin_amdgcn_exp2f(z * (-2.0f * 1.4426950408889634f));
  return u * __builtin_amdgcn_rcpf(1.0f + e);
}
DI float max16(const f32x16& s, float m) {
#pragma unroll
  for (int i = 0; i < 16; i += 2) m = __builtin_fmaxf(__builtin_fmaxf(s[i], s[i + 1]), m);
  return m;
}

#define NEXT_SO() so = so == (NS - 1) * SLOT ? 0 : so + SLOT

template <bool ISV, class Hook>
DI void qkv_consume(lptr L, int& so, int w, int lane, f32x16 (&acc)[2], Hook hook) {
  const int r = lane & 31, h = lane >> 5, wm = w >> 2, wn = w & 3;
  int xa[4];
#pragma unroll
  for (int s = 0; s < 4; ++s) xa[s] = (wm * 2 + (r >> 4)) * 4096 + (r & 15) * 128 + (((2 * s + h) ^ ((r >> 1) & 7)) << 4);
  const int q = (lane & 15) >> 2, p = lane & 3, blk = (lane >> 4) & 1;
  const int wa = h * 4096 + 2048 + q * 256 + (((wn * 4 + blk * 2 + (p >> 1)) ^ (q << 2)) << 4) + (p & 1) * 8;
  bf16x8 xA[2][4], xB[2][4], wA[4], wB[4];
#define QM(X, W, mi, s) do { if (ISV) acc[mi] = MFMA32(X[mi][s], W[s], acc[mi]); else acc[mi] = MFMA32(W[s], X[mi][s], acc[mi]); SB(); } while (0)
#define QRX(X, mi, s) X[mi][s] = ldsr128(sl + xa[s])
#define QRW(W, mi, e) W[2 * mi + e] = cat8(ldstr(sl + wa + e * 8192), ldstr(sl + wa + e * 8192 + 1024))
#define QSTEP(mi, XN, WN, XP, WP, MM) do { lptr sl = L + so; NEXT_SO(); \
    QRX(XN, mi, 0); QRX(XN, mi, 1); QRX(XN, mi, 2); SB(); if (MM) QM(XP, WP, 0, 2 * mi); \
    QRX(XN, mi, 3); QRW(WN, mi, 0); SB(); if (MM) QM(XP, WP, 1, 2 * mi); \
    QRW(WN, mi, 1); SB(); if (MM) { QM(XP, WP, 0, 2 * mi + 1); QM(XP, WP, 1, 2 * mi + 1); } } while (0)
  LBAR(); QSTEP(0, xA, wA, xB, wB, false); LBAR(); QSTEP(1, xA, wA, xB, wB, false);
#pragma unroll 1
  for (int s = 2; s < 14; s += 4) {
    hook(s);     QSTEP(0, xB, wB, xA, wA, true); LBAR(); QSTEP(1, xB, wB, xA, wA, true);
    hook(s + 2); QSTEP(0, xA, wA, xB, wB, true); LBAR(); QSTEP(1, xA, wA, xB, wB, true);
  }
  hook(14); QSTEP(0, xB, wB, xA, wA, true); LBAR(); QSTEP(1, xB, wB, xA, wA, true);
#pragma unroll
  for (int s = 0; s < 4; ++s) { QM(xB, wB, 0, s); QM(xB, wB, 1, s); }
}

__global__ __launch_bounds__(768, 3) void fused_kernel(const char* __restrict__ x, const char* __restrict__ wqkv, const char* __restrict__ wo,
                                                       const char* __restrict__ w1, const char* __restrict__ b1, const char* __restrict__ w2,
                                                       const char* __restrict__ b2, const char* __restrict__ gw, char* __restrict__ ws,
                                                       char* __restrict__ out) {
  extern __shared__ __attribute__((aligned(16))) char smem[];
  asm volatile("" :: "s"(x), "s"(wqkv), "s"(wo), "s"(w1), "s"(b1), "s"(w2), "s"(b2), "s"(gw), "s"(ws), "s"(out));
  STAMP(0);
  const int b = blockIdx.x, xcd = b & 7, idx = b >> 3;
  const int lane = threadIdx.x & 63, w = rfl(threadIdx.x >> 6);
  const unsigned lds0 = (unsigned)(uintptr_t)smem;
  lptr L = (lptr)smem;
  const int Lx = xcd * 31 + (31 - idx);
  const bool hasQ = idx < 24, hasG1 = idx >= 1 && Lx < 242;
  const int tm = (xcd >> 1) * 4 + idx / 6, tn = (xcd & 1) * 6 + idx % 6;
  const int lnb = xcd * 8 + (idx - 24);
  const int g1ft = Lx < 132 ? Lx / 6 : (Lx - 132) / 5, g1dt = Lx < 132 ? Lx % 6 : 6 + (Lx - 132) % 5;
  const int head = xcd * 4 + (idx & 3), qb = idx >> 2;
  const int st = (xcd >> 1) * 8 + (idx >> 2), dt = (xcd & 1) * 4 + (idx & 3);
  unsigned fq_off;
  { const int l = lane < 12 ? lane : 3, wh = l >> 2; int tmi = l & 3; if (wh == 0 || qb == 0) tmi |= 2;
    fq_off = WS_FL + (FL_Q + ((wh * 4 + (head >> 3)) * 16 + 2 * qb - 2 + tmi) * 32) * 4; }
  const unsigned fu_off = WS_FL + (FL_U + (((lane & 32) ? (4 * dt + 3) / 3 : (4 * dt) / 3) * 32 + min(lane & 31, 21)) * 32) * 4;
  const unsigned fo_off = WS_FL + (FL_O + ((st >> 2) * 32 + (lane & 31)) * 32) * 4;
  const unsigned fh_off = WS_FL + FL_H * 4 + lane * 128;
  const rsrc_t rws = __builtin_amdgcn_make_buffer_rsrc(ws, 0, WS_END, 0x00020000);
  volatile LAS unsigned* relay = (volatile LAS unsigned*)(L + L_C + 64);
  unsigned eoff = WS_FL + FL_EPOCH * 4;
  asm volatile("" : "+v"(eoff));
  const unsigned epoch = ld32(rws, eoff);
#define TOKEN_OF(e) ((e) * 1664525u + 1013904223u)

  if (w >= 8) {
    const int j = w - 8;
#define token TOKEN_OF(epoch)
    const unsigned m0b = lds0 + j * 4096;
    const unsigned vl = lane * 16;
    unsigned so = 0;
    const char* hp = ws + WS_H + (size_t)g1dt * 64 * 3072 + j * 1536 - 2048;
    const char* w1p = w1 + (size_t)(j * 16) * 2752 + g1ft * 128;
    const unsigned u0 = (lane >> 3) * 2752 + ((((lane & 7) ^ (((lane >> 4) & 3) << 1)) & (g1ft == 21 ? 3 : 7)) << 4), u1 = u0 + 8 * 2752 - 1024;
    auto fill_g1 = [&]() { dma_g1(m0b + so, w1p, u0, u1, hp, vl); hp += 6144; w1p += 64 * 2752; NEXT_SO(); };
    auto fill_g1w = [&]() { dma_wh(m0b + so, w1p, u0, u1); w1p += 64 * 2752; NEXT_SO(); };
    auto fill_g1h4 = [&](unsigned so0) {
      const unsigned keep = so; so = so0;
#pragma unroll 1
      for (int i = 0; i < 4; ++i) { dma_hh(m0b + so, hp, vl); hp += 6144; NEXT_SO(); }
      so = keep;
    };
    const unsigned swz = ((lane & 7) ^ (((lane >> 4) & 1) << 2)) << 4;
    unsigned vw0 = (lane >> 3) * 4096 + swz, vw1 = vw0 + 8 * 4096 - 1024;
    const char* w2p = w2 + (size_t)(j * 16) * 4096 + st * 128;
    const char* up = ws + WS_U + (size_t)dt * 86 * 2048 + j * 2048 - 2048;
    const size_t w2half = (size_t)(j < 2 ? 21 * 64 : 20 * 64) * 4096;
    const int uhalf = j < 2 ? 21 * 8192 : 20 * 8192;
    auto fill_kv = [&]() {
      int t0 = qb * 8 - 8 + 4 * j; t0 = t0 < 0 ? t0 + 8 : t0;
      dma_lin4_sc1(lds0 + L_K + j * 4096, ws + WS_K + (size_t)(head * 64 + t0) * 1024, vl);
      dma_lin4_sc1(lds0 + L_V + j * 4096, ws + WS_V + (size_t)(head * 64 + t0) * 1024, vl);
    };
    int nwh = 0;
    auto fill_wh = [&]() { dma_wh(m0b + so, nwh == 0 ? w2p + w2half : w2p + (size_t)(nwh - 1) * 64 * 4096, vw0, vw1); ++nwh; NEXT_SO(); };
    unsigned so_g2 = 0;

    if (hasQ) {
      const unsigned x0 = (lane >> 3) * 1024 + (((lane & 7) ^ (lane >> 4)) << 4), x1 = (8 + (lane >> 3)) * 1024 + (((lane & 7) ^ (4 + (lane >> 4))) << 4) - 1024;
      const unsigned q0 = (lane >> 4) * 3072 + (((lane & 15) ^ ((lane >> 4) << 2)) << 4), q1 = q0 + 4 * 3072 - 1024;
      const char* xp = x + (size_t)(tm * 128 + (j >> 1) * 64 + (j & 1) * 16) * 1024;
      const char* qp = wqkv + (size_t)(j * 8) * 3072 + tn * 256 - 2048;
      int kc = QROT ? ((idx / 6 + idx % 6) % 3) * 3 : 0;
      auto fill_a = [&]() { dma_2x2(m0b + so, xp + kc * 128, x0, x1, qp + (size_t)kc * 64 * 3072, q0, q1); NEXT_SO(); };
      auto fill_b = [&]() { dma_2x2(m0b + so, xp + kc * 128 + 32 * 1024, x0, x1, qp + (size_t)kc * 64 * 3072 + 32 * 3072, q0, q1); kc = (kc + 1) & 7; NEXT_SO(); };
#pragma unroll 1
      for (int c = 0; c < 3; ++c) { fill_a(); fill_b(); }
#pragma unroll 1
      for (int c = 0; c < 5; ++c) { VBAR(20); fill_a(); VBAR(20); fill_b(); }
      if (hasG1) {
        const unsigned so0 = so;
        VBAR(20); fill_g1w(); VBAR(18); fill_g1w(); VBAR(16); fill_g1w(); VBAR(14); fill_g1w();
        VBAR(12); fill_g1h4(so0); fill_g1();
        VBAR(20); fill_g1();
      } else {
        VBAR(20); VBAR(16); VBAR(12); VBAR(8); VBAR(4); VBAR(0);
      }
    } else {
      if (PREFETCH && j > 0) {
        __builtin_amdgcn_s_sleep(8);
        const int pw = lnb * 3 + (j - 1);
        const unsigned md = lds0 + L_K + j * 4096;
#pragma unroll 1
        for (int i = pw; i < 1376; i += 192) dma_lin4(md, w1 + (size_t)i * 4096, vl);
#pragma unroll 1
        for (int i = pw; i < 1376; i += 192) dma_lin4(md, w2 + (size_t)i * 4096, vl);
        if (pw < 128) dma_lin4(md, wo + (size_t)pw * 4096, vl);
        BAR(); BAR();
      } else {
        BAR();
        VBAR(0);
      }
#pragma unroll 1
      for (int s = 0; s < 4; ++s) fill_g1w();
      if (j == 0) spin1(rws, fh_off, token);
      BAR();
      fill_g1h4(0); fill_g1(); fill_g1();
    }
    STAMP(1);
    if (hasG1) {
      VBAR(14); fill_g1(); VBAR(16); fill_g1(); VBAR(18); fill_g1();
#pragma unroll 1
      for (int s = 3; s < 19; ++s) { VBAR(20); fill_g1(); }
      fill_kv();
#pragma unroll 1
      for (int s = 19; s < 26; ++s) { VBAR(20); fill_g1(); }
      so_g2 = so;
      VBAR(20); fill_wh(); VBAR(18); fill_wh(); VBAR(16); fill_wh(); VBAR(14); fill_wh(); VBAR(12); fill_wh(); VBAR(10); fill_wh();
      BAR(); fill_wh();
      BAR();
      BAR();
    } else {
      BAR();
      so_g2 = so;
#pragma unroll 1
      for (int i = 0; i < 7; ++i) fill_wh();
      if (j == 0) spin1(rws, fq_off, token);
      BAR();
      fill_kv();
      VBAR(0);
    }
    STAMP(2);
    {
      if (j == 0) { spin1(rws, fu_off, token); *relay = 1u; }
      else { for (unsigned spins = 0; spins < 64 * SPIN_CAP && *relay == 0u; ++spins) __builtin_amdgcn_s_sleep(1); }
      STAMP(3);
      so = so_g2;
      dma_fh(m0b + so, up + uhalf, vl); NEXT_SO();
#pragma unroll 1
      for (int i = 0; i < 6; ++i) { dma_fh(m0b + so, up, vl); up += 8192; NEXT_SO(); }
    }
    w2p += (size_t)6 * 64 * 4096;
    unsigned wstr = 64 * 4096;
    int ci = 7;
    auto fill_g2 = [&]() {
      if (ci == 22) {
        w2p = wo + (size_t)(j * 16) * 1024 + dt * 128; up = ws + WS_O + (size_t)st * 32 * 2048 + j * 2048 - 2048; wstr = 64 * 1024;
        vw0 = (lane >> 3) * 1024 + swz; vw1 = vw0 + 8 * 1024 - 1024;
      }
      dma_wh(m0b + so, w2p, vw0, vw1); dma_fh(m0b + so, up, vl);
      w2p += wstr; up += 8192; NEXT_SO(); ++ci;
    };
    VBAR(12);
    VBAR(10); fill_g2(); VBAR(12); fill_g2(); VBAR(14); fill_g2(); VBAR(16); fill_g2(); VBAR(18); fill_g2();
#pragma unroll 1
    for (int s = 6; s < 24; ++s) { VBAR(20); fill_g2(); }
    VBAR(20); VBAR(16); VBAR(12); VBAR(8); VBAR(4); VBAR(0);
    BAR(); BAR();
    if (b == 0 && j == 0) {
#pragma unroll 1
      for (int k = 0; k < 4; ++k) spin1(rws, WS_FL + FL_O * 4 + (k * 64 + lane) * 128, token);
      if (lane == 0) st32(rws, WS_FL + FL_EPOCH * 4, token);
    }
    STAMP(4);
    if (j == 0) STAMP_OUT(ws, 1);
    return;
  }

#undef token
  unsigned token;
  const int r = lane & 31, h = lane >> 5;
  const int g4 = lane >> 4, q4 = (lane & 15) >> 2, p4 = lane & 3, blk = (lane >> 4) & 1;
  int so = 0;
  if (threadIdx.x == 4) *relay = 0u;
  if (threadIdx.x < 4) {
    const unsigned cv = (threadIdx.x & 1) ? 0u : 0x3f803f80u;
    *(LAS u32x4*)(L + L_C + (threadIdx.x >> 1) * 512 + (threadIdx.x & 1) * 16) = u32x4{cv, cv, cv, cv};
  }
  lptr pk = L + L_P + lane * 16;

  if (hasQ) {
    token = TOKEN_OF(epoch);
    asm volatile("" : "+v"(token));
    if (w == 7) *(LAS unsigned*)pk = ~token;
    STAMP(1);
    f32x16 acc[2] = {};
    const bool poller = w == 7 && hasG1;
    const unsigned post = poller ? (1u << 8) | (1u << 10) | (1u << 12) : 0u;
    auto hook = [&](int s) {
      if (s == 14 && poller) peek_check(pk, rws, fh_off, token);
      LBAR();
      if (RARE((post >> s) & 1)) peek_issue(lds0 + L_P, ws, fh_off);
      SB();
    };
    if (tn >= 8) qkv_consume<true>(L, so, w, lane, acc, hook);
    else qkv_consume<false>(L, so, w, lane, acc, hook);
    STAMP(2);
    const int wm = w >> 2, wn = w & 3;
    const int hd0 = (tn & 3) * 8 + wn * 2;
#pragma unroll
    for (int mi = 0; mi < 2; ++mi) {
      const int tb = tm * 4 + wm * 2 + mi;
      const float sc = tn < 4 ? QSCALE : 1.0f;
      const unsigned dst = tn >= 8 ? WS_V + (((hd0 + (r >> 4)) * 64 + tb) * 64 + h * 16 + (r & 15)) * 16
                                   : (tn < 4 ? WS_Q : WS_K) + ((hd0 * 64 + tb) * 64 + lane) * 16;
      const unsigned dstep = tn >= 8 ? 512 : 65536;
#pragma unroll
      for (int s = 0; s < 2; ++s) {
        u32x4 v;
#pragma unroll
        for (int jj = 0; jj < 4; ++jj) v[jj] = pk2(acc[mi][8 * s + 2 * jj] * sc, acc[mi][8 * s + 2 * jj + 1] * sc);
        st128(rws, dst + s * dstep, v);
      }
    }
  } else {
    constexpr int RS = 1056;
    u32x4 xv[4];
#pragma unroll
    for (int i = 0; i < 4; ++i) xv[i] = *(const u32x4*)(x + (size_t)(lnb * 32 + w * 4 + i) * 1024 + lane * 16);
    const u32x4 gv = *(const u32x4*)(gw + lane * 16);
    float gsel = 0.f;
#pragma unroll
    for (int i = 0; i < 4; ++i) {
      float v[8], s = 0.f, gs = 0.f;
#pragma unroll
      for (int jj = 0; jj < 4; ++jj) {
        v[2 * jj] = bflo(xv[i][jj]); v[2 * jj + 1] = bfhi(xv[i][jj]);
        s += v[2 * jj] + v[2 * jj + 1];
        gs += v[2 * jj] * bflo(gv[jj]) + v[2 * jj + 1] * bfhi(gv[jj]);
      }
      s = wave_sum(s); gs = wave_sum(gs);
      const float m = s * (1.0f / 512);
      float qq = 0.f;
#pragma unroll
      for (int jj = 0; jj < 8; ++jj) { v[jj] -= m; qq += v[jj] * v[jj]; }
      qq = wave_sum(qq);
      const float rs = __builtin_amdgcn_rsqf(qq * (1.0f / 512) + 1e-5f);
      u32x4 o;
#pragma unroll
      for (int jj = 0; jj < 4; ++jj) o[jj] = pk2(v[2 * jj] * rs, v[2 * jj + 1] * rs);
      *(LAS u32x4*)(L + (w * 4 + i) * RS + lane * 16) = o;
      gsel = lane == i ? gs : gsel;
    }
    token = TOKEN_OF(epoch);
    asm volatile("" : "+v"(token));
    if (lane < 4) st32(rws, WS_G + (lnb * 32 + w * 4 + lane) * 4, __float_as_uint(1.0f / (1.0f + __expf(-gsel))));
    STAMP(1);
    LBAR();
#pragma unroll
    for (int i = 0; i < 4; ++i) {
      const int D = w * 4 + i, d48 = D / 3;
      lptr a = L + (4 * g4 + q4) * RS + D * 32 + p4 * 8;
      const bf16x8 f = cat8(ldstr(a), ldstr(a + 16 * RS));
      st128(rws, WS_H + ((d48 * 64 + lnb) * 3 + (D - d48 * 3)) * 1024 + lane * 16, __builtin_bit_cast(u32x4, f));
    }
    DBAR();
    if (threadIdx.x == 0) flag_st(ws, FL_H + lnb * 32, token);
    STAMP(2);
    LBAR();
  }

  bf16x8 qf;
  if (hasG1) {
    const int kh = w >> 2, fb = w & 3;
    int ha[3];
#pragma unroll
    for (int db = 0; db < 3; ++db) { const int o = (kh * 3 + db) * 1024 + lane * 16, rg = o / 1536; ha[db] = rg * 4096 + 2048 + (o - rg * 1536); }
    const int wa = kh * 8192 + (4 * g4 + q4) * 128 + ((fb ^ ((2 * g4 + (q4 >> 1)) & 3)) << 5) + p4 * 8;
    f32x4 acc[3] = {};
    bf16x8 wA, hA[3], wB, hB[3];
#define G1RD(W, H) do { lptr sl = L + so; NEXT_SO(); W = cat8(ldstr(sl + wa), ldstr(sl + wa + 4096)); \
    _Pragma("unroll") for (int db = 0; db < 3; ++db) H[db] = ldsr128(sl + ha[db]); SB(); } while (0)
#define G1MM(W, H) do { _Pragma("unroll") for (int db = 0; db < 3; ++db) acc[db] = MFMA16(W, H[db], acc[db]); SB(); } while (0)
    const unsigned pre = (1u << 6) | (w == 7 ? 1u << 18 : 0u);
    const unsigned post = (w == 0 && hasQ ? 1u << 6 : 0u) | (w == 7 ? (1u << 6) | (1u << 12) | (1u << 14) | (1u << 16) : 0u);
    auto hook = [&](int s) {
      if (RARE((pre >> s) & 1)) { if (s == 6) asm volatile("s_waitcnt vmcnt(0)" ::: "memory"); else peek_check(pk, rws, fq_off, token); }
      LBAR();
      if (RARE((post >> s) & 1)) {
        if (s == 6) { if (w == 7) *(LAS unsigned*)pk = ~token; else if (lane == 0) flag_st(ws, FL_Q + (tn * 16 + tm) * 32, token); }
        else peek_issue(lds0 + L_P, ws, fq_off);
      }
      SB();
    };
    LBAR(); G1RD(wA, hA);
#pragma unroll 1
    for (int s = 2; s < 20; s += 2) {
      LBAR();  G1RD(wB, hB); G1MM(wA, hA);
      hook(s); G1RD(wA, hA); G1MM(wB, hB);
    }
    STAMP(10);
    qf = __builtin_bit_cast(bf16x8, ld128(rws, WS_Q + (head * 64 + qb * 8 + w) * 1024 + lane * 16));
    const int fblk = g1ft * 4 + fb;
    u32x2 bb = *(const u32x2*)(b1 + min(fblk * 16 + 4 * g4, 1372) * 2);
#pragma unroll 1
    for (int s = 20; s < 32; s += 2) {
      LBAR(); G1RD(wB, hB); G1MM(wA, hA);
      LBAR(); G1RD(wA, hA); G1MM(wB, hB);
    }
    asm volatile("" : "+v"(qf), "+v"(bb));
    LBAR(); G1RD(wB, hB); G1MM(wA, hA);
    LBAR();
    G1MM(wB, hB);
    STAMP(3);
    if (kh == 1) { *(LAS f32x4*)(L + L_X + ((fb * 3 + 0) * 64 + lane) * 16) = acc[0]; *(LAS f32x4*)(L + L_X + ((fb * 3 + 1) * 64 + lane) * 16) = acc[1]; }
    else *(LAS f32x4*)(L + L_X + ((fb * 3 + 2) * 64 + lane) * 16) = acc[2];
    LBAR();
    if (fblk < 86) {
#pragma unroll
      for (int db = 0; db < 3; ++db) {
        const int D = g1dt * 3 + db;
        if ((db == 2) == (kh == 1) && D < 32) {
          const f32x4 o = *(const LAS f32x4*)(L + L_X + ((fb * 3 + db) * 64 + lane) * 16);
          const float u0 = gelu_tanh(acc[db][0] + o[0] + bflo(bb[0])), u1 = gelu_tanh(acc[db][1] + o[1] + bfhi(bb[0]));
          const float u2 = gelu_tanh(acc[db][2] + o[2] + bflo(bb[1])), u3 = gelu_tanh(acc[db][3] + o[3] + bfhi(bb[1]));
          const u32x2 v = {pk2(u0, u1), pk2(u2, u3)};
          st64(rws, WS_U + ((((D >> 2) * 86 + fblk) * 2 + ((D >> 1) & 1)) * 64 + (g4 >> 1) * 32 + (D & 1) * 16 + (lane & 15)) * 16 + (g4 & 1) * 8, v);
        }
      }
    }
  } else {
    DBAR();
    if (threadIdx.x == 0) flag_st(ws, FL_Q + (tn * 16 + tm) * 32, token);
    LBAR();
    qf = __builtin_bit_cast(bf16x8, ld128(rws, WS_Q + (head * 64 + qb * 8 + w) * 1024 + lane * 16));
    asm volatile("" : "+v"(qf));
    LBAR();
    STAMP(3);
  }

  {
    lptr kp = L + L_K + w * 1024 + lane * 16;
    const int i_lo = qb > 0 ? 1 : 8 - w;
    const f32x16 zero = {};
    f32x16 sb;
    {
      const f32x16 sd = MFMA32(ldsr128(kp + 8 * 1024), qf, zero);
      const f32x16 sf = MFMA32(ldsr128(kp), qf, zero);
#pragma unroll
      for (int i = 0; i < 16; ++i) sb[i] = (crow(i, h) <= r) ? sd[i] : (qb > 0 ? sf[i] : -INFINITY);
    }
    float m = max16(sb, -INFINITY);
    if (qb > 0) {
      float m2 = -INFINITY;
#pragma unroll
      for (int i = 1; i < 8; ++i) {
        const f32x16 s = MFMA32(ldsr128(kp + i * 1024), qf, zero);
        if (i & 1) m2 = max16(s, m2); else m = max16(s, m);
      }
      m = __builtin_fmaxf(m, m2);
    } else {
#pragma unroll 1
      for (int i = i_lo; i < 8; ++i) {
        const f32x16 s = MFMA32(ldsr128(kp + i * 1024), qf, zero);
        m = max16(s, m);
      }
    }
    {
      auto rr = __builtin_amdgcn_permlane32_swap(__float_as_uint(m), __float_as_uint(m), false, false);
      m = __builtin_fmaxf(__uint_as_float(rr[0]), __uint_as_float(rr[1]));
    }
    STAMP(11);
    const int vstr = r < 16 ? 1024 : 0;
    lptr vp0 = L + (r < 16 ? L_V + w * 1024 + h * 256 + r * 16 : (L_C + ((r == 16 || r == 20) ? 0 : 16)));
    f32x16 o = {};
    {
      u32x4 pd[2], pf[2];
#pragma unroll
      for (int i = 0; i < 16; i += 2) {
        const float p0 = __builtin_amdgcn_exp2f(sb[i] - m), p1 = __builtin_amdgcn_exp2f(sb[i + 1] - m);
        const bool v0 = crow(i, h) <= r, v1 = crow(i + 1, h) <= r;
        pd[i >> 3][(i >> 1) & 3] = pk2(v0 ? p0 : 0.f, v1 ? p1 : 0.f);
        pf[i >> 3][(i >> 1) & 3] = pk2(v0 ? 0.f : p0, v1 ? 0.f : p1);
      }
      lptr vp = vp0 + 8 * vstr;
      o = MFMA32(ldsr128(vp), __builtin_bit_cast(bf16x8, pd[0]), o);
      o = MFMA32(ldsr128(vp + 512), __builtin_bit_cast(bf16x8, pd[1]), o);
      if (qb > 0) {
        o = MFMA32(ldsr128(vp0), __builtin_bit_cast(bf16x8, pf[0]), o);
        o = MFMA32(ldsr128(vp0 + 512), __builtin_bit_cast(bf16x8, pf[1]), o);
      }
    }
    if (hasG1) {
      DBAR();
      if (threadIdx.x == 0) flag_st(ws, FL_U + (g1dt * 32 + g1ft) * 32, token);
    }
    STAMP(4);
    f32x16 cin;
#pragma unroll
    for (int i = 0; i < 16; ++i) cin[i] = -m;
    if (i_lo < 8) {
      f32x16 s = MFMA32(ldsr128(kp + i_lo * 1024), qf, cin);
#pragma unroll 1
      for (int i = i_lo; i < 8; ++i) {
        lptr vp = vp0 + i * vstr;
        const bf16x8 kn = ldsr128(kp + (i + 1) * 1024), v0 = ldsr128(vp), v1 = ldsr128(vp + 512);
        u32x4 p0, p1;
#pragma unroll
        for (int e = 0; e < 8; e += 2) p0[e >> 1] = pk2(__builtin_amdgcn_exp2f(s[e]), __builtin_amdgcn_exp2f(s[e + 1]));
        o = MFMA32(v0, __builtin_bit_cast(bf16x8, p0), o);
#pragma unroll
        for (int e = 8; e < 16; e += 2) p1[(e >> 1) & 3] = pk2(__builtin_amdgcn_exp2f(s[e]), __builtin_amdgcn_exp2f(s[e + 1]));
        s = MFMA32(kn, qf, cin);
        o = MFMA32(v1, __builtin_bit_cast(bf16x8, p1), o);
      }
    }
    const float il = 1.0f / o[8];
    u32x4 ov;
#pragma unroll
    for (int jj = 0; jj < 4; ++jj) ov[jj] = pk2(o[2 * jj] * il, o[2 * jj + 1] * il);
    const int B = qb * 8 + w;
    st128(rws, WS_O + (((B >> 1) * 32 + head) * 2 + (B & 1)) * 1024 + lane * 16, ov);
  }
  STAMP(5);

  {
    const int kh = w >> 2, mt = (w >> 1) & 1, nt = w & 1;
    const int tok = st * 64 + nt * 32 + r;
    const int ta2 = kh * 8192 + (8 * h + q4) * 128 + (((nt * 4 + blk * 2 + (p4 >> 1)) ^ ((q4 >> 1) << 2)) << 4) + (p4 & 1) * 8;
    const int tao = kh * 8192 + (4 * h + q4) * 128 + (((mt * 4 + blk * 2 + (p4 >> 1)) ^ ((q4 >> 1) << 2)) << 4) + (p4 & 1) * 8;
    const int fa2 = kh * 8192 + 2048 + mt * 1024 + lane * 16;
    const int fao = kh * 8192 + 2048 + nt * 1024 + lane * 16;
    f32x16 ay = {}, aa = {};
    bf16x8 pa0, pb0, pa1, pb1, qa0, qb0, qa1, qb1;
    float g = 0.f; unsigned b2v = 0;
#define RD2(A0, B0, A1, B1) do { lptr sl = L + so; NEXT_SO(); B0 = cat8(ldstr(sl + ta2), ldstr(sl + ta2 + 512)); A0 = ldsr128(sl + fa2); \
    B1 = cat8(ldstr(sl + ta2 + 4096), ldstr(sl + ta2 + 4096 + 512)); A1 = ldsr128(sl + fa2 + 4096); SB(); } while (0)
#define RDO(A0, B0, A1, B1) do { lptr sl = L + so; NEXT_SO(); A0 = cat8(ldstr(sl + tao), ldstr(sl + tao + 1024)); B0 = ldsr128(sl + fao); \
    A1 = cat8(ldstr(sl + tao + 4096), ldstr(sl + tao + 4096 + 1024)); B1 = ldsr128(sl + fao + 4096); SB(); } while (0)
#define MM2(ACC, A0, B0, A1, B1) do { ACC = MFMA32(A0, B0, ACC); ACC = MFMA32(A1, B1, ACC); SB(); } while (0)
    const unsigned pre = (1u << 4) | (w == 7 ? 1u << 16 : 0u);
    const unsigned post = (w == 0 ? 1u << 4 : 0u) | (w == 7 ? (1u << 4) | (1u << 10) | (1u << 12) | (1u << 14) : 0u);
    auto hook = [&](int s) {
      if (RARE((pre >> s) & 1)) { if (s == 4) asm volatile("s_waitcnt vmcnt(0)" ::: "memory"); else peek_check(pk, rws, fo_off, token); }
      LBAR();
      if (RARE((post >> s) & 1)) {
        if (s == 4) { if (w == 7) *(LAS unsigned*)pk = ~token; else if (lane == 0) flag_st(ws, FL_O + (qb * 32 + head) * 32, token); }
        else peek_issue(lds0 + L_P, ws, fo_off);
      }
      SB();
    };
    LBAR(); STAMP(6);
    RD2(pa0, pb0, pa1, pb1);
    if (kh) { pa0 = bf16x8{}; pa1 = bf16x8{}; pb0 = bf16x8{}; pb1 = bf16x8{}; }
#pragma unroll 1
    for (int s = 2; s < 22; s += 2) {
      LBAR();  RD2(qa0, qb0, qa1, qb1); MM2(ay, pa0, pb0, pa1, pb1);
      hook(s); RD2(pa0, pb0, pa1, pb1); MM2(ay, qa0, qb0, qa1, qb1);
    }
    g = __uint_as_float(ld32(rws, WS_G + tok * 4));
    b2v = ((const unsigned short*)b2)[tok];
    LBAR(); RD2(qa0, qb0, qa1, qb1); MM2(ay, pa0, pb0, pa1, pb1);
    STAMP(7);
    LBAR(); RDO(pa0, pb0, pa1, pb1); MM2(ay, qa0, qb0, qa1, qb1);
#pragma unroll 1
    for (int s = 23; s < 29; s += 2) {
      LBAR(); RDO(qa0, qb0, qa1, qb1); MM2(aa, pa0, pb0, pa1, pb1);
      LBAR(); RDO(pa0, pb0, pa1, pb1); MM2(aa, qa0, qb0, qa1, qb1);
    }
    LBAR(); RDO(qa0, qb0, qa1, qb1); MM2(aa, pa0, pb0, pa1, pb1);
    MM2(aa, qa0, qb0, qa1, qb1);
    STAMP(8);
    const float og = 1.0f - g;
    float v[16];
#pragma unroll
    for (int i = 0; i < 16; ++i) v[i] = ay[i] * og + aa[i] * g;
    LBAR();
    {
      lptr ex = L + w * 2048 + lane * 16;
      f32x4 s0, s1;
#pragma unroll
      for (int jj = 0; jj < 4; ++jj) { s0[jj] = kh ? v[jj] : v[8 + jj]; s1[jj] = kh ? v[4 + jj] : v[12 + jj]; }
      *(LAS f32x4*)(ex) = s0; *(LAS f32x4*)(ex + 1024) = s1;
    }
    LBAR();
    {
      lptr ex = L + (w ^ 4) * 2048 + lane * 16;
      const f32x4 r0 = *(const LAS f32x4*)(ex), r1 = *(const LAS f32x4*)(ex + 1024);
      const float bo = bflo(b2v) * og;
      float f[8];
#pragma unroll
      for (int jj = 0; jj < 4; ++jj) { f[jj] = (kh ? v[8 + jj] : v[jj]) + r0[jj] + bo; f[4 + jj] = (kh ? v[12 + jj] : v[4 + jj]) + r1[jj] + bo; }
      const unsigned a0 = pk2(f[0], f[1]), a1 = pk2(f[2], f[3]), c0 = pk2(f[4], f[5]), c1 = pk2(f[6], f[7]);
      auto x0 = __builtin_amdgcn_permlane32_swap(a0, c0, false, false);
      auto x1 = __builtin_amdgcn_permlane32_swap(a1, c1, false, false);
      const u32x4 ov = {x0[0], x1[0], x0[1], x1[1]};
      *(u32x4*)(out + (size_t)tok * 1024 + (dt * 64 + mt * 32 + kh * 16 + h * 8) * 2) = ov;
    }
  }
  STAMP(9);
  if (w == 0) STAMP_OUT(ws, 0);
}

extern "C" void kernel_launch(void* const* d_in, const int* in_sizes, int n_in, void* d_out, int out_size, void* d_ws, size_t ws_size,
                              hipStream_t stream) {
  (void)hipFuncSetAttribute((const void*)fused_kernel, hipFuncAttributeMaxDynamicSharedMemorySize, LDS_BYTES);
  fused_kernel<<<256, 768, LDS_BYTES, stream>>>((const char*)d_in[0], (const char*)d_in[1], (const char*)d_in[2], (const char*)d_in[4],
                                                (const char*)d_in[5], (const char*)d_in[6], (const char*)d_in[7], (const char*)d_in[8],
                                                (char*)d_ws, (char*)d_out);
}
```

```cpp
#include <hip/hip_runtime.h>
#include <hip/hip_bf16.h>
#include <stdint.h>

#define LAS __attribute__((address_space(3)))
#define GAS __attribute__((address_space(1)))
#define DI __device__ __forceinline__
typedef __attribute__((ext_vector_type(8))) __bf16 bf16x8;
typedef __attribute__((ext_vector_type(2))) __bf16 bf16x2;
typedef __attribute__((ext_vector_type(4))) short s16x4;
typedef __attribute__((ext_vector_type(2))) float f32x2;
typedef __attribute__((ext_vector_type(4))) float f32x4;
typedef __attribute__((ext_vector_type(16))) float f32x16;
typedef __attribute__((ext_vector_type(2))) unsigned u32x2;
typedef __attribute__((ext_vector_type(4))) unsigned u32x4;
typedef LAS char* lptr;
typedef __amdgpu_buffer_rsrc_t rsrc_t;

constexpr unsigned WS_Q = 0, WS_K = 2u << 20, WS_V = 4u << 20, WS_O = 6u << 20, WS_U = 8u << 20, WS_H = 10u << 20,
                   WS_G = 13u << 20, WS_FL = 14u << 20, WS_END = 15u << 20;
constexpr int FL_H = 0;
constexpr int FL_Q = 64 * 32;
constexpr int FL_U = FL_Q + 192 * 32;
constexpr int FL_O = FL_U + 352 * 32;
constexpr int FL_EPOCH = FL_O + 256 * 32;
constexpr float QSCALE = 0.25f * 1.4426950408889634f;
constexpr int NS = 7, SLOT = 16384, RING = NS * SLOT;
constexpr int L_K = RING, L_V = RING + 16384, L_X = RING + 32768  , L_C = L_X + 12288  , L_P = L_C + 1024  ;
constexpr int LDS_BYTES = 163840;
constexpr unsigned SPIN_CAP = 200000;
#define CODEPF 3
#define PREFETCH 0
#define QROT 1

#define MFMA32(a, b, c) __builtin_amdgcn_mfma_f32_32x32x16_bf16(a, b, c, 0, 0, 0)
#define MFMA16(a, b, c) __builtin_amdgcn_mfma_f32_16x16x32_bf16(a, b, c, 0, 0, 0)
#define SB() __builtin_amdgcn_sched_barrier(0)
#define LBAR() do { asm volatile("s_waitcnt lgkmcnt(0)\n\ts_barrier" ::: "memory"); SB(); } while (0)
#define DBAR() do { asm volatile("s_waitcnt vmcnt(0) lgkmcnt(0)\n\ts_barrier" ::: "memory"); SB(); } while (0)
#define VBAR(N) asm volatile("s_waitcnt vmcnt(" #N ")\n\ts_barrier" ::: "memory")
#define BAR() asm volatile("s_barrier" ::: "memory")

#define STAMP(i) do {} while (0)
#define STAMP_OUT(ws, slot) do {} while (0)

DI int rfl(int x) { return __builtin_amdgcn_readfirstlane(x); }
DI bf16x8 ldsr128(lptr p) { return *(const LAS bf16x8*)p; }
DI s16x4 ldstr(lptr p) { return __builtin_amdgcn_ds_read_tr16_b64_v4i16((LAS s16x4*)p); }
DI bf16x8 cat8(s16x4 a, s16x4 b) { return __builtin_bit_cast(bf16x8, __builtin_shufflevector(a, b, 0, 1, 2, 3, 4, 5, 6, 7)); }
DI unsigned pk2(float a, float b) { f32x2 f = {a, b}; return __builtin_bit_cast(unsigned, __builtin_convertvector(f, bf16x2)); }
DI float bflo(unsigned u) { return __uint_as_float(u << 16); }
DI float bfhi(unsigned u) { return __uint_as_float(u & 0xffff0000u); }
DI int crow(int i, int h) { return (i & 3) + 8 * (i >> 2) + 4 * h; }
DI void st128(rsrc_t r, unsigned off, u32x4 v) { __builtin_amdgcn_raw_buffer_store_b128(v, r, off, 0, 16); }
DI void st64(rsrc_t r, unsigned off, u32x2 v) { __builtin_amdgcn_raw_buffer_store_b64(v, r, off, 0, 16); }
DI void st32(rsrc_t r, unsigned off, unsigned v) { __builtin_amdgcn_raw_buffer_store_b32(v, r, off, 0, 16); }
DI u32x4 ld128(rsrc_t r, unsigned off) { return __builtin_amdgcn_raw_buffer_load_b128(r, off, 0, 16); }
DI unsigned ld32(rsrc_t r, unsigned off) { return __builtin_amdgcn_raw_buffer_load_b32(r, off, 0, 16); }
DI unsigned flag_ld(const char* ws, int idx) { return __hip_atomic_load((const GAS unsigned*)(ws + WS_FL) + idx, __ATOMIC_RELAXED, __HIP_MEMORY_SCOPE_AGENT); }
DI void flag_st(char* ws, int idx, unsigned v) { __hip_atomic_store((GAS unsigned*)(ws + WS_FL) + idx, v, __ATOMIC_RELAXED, __HIP_MEMORY_SCOPE_AGENT); }

#define M0_IN "s_mov_b32 %0, m0\n\ts_mov_b32 m0, %1\n\ts_nop 0\n\t"
#define M0_OUT "s_mov_b32 m0, %0"
DI void dma_2x2(unsigned m0v, const void* b0, unsigned v0, unsigned v1, const void* b1, unsigned v2, unsigned v3) {
  unsigned sv;
  asm volatile(M0_IN "global_load_lds_dwordx4 %4, %2\n\tglobal_load_lds_dwordx4 %5, %2 offset:1024\n\t"
               "global_load_lds_dwordx4 %6, %3 offset:2048\n\tglobal_load_lds_dwordx4 %7, %3 offset:3072\n\t" M0_OUT
               : "=&s"(sv) : "s"(m0v), "s"(b0), "s"(b1), "v"(v0), "v"(v1), "v"(v2), "v"(v3) : "memory");
}
DI void dma_lin4_sc1(unsigned m0v, const void* b, unsigned vl) {
  unsigned sv;
  asm volatile(M0_IN "global_load_lds_dwordx4 %3, %2 sc1\n\tglobal_load_lds_dwordx4 %3, %2 offset:1024 sc1\n\t"
               "global_load_lds_dwordx4 %3, %2 offset:2048 sc1\n\tglobal_load_lds_dwordx4 %3, %2 offset:3072 sc1\n\t" M0_OUT
               : "=&s"(sv) : "s"(m0v), "s"(b), "v"(vl) : "memory");
}
DI void dma_g1(unsigned m0v, const void* wb, unsigned v0, unsigned v1, const void* hb_m2048, unsigned vl) {
  unsigned sv;
  asm volatile(M0_IN "global_load_lds_dwordx4 %4, %2\n\tglobal_load_lds_dwordx4 %5, %2 offset:1024\n\t"
               "global_load_lds_dwordx4 %6, %3 offset:2048 sc1\n\t"
               "s_mov_b32 exec_hi, 0\n\tglobal_load_lds_dwordx4 %6, %3 offset:3072 sc1\n\ts_mov_b32 exec_hi, -1\n\t" M0_OUT
               : "=&s"(sv) : "s"(m0v), "s"(wb), "s"(hb_m2048), "v"(v0), "v"(v1), "v"(vl) : "memory");
}
DI void dma_hh(unsigned m0v, const void* hb_m2048, unsigned vl) {
  unsigned sv;
  asm volatile(M0_IN "global_load_lds_dwordx4 %3, %2 offset:2048 sc1\n\t"
               "s_mov_b32 exec_hi, 0\n\tglobal_load_lds_dwordx4 %3, %2 offset:3072 sc1\n\ts_mov_b32 exec_hi, -1\n\t" M0_OUT
               : "=&s"(sv) : "s"(m0v), "s"(hb_m2048), "v"(vl) : "memory");
}
DI void dma_lin4(unsigned m0v, const void* b, unsigned vl) {
  unsigned sv;
  asm volatile(M0_IN "global_load_lds_dwordx4 %3, %2\n\tglobal_load_lds_dwordx4 %3, %2 offset:1024\n\t"
               "global_load_lds_dwordx4 %3, %2 offset:2048\n\tglobal_load_lds_dwordx4 %3, %2 offset:3072\n\t" M0_OUT
               : "=&s"(sv) : "s"(m0v), "s"(b), "v"(vl) : "memory");
}
DI void dma_touch(unsigned m0v, const void* b, unsigned voff) {
  unsigned sv;
  asm volatile(M0_IN "global_load_lds_dword %3, %2\n\t" M0_OUT : "=&s"(sv) : "s"(m0v), "s"(b), "v"(voff) : "memory");
}
DI void dma_wh(unsigned m0v, const void* wb, unsigned v0, unsigned v1) {
  unsigned sv;
  asm volatile(M0_IN "global_load_lds_dwordx4 %3, %2\n\tglobal_load_lds_dwordx4 %4, %2 offset:1024\n\t" M0_OUT
               : "=&s"(sv) : "s"(m0v), "s"(wb), "v"(v0), "v"(v1) : "memory");
}
DI void dma_fh(unsigned m0v, const void* fb_m2048, unsigned vl) {
  unsigned sv;
  asm volatile(M0_IN "global_load_lds_dwordx4 %3, %2 offset:2048 sc1\n\tglobal_load_lds_dwordx4 %3, %2 offset:3072 sc1\n\t" M0_OUT
               : "=&s"(sv) : "s"(m0v), "s"(fb_m2048), "v"(vl) : "memory");
}

DI void spin1(rsrc_t r, unsigned off, unsigned tok) {
  unsigned n = 0;
#pragma nounroll
  do { const unsigned v = ld32(r, off); if (__all(v == tok)) break; __builtin_amdgcn_s_sleep(4); asm volatile("" ::: "memory"); } while (++n < SPIN_CAP);
  __builtin_amdgcn_fence(__ATOMIC_ACQUIRE, "wavefront");
}
DI void peek_issue(unsigned m0v, const void* ws, unsigned off) {
  unsigned sv;
  asm volatile(M0_IN "global_load_lds_dwordx4 %3, %2 sc1\n\t" M0_OUT : "=&s"(sv) : "s"(m0v), "s"(ws), "v"(off) : "memory");
}
DI void peek_check(lptr p, rsrc_t r, unsigned off, unsigned tok) {
  if (__all(*(const volatile LAS unsigned*)p == tok)) return;
  asm volatile("s_waitcnt vmcnt(0)" ::: "memory");
  if (__all(*(const volatile LAS unsigned*)p == tok)) return;
  spin1(r, off, tok);
}
#define RARE(c) __builtin_expect((c) != 0, 0)

DI float wave_sum(float v) {
#define DPPADD(ctrl, rm) v += __int_as_float(__builtin_amdgcn_update_dpp(0, __float_as_int(v), ctrl, rm, 0xf, false))
  DPPADD(0x111, 0xf); DPPADD(0x112, 0xf); DPPADD(0x114, 0xf); DPPADD(0x118, 0xf); DPPADD(0x142, 0xa); DPPADD(0x143, 0xc);
#undef DPPADD
  return __int_as_float(__builtin_amdgcn_readlane(__float_as_int(v), 63));
}
DI float gelu_tanh(float u) {
  const float z = u * (0.7978845608028654f + 0.7978845608028654f * 0.044715f * u * u);
  const float e = __builtin_amdgcn_exp2f(z * (-2.0f * 1.4426950408889634f));
  return u * __builtin_amdgcn_rcpf(1.0f + e);
}
DI float max16(const f32x16& s, float m) {
#pragma unroll
  for (int i = 0; i < 16; i += 2) m = __builtin_fmaxf(__builtin_fmaxf(s[i], s[i + 1]), m);
  return m;
}

#define NEXT_SO() so = so == (NS - 1) * SLOT ? 0 : so + SLOT

template <bool ISV, class Hook>
DI void qkv_consume(lptr L, int& so, int w, int lane, f32x16 (&acc)[2], Hook hook) {
  const int r = lane & 31, h = lane >> 5, wm = w >> 2, wn = w & 3;
  int xa[4];
#pragma unroll
  for (int s = 0; s < 4; ++s) xa[s] = (wm * 2 + (r >> 4)) * 4096 + (r & 15) * 128 + (((2 * s + h) ^ ((r >> 1) & 7)) << 4);
  const int q = (lane & 15) >> 2, p = lane & 3, blk = (lane >> 4) & 1;
  const int wa = h * 4096 + 2048 + q * 256 + (((wn * 4 + blk * 2 + (p >> 1)) ^ (q << 2)) << 4) + (p & 1) * 8;
  bf16x8 xA[2][4], xB[2][4], wA[4], wB[4];
#define QM(X, W, mi, s) do { if (ISV) acc[mi] = MFMA32(X[mi][s], W[s], acc[mi]); else acc[mi] = MFMA32(W[s], X[mi][s], acc[mi]); SB(); } while (0)
#define QRX(X, mi, s) X[mi][s] = ldsr128(sl + xa[s])
#define QRW(W, mi, e) W[2 * mi + e] = cat8(ldstr(sl + wa + e * 8192), ldstr(sl + wa + e * 8192 + 1024))
#define QSTEP(mi, XN, WN, XP, WP, MM) do { lptr sl = L + so; NEXT_SO(); \
    QRX(XN, mi, 0); QRX(XN, mi, 1); QRX(XN, mi, 2); SB(); if (MM) QM(XP, WP, 0, 2 * mi); \
    QRX(XN, mi, 3); QRW(WN, mi, 0); SB(); if (MM) QM(XP, WP, 1, 2 * mi); \
    QRW(WN, mi, 1); SB(); if (MM) { QM(XP, WP, 0, 2 * mi + 1); QM(XP, WP, 1, 2 * mi + 1); } } while (0)
  LBAR(); QSTEP(0, xA, wA, xB, wB, false); LBAR(); QSTEP(1, xA, wA, xB, wB, false);
#pragma unroll 1
  for (int s = 2; s < 14; s += 4) {
    hook(s);     QSTEP(0, xB, wB, xA, wA, true); LBAR(); QSTEP(1, xB, wB, xA, wA, true);
    hook(s + 2); QSTEP(0, xA, wA, xB, wB, true); LBAR(); QSTEP(1, xA, wA, xB, wB, true);
  }
  hook(14); QSTEP(0, xB, wB, xA, wA, true); LBAR(); QSTEP(1, xB, wB, xA, wA, true);
#pragma unroll
  for (int s = 0; s < 4; ++s) { QM(xB, wB, 0, s); QM(xB, wB, 1, s); }
}

__global__ __launch_bounds__(768, 3) void fused_kernel(const char* __restrict__ x, const char* __restrict__ wqkv, const char* __restrict__ wo,
                                                       const char* __restrict__ w1, const char* __restrict__ b1, const char* __restrict__ w2,
                                                       const char* __restrict__ b2, const char* __restrict__ gw, char* __restrict__ ws,
                                                       char* __restrict__ out) {
  extern __shared__ __attribute__((aligned(16))) char smem[];
  const char* pc0;
  asm volatile("s_getpc_b64 %0" : "=s"(pc0));
  asm volatile("" :: "s"(x), "s"(wqkv), "s"(wo), "s"(w1), "s"(b1), "s"(w2), "s"(b2), "s"(gw), "s"(ws), "s"(out));
  STAMP(0);
  const int b = blockIdx.x, xcd = b & 7, idx = b >> 3;
  const int lane = threadIdx.x & 63, w = rfl(threadIdx.x >> 6);
  const unsigned lds0 = (unsigned)(uintptr_t)smem;
  lptr L = (lptr)smem;
  const int Lx = xcd * 31 + (31 - idx);
  const bool hasQ = idx < 24, hasG1 = idx >= 1 && Lx < 242;
  const int tm = (xcd >> 1) * 4 + idx / 6, tn = (xcd & 1) * 6 + idx % 6;
  const int lnb = xcd * 8 + (idx - 24);
  const int g1ft = Lx < 132 ? Lx / 6 : (Lx - 132) / 5, g1dt = Lx < 132 ? Lx % 6 : 6 + (Lx - 132) % 5;
  const int head = xcd * 4 + (idx & 3), qb = idx >> 2;
  const int st = (xcd >> 1) * 8 + (idx >> 2), dt = (xcd & 1) * 4 + (idx & 3);
  unsigned fq_off;
  { const int l = lane < 12 ? lane : 3, wh = l >> 2; int tmi = l & 3; if (wh == 0 || qb == 0) tmi |= 2;
    fq_off = WS_FL + (FL_Q + ((wh * 4 + (head >> 3)) * 16 + 2 * qb - 2 + tmi) * 32) * 4; }
  const unsigned fu_off = WS_FL + (FL_U + (((lane & 32) ? (4 * dt + 3) / 3 : (4 * dt) / 3) * 32 + min(lane & 31, 21)) * 32) * 4;
  const unsigned fo_off = WS_FL + (FL_O + ((st >> 2) * 32 + (lane & 31)) * 32) * 4;
  const unsigned fh_off = WS_FL + FL_H * 4 + lane * 128;
  const rsrc_t rws = __builtin_amdgcn_make_buffer_rsrc(ws, 0, WS_END, 0x00020000);
  volatile LAS unsigned* relay = (volatile LAS unsigned*)(L + L_C + 64);
  unsigned eoff = WS_FL + FL_EPOCH * 4;
  asm volatile("" : "+v"(eoff));
  const unsigned epoch = ld32(rws, eoff);
#define TOKEN_OF(e) ((e) * 1664525u + 1013904223u)

  if (w >= 8) {
    const int j = w - 8;
#define token TOKEN_OF(epoch)
    const unsigned m0b = lds0 + j * 4096;
    const unsigned vl = lane * 16;
    unsigned so = 0;
    const char* hp = ws + WS_H + (size_t)g1dt * 64 * 3072 + j * 1536 - 2048;
    const char* w1p = w1 + (size_t)(j * 16) * 2752 + g1ft * 128;
    const unsigned u0 = (lane >> 3) * 2752 + ((((lane & 7) ^ (((lane >> 4) & 3) << 1)) & (g1ft == 21 ? 3 : 7)) << 4), u1 = u0 + 8 * 2752 - 1024;
    auto fill_g1 = [&]() { dma_g1(m0b + so, w1p, u0, u1, hp, vl); hp += 6144; w1p += 64 * 2752; NEXT_SO(); };
    auto fill_g1w = [&]() { dma_wh(m0b + so, w1p, u0, u1); w1p += 64 * 2752; NEXT_SO(); };
    auto fill_g1h4 = [&](unsigned so0) {
      const unsigned keep = so; so = so0;
#pragma unroll 1
      for (int i = 0; i < 4; ++i) { dma_hh(m0b + so, hp, vl); hp += 6144; NEXT_SO(); }
      so = keep;
    };
    const unsigned swz = ((lane & 7) ^ (((lane >> 4) & 1) << 2)) << 4;
    unsigned vw0 = (lane >> 3) * 4096 + swz, vw1 = vw0 + 8 * 4096 - 1024;
    const char* w2p = w2 + (size_t)(j * 16) * 4096 + st * 128;
    const char* up = ws + WS_U + (size_t)dt * 86 * 2048 + j * 2048 - 2048;
    const size_t w2half = (size_t)(j < 2 ? 21 * 64 : 20 * 64) * 4096;
    const int uhalf = j < 2 ? 21 * 8192 : 20 * 8192;
    auto fill_kv = [&]() {
      int t0 = qb * 8 - 8 + 4 * j; t0 = t0 < 0 ? t0 + 8 : t0;
      dma_lin4_sc1(lds0 + L_K + j * 4096, ws + WS_K + (size_t)(head * 64 + t0) * 1024, vl);
      dma_lin4_sc1(lds0 + L_V + j * 4096, ws + WS_V + (size_t)(head * 64 + t0) * 1024, vl);
    };
    int nwh = 0;
    auto fill_wh = [&]() { dma_wh(m0b + so, nwh == 0 ? w2p + w2half : w2p + (size_t)(nwh - 1) * 64 * 4096, vw0, vw1); ++nwh; NEXT_SO(); };
    unsigned so_g2 = 0;

    if (hasQ) {
      const unsigned x0 = (lane >> 3) * 1024 + (((lane & 7) ^ (lane >> 4)) << 4), x1 = (8 + (lane >> 3)) * 1024 + (((lane & 7) ^ (4 + (lane >> 4))) << 4) - 1024;
      const unsigned q0 = (lane >> 4) * 3072 + (((lane & 15) ^ ((lane >> 4) << 2)) << 4), q1 = q0 + 4 * 3072 - 1024;
      const char* xp = x + (size_t)(tm * 128 + (j >> 1) * 64 + (j & 1) * 16) * 1024;
      const char* qp = wqkv + (size_t)(j * 8) * 3072 + tn * 256 - 2048;
      int kc = QROT ? ((idx / 6 + idx % 6) % 3) * 3 : 0;
      auto fill_a = [&]() { dma_2x2(m0b + so, xp + kc * 128, x0, x1, qp + (size_t)kc * 64 * 3072, q0, q1); NEXT_SO(); };
      auto fill_b = [&]() { dma_2x2(m0b + so, xp + kc * 128 + 32 * 1024, x0, x1, qp + (size_t)kc * 64 * 3072 + 32 * 3072, q0, q1); kc = (kc + 1) & 7; NEXT_SO(); };
#pragma unroll 1
      for (int c = 0; c < 3; ++c) { fill_a(); fill_b(); }
#pragma unroll 1
      for (int c = 0; c < 5; ++c) { VBAR(20); fill_a(); VBAR(20); fill_b(); }
      if (hasG1) {
        const unsigned so0 = so;
        VBAR(20); fill_g1w(); VBAR(18); fill_g1w(); VBAR(16); fill_g1w(); VBAR(14); fill_g1w();
        VBAR(12); fill_g1h4(so0); fill_g1();
        VBAR(20); fill_g1();
      } else {
        VBAR(20); VBAR(16); VBAR(12); VBAR(8); VBAR(4); VBAR(0);
      }
    } else {
      if (PREFETCH && j > 0) {
        __builtin_amdgcn_s_sleep(8);
        const int pw = lnb * 3 + (j - 1);
        const unsigned md = lds0 + L_K + j * 4096;
#pragma unroll 1
        for (int i = pw; i < 1376; i += 192) dma_lin4(md, w1 + (size_t)i * 4096, vl);
#pragma unroll 1
        for (int i = pw; i < 1376; i += 192) dma_lin4(md, w2 + (size_t)i * 4096, vl);
        if (pw < 128) dma_lin4(md, wo + (size_t)pw * 4096, vl);
        BAR(); BAR();
      } else {
        BAR();
        VBAR(0);
      }
#pragma unroll 1
      for (int s = 0; s < 4; ++s) fill_g1w();
      if (j == 0) spin1(rws, fh_off, token);
      BAR();
      fill_g1h4(0); fill_g1(); fill_g1();
    }
    STAMP(1);
    if (hasG1) {
      VBAR(14); fill_g1(); VBAR(16); fill_g1(); VBAR(18); fill_g1();
#pragma unroll 1
      for (int s = 3; s < 19; ++s) { VBAR(20); fill_g1(); }
      fill_kv();
#pragma unroll 1
      for (int s = 19; s < 26; ++s) { VBAR(20); fill_g1(); }
      so_g2 = so;
      VBAR(20); fill_wh(); VBAR(18); fill_wh(); VBAR(16); fill_wh(); VBAR(14); fill_wh(); VBAR(12); fill_wh(); VBAR(10); fill_wh();
      BAR(); fill_wh();
      BAR();
      BAR();
    } else {
      BAR();
      so_g2 = so;
#pragma unroll 1
      for (int i = 0; i < 7; ++i) fill_wh();
      if (j == 0) spin1(rws, fq_off, token);
      BAR();
      fill_kv();
      VBAR(0);
    }
    STAMP(2);
    {
      if (j == 0) { spin1(rws, fu_off, token); *relay = 1u; }
      else { for (unsigned spins = 0; spins < 64 * SPIN_CAP && *relay == 0u; ++spins) __builtin_amdgcn_s_sleep(1); }
      STAMP(3);
      so = so_g2;
      dma_fh(m0b + so, up + uhalf, vl); NEXT_SO();
#pragma unroll 1
      for (int i = 0; i < 6; ++i) { dma_fh(m0b + so, up, vl); up += 8192; NEXT_SO(); }
    }
    w2p += (size_t)6 * 64 * 4096;
    unsigned wstr = 64 * 4096;
    int ci = 7;
    auto fill_g2 = [&]() {
      if (ci == 22) {
        w2p = wo + (size_t)(j * 16) * 1024 + dt * 128; up = ws + WS_O + (size_t)st * 32 * 2048 + j * 2048 - 2048; wstr = 64 * 1024;
        vw0 = (lane >> 3) * 1024 + swz; vw1 = vw0 + 8 * 1024 - 1024;
      }
      dma_wh(m0b + so, w2p, vw0, vw1); dma_fh(m0b + so, up, vl);
      w2p += wstr; up += 8192; NEXT_SO(); ++ci;
    };
    VBAR(12);
    VBAR(10); fill_g2(); VBAR(12); fill_g2(); VBAR(14); fill_g2(); VBAR(16); fill_g2(); VBAR(18); fill_g2();
#pragma unroll 1
    for (int s = 6; s < 24; ++s) { VBAR(20); fill_g2(); }
    VBAR(20); VBAR(16); VBAR(12); VBAR(8); VBAR(4); VBAR(0);
    BAR(); BAR();
    if (b == 0 && j == 0) {
#pragma unroll 1
      for (int k = 0; k < 4; ++k) spin1(rws, WS_FL + FL_O * 4 + (k * 64 + lane) * 128, token);
      if (lane == 0) st32(rws, WS_FL + FL_EPOCH * 4, token);
    }
    STAMP(4);
    if (j == 0) STAMP_OUT(ws, 1);
    return;
  }

#undef token
  unsigned token;
  const int r = lane & 31, h = lane >> 5;
  const int g4 = lane >> 4, q4 = (lane & 15) >> 2, p4 = lane & 3, blk = (lane >> 4) & 1;
  int so = 0;
  if (CODEPF && (idx == 0 || idx == 12) && w < CODEPF) dma_touch(lds0 + L_P + 1024, pc0 + w * 8192, lane * 128);
  if (threadIdx.x == 4) *relay = 0u;
  if (threadIdx.x < 4) {
    const unsigned cv = (threadIdx.x & 1) ? 0u : 0x3f803f80u;
    *(LAS u32x4*)(L + L_C + (threadIdx.x >> 1) * 512 + (threadIdx.x & 1) * 16) = u32x4{cv, cv, cv, cv};
  }
  lptr pk = L + L_P + lane * 16;

  if (hasQ) {
    token = TOKEN_OF(epoch);
    asm volatile("" : "+v"(token));
    if (w == 7) *(LAS unsigned*)pk = ~token;
    STAMP(1);
    f32x16 acc[2] = {};
    const bool poller = w == 7 && hasG1;
    const unsigned post = poller ? (1u << 8) | (1u << 10) | (1u << 12) : 0u;
    auto hook = [&](int s) {
      if (s == 14 && poller) peek_check(pk, rws, fh_off, token);
      LBAR();
      if (RARE((post >> s) & 1)) peek_issue(lds0 + L_P, ws, fh_off);
      SB();
    };
    if (tn >= 8) qkv_consume<true>(L, so, w, lane, acc, hook);
    else qkv_consume<false>(L, so, w, lane, acc, hook);
    STAMP(2);
    const int wm = w >> 2, wn = w & 3;
    const int hd0 = (tn & 3) * 8 + wn * 2;
#pragma unroll
    for (int mi = 0; mi < 2; ++mi) {
      const int tb = tm * 4 + wm * 2 + mi;
      const float sc = tn < 4 ? QSCALE : 1.0f;
      const unsigned dst = tn >= 8 ? WS_V + (((hd0 + (r >> 4)) * 64 + tb) * 64 + h * 16 + (r & 15)) * 16
                                   : (tn < 4 ? WS_Q : WS_K) + ((hd0 * 64 + tb) * 64 + lane) * 16;
      const unsigned dstep = tn >= 8 ? 512 : 65536;
#pragma unroll
      for (int s = 0; s < 2; ++s) {
        u32x4 v;
#pragma unroll
        for (int jj = 0; jj < 4; ++jj) v[jj] = pk2(acc[mi][8 * s + 2 * jj] * sc, acc[mi][8 * s + 2 * jj + 1] * sc);
        st128(rws, dst + s * dstep, v);
      }
    }
  } else {
    constexpr int RS = 1056;
    u32x4 xv[4];
#pragma unroll
    for (int i = 0; i < 4; ++i) xv[i] = *(const u32x4*)(x + (size_t)(lnb * 32 + w * 4 + i) * 1024 + lane * 16);
    const u32x4 gv = *(const u32x4*)(gw + lane * 16);
    float gsel = 0.f;
#pragma unroll
    for (int i = 0; i < 4; ++i) {
      float v[8], s = 0.f, gs = 0.f;
#pragma unroll
      for (int jj = 0; jj < 4; ++jj) {
        v[2 * jj] = bflo(xv[i][jj]); v[2 * jj + 1] = bfhi(xv[i][jj]);
        s += v[2 * jj] + v[2 * jj + 1];
        gs += v[2 * jj] * bflo(gv[jj]) + v[2 * jj + 1] * bfhi(gv[jj]);
      }
      s = wave_sum(s); gs = wave_sum(gs);
      const float m = s * (1.0f / 512);
      float qq = 0.f;
#pragma unroll
      for (int jj = 0; jj < 8; ++jj) { v[jj] -= m; qq += v[jj] * v[jj]; }
      qq = wave_sum(qq);
      const float rs = __builtin_amdgcn_rsqf(qq * (1.0f / 512) + 1e-5f);
      u32x4 o;
#pragma unroll
      for (int jj = 0; jj < 4; ++jj) o[jj] = pk2(v[2 * jj] * rs, v[2 * jj + 1] * rs);
      *(LAS u32x4*)(L + (w * 4 + i) * RS + lane * 16) = o;
      gsel = lane == i ? gs : gsel;
    }
    token = TOKEN_OF(epoch);
    asm volatile("" : "+v"(token));
    if (lane < 4) st32(rws, WS_G + (lnb * 32 + w * 4 + lane) * 4, __float_as_uint(1.0f / (1.0f + __expf(-gsel))));
    STAMP(1);
    LBAR();
#pragma unroll
    for (int i = 0; i < 4; ++i) {
      const int D = w * 4 + i, d48 = D / 3;
      lptr a = L + (4 * g4 + q4) * RS + D * 32 + p4 * 8;
      const bf16x8 f = cat8(ldstr(a), ldstr(a + 16 * RS));
      st128(rws, WS_H + ((d48 * 64 + lnb) * 3 + (D - d48 * 3)) * 1024 + lane * 16, __builtin_bit_cast(u32x4, f));
    }
    DBAR();
    if (threadIdx.x == 0) flag_st(ws, FL_H + lnb * 32, token);
    STAMP(2);
    LBAR();
  }

  bf16x8 qf;
  if (hasG1) {
    const int kh = w >> 2, fb = w & 3;
    int ha[3];
#pragma unroll
    for (int db = 0; db < 3; ++db) { const int o = (kh * 3 + db) * 1024 + lane * 16, rg = o / 1536; ha[db] = rg * 4096 + 2048 + (o - rg * 1536); }
    const int wa = kh * 8192 + (4 * g4 + q4) * 128 + ((fb ^ ((2 * g4 + (q4 >> 1)) & 3)) << 5) + p4 * 8;
    f32x4 acc[3] = {};
    bf16x8 wA, hA[3], wB, hB[3];
#define G1RD(W, H) do { lptr sl = L + so; NEXT_SO(); W = cat8(ldstr(sl + wa), ldstr(sl + wa + 4096)); \
    _Pragma("unroll") for (int db = 0; db < 3; ++db) H[db] = ldsr128(sl + ha[db]); SB(); } while (0)
#define G1MM(W, H) do { _Pragma("unroll") for (int db = 0; db < 3; ++db) acc[db] = MFMA16(W, H[db], acc[db]); SB(); } while (0)
    const unsigned pre = (1u << 6) | (w == 7 ? 1u << 18 : 0u);
    const unsigned post = (w == 0 && hasQ ? 1u << 6 : 0u) | (w == 7 ? (1u << 6) | (1u << 12) | (1u << 14) | (1u << 16) : 0u);
    auto hook = [&](int s) {
      if (RARE((pre >> s) & 1)) { if (s == 6) asm volatile("s_waitcnt vmcnt(0)" ::: "memory"); else peek_check(pk, rws, fq_off, token); }
      LBAR();
      if (RARE((post >> s) & 1)) {
        if (s == 6) { if (w == 7) *(LAS unsigned*)pk = ~token; else if (lane == 0) flag_st(ws, FL_Q + (tn * 16 + tm) * 32, token); }
        else peek_issue(lds0 + L_P, ws, fq_off);
      }
      SB();
    };
    LBAR(); G1RD(wA, hA);
#pragma unroll 1
    for (int s = 2; s < 20; s += 2) {
      LBAR();  G1RD(wB, hB); G1MM(wA, hA);
      hook(s); G1RD(wA, hA); G1MM(wB, hB);
    }
    STAMP(10);
    qf = __builtin_bit_cast(bf16x8, ld128(rws, WS_Q + (head * 64 + qb * 8 + w) * 1024 + lane * 16));
    const int fblk = g1ft * 4 + fb;
    u32x2 bb = *(const u32x2*)(b1 + min(fblk * 16 + 4 * g4, 1372) * 2);
#pragma unroll 1
    for (int s = 20; s < 32; s += 2) {
      LBAR(); G1RD(wB, hB); G1MM(wA, hA);
      LBAR(); G1RD(wA, hA); G1MM(wB, hB);
    }
    asm volatile("" : "+v"(qf), "+v"(bb));
    LBAR(); G1RD(wB, hB); G1MM(wA, hA);
    LBAR();
    G1MM(wB, hB);
    STAMP(3);
    if (kh == 1) { *(LAS f32x4*)(L + L_X + ((fb * 3 + 0) * 64 + lane) * 16) = acc[0]; *(LAS f32x4*)(L + L_X + ((fb * 3 + 1) * 64 + lane) * 16) = acc[1]; }
    else *(LAS f32x4*)(L + L_X + ((fb * 3 + 2) * 64 + lane) * 16) = acc[2];
    LBAR();
    if (fblk < 86) {
#pragma unroll
      for (int db = 0; db < 3; ++db) {
        const int D = g1dt * 3 + db;
        if ((db == 2) == (kh == 1) && D < 32) {
          const f32x4 o = *(const LAS f32x4*)(L + L_X + ((fb * 3 + db) * 64 + lane) * 16);
          const float u0 = gelu_tanh(acc[db][0] + o[0] + bflo(bb[0])), u1 = gelu_tanh(acc[db][1] + o[1] + bfhi(bb[0]));
          const float u2 = gelu_tanh(acc[db][2] + o[2] + bflo(bb[1])), u3 = gelu_tanh(acc[db][3] + o[3] + bfhi(bb[1]));
          const u32x2 v = {pk2(u0, u1), pk2(u2, u3)};
          st64(rws, WS_U + ((((D >> 2) * 86 + fblk) * 2 + ((D >> 1) & 1)) * 64 + (g4 >> 1) * 32 + (D & 1) * 16 + (lane & 15)) * 16 + (g4 & 1) * 8, v);
        }
      }
    }
  } else {
    DBAR();
    if (threadIdx.x == 0) flag_st(ws, FL_Q + (tn * 16 + tm) * 32, token);
    LBAR();
    qf = __builtin_bit_cast(bf16x8, ld128(rws, WS_Q + (head * 64 + qb * 8 + w) * 1024 + lane * 16));
    asm volatile("" : "+v"(qf));
    LBAR();
    STAMP(3);
  }

  {
    lptr kp = L + L_K + w * 1024 + lane * 16;
    const int i_lo = qb > 0 ? 1 : 8 - w;
    const f32x16 zero = {};
    f32x16 sb;
    {
      const f32x16 sd = MFMA32(ldsr128(kp + 8 * 1024), qf, zero);
      const f32x16 sf = MFMA32(ldsr128(kp), qf, zero);
#pragma unroll
      for (int i = 0; i < 16; ++i) sb[i] = (crow(i, h) <= r) ? sd[i] : (qb > 0 ? sf[i] : -INFINITY);
    }
    float m = max16(sb, -INFINITY);
    if (qb > 0) {
      float m2 = -INFINITY;
#pragma unroll
      for (int i = 1; i < 8; ++i) {
        const f32x16 s = MFMA32(ldsr128(kp + i * 1024), qf, zero);
        if (i & 1) m2 = max16(s, m2); else m = max16(s, m);
      }
      m = __builtin_fmaxf(m, m2);
    } else {
#pragma unroll 1
      for (int i = i_lo; i < 8; ++i) {
        const f32x16 s = MFMA32(ldsr128(kp + i * 1024), qf, zero);
        m = max16(s, m);
      }
    }
    {
      auto rr = __builtin_amdgcn_permlane32_swap(__float_as_uint(m), __float_as_uint(m), false, false);
      m = __builtin_fmaxf(__uint_as_float(rr[0]), __uint_as_float(rr[1]));
    }
    STAMP(11);
    const int vstr = r < 16 ? 1024 : 0;
    lptr vp0 = L + (r < 16 ? L_V + w * 1024 + h * 256 + r * 16 : (L_C + ((r == 16 || r == 20) ? 0 : 16)));
    f32x16 o = {};
    {
      u32x4 pd[2], pf[2];
#pragma unroll
      for (int i = 0; i < 16; i += 2) {
        const float p0 = __builtin_amdgcn_exp2f(sb[i] - m), p1 = __builtin_amdgcn_exp2f(sb[i + 1] - m);
        const bool v0 = crow(i, h) <= r, v1 = crow(i + 1, h) <= r;
        pd[i >> 3][(i >> 1) & 3] = pk2(v0 ? p0 : 0.f, v1 ? p1 : 0.f);
        pf[i >> 3][(i >> 1) & 3] = pk2(v0 ? 0.f : p0, v1 ? 0.f : p1);
      }
      lptr vp = vp0 + 8 * vstr;
      o = MFMA32(ldsr128(vp), __builtin_bit_cast(bf16x8, pd[0]), o);
      o = MFMA32(ldsr128(vp + 512), __builtin_bit_cast(bf16x8, pd[1]), o);
      if (qb > 0) {
        o = MFMA32(ldsr128(vp0), __builtin_bit_cast(bf16x8, pf[0]), o);
        o = MFMA32(ldsr128(vp0 + 512), __builtin_bit_cast(bf16x8, pf[1]), o);
      }
    }
    if (hasG1) {
      DBAR();
      if (threadIdx.x == 0) flag_st(ws, FL_U + (g1dt * 32 + g1ft) * 32, token);
    }
    STAMP(4);
    f32x16 cin;
#pragma unroll
    for (int i = 0; i < 16; ++i) cin[i] = -m;
    if (i_lo < 8) {
      f32x16 s = MFMA32(ldsr128(kp + i_lo * 1024), qf, cin);
#pragma unroll 1
      for (int i = i_lo; i < 8; ++i) {
        lptr vp = vp0 + i * vstr;
        const bf16x8 kn = ldsr128(kp + (i + 1) * 1024), v0 = ldsr128(vp), v1 = ldsr128(vp + 512);
        u32x4 p0, p1;
#pragma unroll
        for (int e = 0; e < 8; e += 2) p0[e >> 1] = pk2(__builtin_amdgcn_exp2f(s[e]), __builtin_amdgcn_exp2f(s[e + 1]));
        o = MFMA32(v0, __builtin_bit_cast(bf16x8, p0), o);
#pragma unroll
        for (int e = 8; e < 16; e += 2) p1[(e >> 1) & 3] = pk2(__builtin_amdgcn_exp2f(s[e]), __builtin_amdgcn_exp2f(s[e + 1]));
        s = MFMA32(kn, qf, cin);
        o = MFMA32(v1, __builtin_bit_cast(bf16x8, p1), o);
      }
    }
    const float il = 1.0f / o[8];
    u32x4 ov;
#pragma unroll
    for (int jj = 0; jj < 4; ++jj) ov[jj] = pk2(o[2 * jj] * il, o[2 * jj + 1] * il);
    const int B = qb * 8 + w;
    st128(rws, WS_O + (((B >> 1) * 32 + head) * 2 + (B & 1)) * 1024 + lane * 16, ov);
  }
  STAMP(5);

  {
    const int kh = w >> 2, mt = (w >> 1) & 1, nt = w & 1;
    const int tok = st * 64 + nt * 32 + r;
    const int ta2 = kh * 8192 + (8 * h + q4) * 128 + (((nt * 4 + blk * 2 + (p4 >> 1)) ^ ((q4 >> 1) << 2)) << 4) + (p4 & 1) * 8;
    const int tao = kh * 8192 + (4 * h + q4) * 128 + (((mt * 4 + blk * 2 + (p4 >> 1)) ^ ((q4 >> 1) << 2)) << 4) + (p4 & 1) * 8;
    const int fa2 = kh * 8192 + 2048 + mt * 1024 + lane * 16;
    const int fao = kh * 8192 + 2048 + nt * 1024 + lane * 16;
    f32x16 ay = {}, aa = {};
    bf16x8 pa0, pb0, pa1, pb1, qa0, qb0, qa1, qb1;
    float g = 0.f; unsigned b2v = 0;
#define RD2(A0, B0, A1, B1) do { lptr sl = L + so; NEXT_SO(); B0 = cat8(ldstr(sl + ta2), ldstr(sl + ta2 + 512)); A0 = ldsr128(sl + fa2); \
    B1 = cat8(ldstr(sl + ta2 + 4096), ldstr(sl + ta2 + 4096 + 512)); A1 = ldsr128(sl + fa2 + 4096); SB(); } while (0)
#define RDO(A0, B0, A1, B1) do { lptr sl = L + so; NEXT_SO(); A0 = cat8(ldstr(sl + tao), ldstr(sl + tao + 1024)); B0 = ldsr128(sl + fao); \
    A1 = cat8(ldstr(sl + tao + 4096), ldstr(sl + tao + 4096 + 1024)); B1 = ldsr128(sl + fao + 4096); SB(); } while (0)
#define MM2(ACC, A0, B0, A1, B1) do { ACC = MFMA32(A0, B0, ACC); ACC = MFMA32(A1, B1, ACC); SB(); } while (0)
    const unsigned pre = (1u << 4) | (w == 7 ? 1u << 16 : 0u);
    const unsigned post = (w == 0 ? 1u << 4 : 0u) | (w == 7 ? (1u << 4) | (1u << 10) | (1u << 12) | (1u << 14) : 0u);
    auto hook = [&](int s) {
      if (RARE((pre >> s) & 1)) { if (s == 4) asm volatile("s_waitcnt vmcnt(0)" ::: "memory"); else peek_check(pk, rws, fo_off, token); }
      LBAR();
      if (RARE((post >> s) & 1)) {
        if (s == 4) { if (w == 7) *(LAS unsigned*)pk = ~token; else if (lane == 0) flag_st(ws, FL_O + (qb * 32 + head) * 32, token); }
        else peek_issue(lds0 + L_P, ws, fo_off);
      }
      SB();
    };
    LBAR(); STAMP(6);
    RD2(pa0, pb0, pa1, pb1);
    if (kh) { pa0 = bf16x8{}; pa1 = bf16x8{}; pb0 = bf16x8{}; pb1 = bf16x8{}; }
#pragma unroll 1
    for (int s = 2; s < 22; s += 2) {
      LBAR();  RD2(qa0, qb0, qa1, qb1); MM2(ay, pa0, pb0, pa1, pb1);
      hook(s); RD2(pa0, pb0, pa1, pb1); MM2(ay, qa0, qb0, qa1, qb1);
    }
    g = __uint_as_float(ld32(rws, WS_G + tok * 4));
    b2v = ((const unsigned short*)b2)[tok];
    LBAR(); RD2(qa0, qb0, qa1, qb1); MM2(ay, pa0, pb0, pa1, pb1);
    STAMP(7);
    LBAR(); RDO(pa0, pb0, pa1, pb1); MM2(ay, qa0, qb0, qa1, qb1);
#pragma unroll 1
    for (int s = 23; s < 29; s += 2) {
      LBAR(); RDO(qa0, qb0, qa1, qb1); MM2(aa, pa0, pb0, pa1, pb1);
      LBAR(); RDO(pa0, pb0, pa1, pb1); MM2(aa, qa0, qb0, qa1, qb1);
    }
    LBAR(); RDO(qa0, qb0, qa1, qb1); MM2(aa, pa0, pb0, pa1, pb1);
    MM2(aa, qa0, qb0, qa1, qb1);
    STAMP(8);
    const float og = 1.0f - g;
    float v[16];
#pragma unroll
    for (int i = 0; i < 16; ++i) v[i] = ay[i] * og + aa[i] * g;
    LBAR();
    {
      lptr ex = L + w * 2048 + lane * 16;
      f32x4 s0, s1;
#pragma unroll
      for (int jj = 0; jj < 4; ++jj) { s0[jj] = kh ? v[jj] : v[8 + jj]; s1[jj] = kh ? v[4 + jj] : v[12 + jj]; }
      *(LAS f32x4*)(ex) = s0; *(LAS f32x4*)(ex + 1024) = s1;
    }
    LBAR();
    {
      lptr ex = L + (w ^ 4) * 2048 + lane * 16;
      const f32x4 r0 = *(const LAS f32x4*)(ex), r1 = *(const LAS f32x4*)(ex + 1024);
      const float bo = bflo(b2v) * og;
      float f[8];
#pragma unroll
      for (int jj = 0; jj < 4; ++jj) { f[jj] = (kh ? v[8 + jj] : v[jj]) + r0[jj] + bo; f[4 + jj] = (kh ? v[12 + jj] : v[4 + jj]) + r1[jj] + bo; }
      const unsigned a0 = pk2(f[0], f[1]), a1 = pk2(f[2], f[3]), c0 = pk2(f[4], f[5]), c1 = pk2(f[6], f[7]);
      auto x0 = __builtin_amdgcn_permlane32_swap(a0, c0, false, false);
      auto x1 = __builtin_amdgcn_permlane32_swap(a1, c1, false, false);
      const u32x4 ov = {x0[0], x1[0], x0[1], x1[1]};
      *(u32x4*)(out + (size_t)tok * 1024 + (dt * 64 + mt * 32 + kh * 16 + h * 8) * 2) = ov;
    }
  }
  STAMP(9);
  if (w == 0) STAMP_OUT(ws, 0);
}

extern "C" void kernel_launch(void* const* d_in, const int* in_sizes, int n_in, void* d_out, int out_size, void* d_ws, size_t ws_size,
                              hipStream_t stream) {
  (void)hipFuncSetAttribute((const void*)fused_kernel, hipFuncAttributeMaxDynamicSharedMemorySize, LDS_BYTES);
  fused_kernel<<<256, 768, LDS_BYTES, stream>>>((const char*)d_in[0], (const char*)d_in[1], (const char*)d_in[2], (const char*)d_in[4],
                                                (const char*)d_in[5], (const char*)d_in[6], (const char*)d_in[7], (const char*)d_in[8],
                                                (char*)d_ws, (char*)d_out);
}
```

```cpp
#include <hip/hip_runtime.h>
#include <hip/hip_bf16.h>
#include <stdint.h>

#define LAS __attribute__((address_space(3)))
#define GAS __attribute__((address_space(1)))
#define DI __device__ __forceinline__
typedef __attribute__((ext_vector_type(8))) __bf16 bf16x8;
typedef __attribute__((ext_vector_type(2))) __bf16 bf16x2;
typedef __attribute__((ext_vector_type(4))) short s16x4;
typedef __attribute__((ext_vector_type(2))) float f32x2;
typedef __attribute__((ext_vector_type(4))) float f32x4;
typedef __attribute__((ext_vector_type(16))) float f32x16;
typedef __attribute__((ext_vector_type(2))) unsigned u32x2;
typedef __attribute__((ext_vector_type(4))) unsigned u32x4;
typedef LAS char* lptr;
typedef __amdgpu_buffer_rsrc_t rsrc_t;

constexpr unsigned WS_Q = 0, WS_K = 2u << 20, WS_V = 4u << 20, WS_O = 6u << 20, WS_U = 8u << 20, WS_H = 10u << 20,
                   WS_G = 13u << 20, WS_FL = 14u << 20, WS_END = 15u << 20;
constexpr int FL_H = 0;
constexpr int FL_Q = 64 * 32;
constexpr int FL_U = FL_Q + 192 * 32;
constexpr int FL_O = FL_U + 352 * 32;
constexpr int FL_EPOCH = FL_O + 256 * 32;
constexpr float QSCALE = 0.25f * 1.4426950408889634f;
constexpr int NS = 7, SLOT = 16384, RING = NS * SLOT;
constexpr int L_K = RING, L_V = RING + 16384, L_X = RING + 32768  , L_C = L_X + 12288  , L_P = L_C + 1024  ;
constexpr int LDS_BYTES = 163840;
constexpr unsigned SPIN_CAP = 200000;
#define CODEPF 0
#define PREFETCH 3
#define QROT 1

#define MFMA32(a, b, c) __builtin_amdgcn_mfma_f32_32x32x16_bf16(a, b, c, 0, 0, 0)
#define MFMA16(a, b, c) __builtin_amdgcn_mfma_f32_16x16x32_bf16(a, b, c, 0, 0, 0)
#define SB() __builtin_amdgcn_sched_barrier(0)
#define LBAR() do { asm volatile("s_waitcnt lgkmcnt(0)\n\ts_barrier" ::: "memory"); SB(); } while (0)
#define DBAR() do { asm volatile("s_waitcnt vmcnt(0) lgkmcnt(0)\n\ts_barrier" ::: "memory"); SB(); } while (0)
#define VBAR(N) asm volatile("s_waitcnt vmcnt(" #N ")\n\ts_barrier" ::: "memory")
#define BAR() asm volatile("s_barrier" ::: "memory")

#define STAMP(i) do {} while (0)
#define STAMP_OUT(ws, slot) do {} while (0)

DI int rfl(int x) { return __builtin_amdgcn_readfirstlane(x); }
DI bf16x8 ldsr128(lptr p) { return *(const LAS bf16x8*)p; }
DI s16x4 ldstr(lptr p) { return __builtin_amdgcn_ds_read_tr16_b64_v4i16((LAS s16x4*)p); }
DI bf16x8 cat8(s16x4 a, s16x4 b) { return __builtin_bit_cast(bf16x8, __builtin_shufflevector(a, b, 0, 1, 2, 3, 4, 5, 6, 7)); }
DI unsigned pk2(float a, float b) { f32x2 f = {a, b}; return __builtin_bit_cast(unsigned, __builtin_convertvector(f, bf16x2)); }
DI float bflo(unsigned u) { return __uint_as_float(u << 16); }
DI float bfhi(unsigned u) { return __uint_as_float(u & 0xffff0000u); }
DI int crow(int i, int h) { return (i & 3) + 8 * (i >> 2) + 4 * h; }
DI void st128(rsrc_t r, unsigned off, u32x4 v) { __builtin_amdgcn_raw_buffer_store_b128(v, r, off, 0, 16); }
DI void st64(rsrc_t r, unsigned off, u32x2 v) { __builtin_amdgcn_raw_buffer_store_b64(v, r, off, 0, 16); }
DI void st32(rsrc_t r, unsigned off, unsigned v) { __builtin_amdgcn_raw_buffer_store_b32(v, r, off, 0, 16); }
DI u32x4 ld128(rsrc_t r, unsigned off) { return __builtin_amdgcn_raw_buffer_load_b128(r, off, 0, 16); }
DI unsigned ld32(rsrc_t r, unsigned off) { return __builtin_amdgcn_raw_buffer_load_b32(r, off, 0, 16); }
DI unsigned flag_ld(const char* ws, int idx) { return __hip_atomic_load((const GAS unsigned*)(ws + WS_FL) + idx, __ATOMIC_RELAXED, __HIP_MEMORY_SCOPE_AGENT); }
DI void flag_st(char* ws, int idx, unsigned v) { __hip_atomic_store((GAS unsigned*)(ws + WS_FL) + idx, v, __ATOMIC_RELAXED, __HIP_MEMORY_SCOPE_AGENT); }

#define M0_IN "s_mov_b32 %0, m0\n\ts_mov_b32 m0, %1\n\ts_nop 0\n\t"
#define M0_OUT "s_mov_b32 m0, %0"
DI void dma_2x2(unsigned m0v, const void* b0, unsigned v0, unsigned v1, const void* b1, unsigned v2, unsigned v3) {
  unsigned sv;
  asm volatile(M0_IN "global_load_lds_dwordx4 %4, %2\n\tglobal_load_lds_dwordx4 %5, %2 offset:1024\n\t"
               "global_load_lds_dwordx4 %6, %3 offset:2048\n\tglobal_load_lds_dwordx4 %7, %3 offset:3072\n\t" M0_OUT
               : "=&s"(sv) : "s"(m0v), "s"(b0), "s"(b1), "v"(v0), "v"(v1), "v"(v2), "v"(v3) : "memory");
}
DI void dma_lin4_sc1(unsigned m0v, const void* b, unsigned vl) {
  unsigned sv;
  asm volatile(M0_IN "global_load_lds_dwordx4 %3, %2 sc1\n\tglobal_load_lds_dwordx4 %3, %2 offset:1024 sc1\n\t"
               "global_load_lds_dwordx4 %3, %2 offset:2048 sc1\n\tglobal_load_lds_dwordx4 %3, %2 offset:3072 sc1\n\t" M0_OUT
               : "=&s"(sv) : "s"(m0v), "s"(b), "v"(vl) : "memory");
}
DI void dma_g1(unsigned m0v, const void* wb, unsigned v0, unsigned v1, const void* hb_m2048, unsigned vl) {
  unsigned sv;
  asm volatile(M0_IN "global_load_lds_dwordx4 %4, %2\n\tglobal_load_lds_dwordx4 %5, %2 offset:1024\n\t"
               "global_load_lds_dwordx4 %6, %3 offset:2048 sc1\n\t"
               "s_mov_b32 exec_hi, 0\n\tglobal_load_lds_dwordx4 %6, %3 offset:3072 sc1\n\ts_mov_b32 exec_hi, -1\n\t" M0_OUT
               : "=&s"(sv) : "s"(m0v), "s"(wb), "s"(hb_m2048), "v"(v0), "v"(v1), "v"(vl) : "memory");
}
DI void dma_hh(unsigned m0v, const void* hb_m2048, unsigned vl) {
  unsigned sv;
  asm volatile(M0_IN "global_load_lds_dwordx4 %3, %2 offset:2048 sc1\n\t"
               "s_mov_b32 exec_hi, 0\n\tglobal_load_lds_dwordx4 %3, %2 offset:3072 sc1\n\ts_mov_b32 exec_hi, -1\n\t" M0_OUT
               : "=&s"(sv) : "s"(m0v), "s"(hb_m2048), "v"(vl) : "memory");
}
DI void dma_lin4(unsigned m0v, const void* b, unsigned vl) {
  unsigned sv;
  asm volatile(M0_IN "global_load_lds_dwordx4 %3, %2\n\tglobal_load_lds_dwordx4 %3, %2 offset:1024\n\t"
               "global_load_lds_dwordx4 %3, %2 offset:2048\n\tglobal_load_lds_dwordx4 %3, %2 offset:3072\n\t" M0_OUT
               : "=&s"(sv) : "s"(m0v), "s"(b), "v"(vl) : "memory");
}
DI void dma_touch(unsigned m0v, const void* b, unsigned voff) {
  unsigned sv;
  asm volatile(M0_IN "global_load_lds_dword %3, %2\n\t" M0_OUT : "=&s"(sv) : "s"(m0v), "s"(b), "v"(voff) : "memory");
}
DI void dma_wh(unsigned m0v, const void* wb, unsigned v0, unsigned v1) {
  unsigned sv;
  asm volatile(M0_IN "global_load_lds_dwordx4 %3, %2\n\tglobal_load_lds_dwordx4 %4, %2 offset:1024\n\t" M0_OUT
               : "=&s"(sv) : "s"(m0v), "s"(wb), "v"(v0), "v"(v1) : "memory");
}
DI void dma_fh(unsigned m0v, const void* fb_m2048, unsigned vl) {
  unsigned sv;
  asm volatile(M0_IN "global_load_lds_dwordx4 %3, %2 offset:2048 sc1\n\tglobal_load_lds_dwordx4 %3, %2 offset:3072 sc1\n\t" M0_OUT
               : "=&s"(sv) : "s"(m0v), "s"(fb_m2048), "v"(vl) : "memory");
}

DI void spin1(rsrc_t r, unsigned off, unsigned tok) {
  unsigned n = 0;
#pragma nounroll
  do { const unsigned v = ld32(r, off); if (__all(v == tok)) break; __builtin_amdgcn_s_sleep(4); asm volatile("" ::: "memory"); } while (++n < SPIN_CAP);
  __builtin_amdgcn_fence(__ATOMIC_ACQUIRE, "wavefront");
}
DI void peek_issue(unsigned m0v, const void* ws, unsigned off) {
  unsigned sv;
  asm volatile(M0_IN "global_load_lds_dwordx4 %3, %2 sc1\n\t" M0_OUT : "=&s"(sv) : "s"(m0v), "s"(ws), "v"(off) : "memory");
}
DI void peek_check(lptr p, rsrc_t r, unsigned off, unsigned tok) {
  if (__all(*(const volatile LAS unsigned*)p == tok)) return;
  asm volatile("s_waitcnt vmcnt(0)" ::: "memory");
  if (__all(*(const volatile LAS unsigned*)p == tok)) return;
  spin1(r, off, tok);
}
#define RARE(c) __builtin_expect((c) != 0, 0)

DI float wave_sum(float v) {
#define DPPADD(ctrl, rm) v += __int_as_float(__builtin_amdgcn_update_dpp(0, __float_as_int(v), ctrl, rm, 0xf, false))
  DPPADD(0x111, 0xf); DPPADD(0x112, 0xf); DPPADD(0x114, 0xf); DPPADD(0x118, 0xf); DPPADD(0x142, 0xa); DPPADD(0x143, 0xc);
#undef DPPADD
  return __int_as_float(__builtin_amdgcn_readlane(__float_as_int(v), 63));
}
DI float gelu_tanh(float u) {
  const float z = u * (0.7978845608028654f + 0.7978845608028654f * 0.044715f * u * u);
  const float e = __builtin_amdgcn_exp2f(z * (-2.0f * 1.4426950408889634f));
  return u * __builtin_amdgcn_rcpf(1.0f + e);
}
DI float max16(const f32x16& s, float m) {
#pragma unroll
  for (int i = 0; i < 16; i += 2) m = __builtin_fmaxf(__builtin_fmaxf(s[i], s[i + 1]), m);
  return m;
}

#define NEXT_SO() so = so == (NS - 1) * SLOT ? 0 : so + SLOT

template <bool ISV, class Hook, class Hook14>
DI void qkv_consume(lptr L, int& so, int w, int lane, f32x16 (&acc)[2], Hook hook, Hook14 hook14) {
  const int r = lane & 31, h = lane >> 5, wm = w >> 2, wn = w & 3;
  int xa[4];
#pragma unroll
  for (int s = 0; s < 4; ++s) xa[s] = (wm * 2 + (r >> 4)) * 4096 + (r & 15) * 128 + (((2 * s + h) ^ ((r >> 1) & 7)) << 4);
  const int q = (lane & 15) >> 2, p = lane & 3, blk = (lane >> 4) & 1;
  const int wa = h * 4096 + 2048 + q * 256 + (((wn * 4 + blk * 2 + (p >> 1)) ^ (q << 2)) << 4) + (p & 1) * 8;
  bf16x8 xA[2][4], xB[2][4], wA[4], wB[4];
#define QM(X, W, mi, s) do { if (ISV) acc[mi] = MFMA32(X[mi][s], W[s], acc[mi]); else acc[mi] = MFMA32(W[s], X[mi][s], acc[mi]); SB(); } while (0)
#define QRX(X, mi, s) X[mi][s] = ldsr128(sl + xa[s])
#define QRW(W, mi, e) W[2 * mi + e] = cat8(ldstr(sl + wa + e * 8192), ldstr(sl + wa + e * 8192 + 1024))
#define QSTEP(mi, XN, WN, XP, WP, MM) do { lptr sl = L + so; NEXT_SO(); \
    QRX(XN, mi, 0); QRX(XN, mi, 1); QRX(XN, mi, 2); SB(); if (MM) QM(XP, WP, 0, 2 * mi); \
    QRX(XN, mi, 3); QRW(WN, mi, 0); SB(); if (MM) QM(XP, WP, 1, 2 * mi); \
    QRW(WN, mi, 1); SB(); if (MM) { QM(XP, WP, 0, 2 * mi + 1); QM(XP, WP, 1, 2 * mi + 1); } } while (0)
  LBAR(); QSTEP(0, xA, wA, xB, wB, false); LBAR(); QSTEP(1, xA, wA, xB, wB, false);
#pragma unroll 1
  for (int s = 2; s < 14; s += 4) {
    hook(s);     QSTEP(0, xB, wB, xA, wA, true); LBAR(); QSTEP(1, xB, wB, xA, wA, true);
    hook(s + 2); QSTEP(0, xA, wA, xB, wB, true); LBAR(); QSTEP(1, xA, wA, xB, wB, true);
  }
  hook14(); QSTEP(0, xB, wB, xA, wA, true); LBAR(); QSTEP(1, xB, wB, xA, wA, true);
#pragma unroll
  for (int s = 0; s < 4; ++s) { QM(xB, wB, 0, s); QM(xB, wB, 1, s); }
}

__global__ __launch_bounds__(768, 3) void fused_kernel(const char* __restrict__ x, const char* __restrict__ wqkv, const char* __restrict__ wo,
                                                       const char* __restrict__ w1, const char* __restrict__ b1, const char* __restrict__ w2,
                                                       const char* __restrict__ b2, const char* __restrict__ gw, char* __restrict__ ws,
                                                       char* __restrict__ out) {
  extern __shared__ __attribute__((aligned(16))) char smem[];
  const char* pc0;
  asm volatile("s_getpc_b64 %0" : "=s"(pc0));
  asm volatile("" :: "s"(x), "s"(wqkv), "s"(wo), "s"(w1), "s"(b1), "s"(w2), "s"(b2), "s"(gw), "s"(ws), "s"(out));
  STAMP(0);
  const int b = blockIdx.x, xcd = b & 7, idx = b >> 3;
  const int lane = threadIdx.x & 63, w = rfl(threadIdx.x >> 6);
  const unsigned lds0 = (unsigned)(uintptr_t)smem;
  lptr L = (lptr)smem;
  const int Lx = xcd * 31 + (31 - idx);
  const bool hasQ = idx < 24, hasG1 = idx >= 1 && Lx < 242;
  const int tm = (xcd >> 1) * 4 + idx / 6, tn = (xcd & 1) * 6 + idx % 6;
  const int lnb = xcd * 8 + (idx - 24);
  const int g1ft = Lx < 132 ? Lx / 6 : (Lx - 132) / 5, g1dt = Lx < 132 ? Lx % 6 : 6 + (Lx - 132) % 5;
  const int head = xcd * 4 + (idx & 3), qb = idx >> 2;
  const int st = (xcd >> 1) * 8 + (idx >> 2), dt = (xcd & 1) * 4 + (idx & 3);
  unsigned fq_off;
  { const int l = lane < 12 ? lane : 3, wh = l >> 2; int tmi = l & 3; if (wh == 0 || qb == 0) tmi |= 2;
    fq_off = WS_FL + (FL_Q + ((wh * 4 + (head >> 3)) * 16 + 2 * qb - 2 + tmi) * 32) * 4; }
  const unsigned fu_off = WS_FL + (FL_U + (((lane & 32) ? (4 * dt + 3) / 3 : (4 * dt) / 3) * 32 + min(lane & 31, 21)) * 32) * 4;
  const unsigned fo_off = WS_FL + (FL_O + ((st >> 2) * 32 + (lane & 31)) * 32) * 4;
  const unsigned fh_off = WS_FL + FL_H * 4 + lane * 128;
  const rsrc_t rws = __builtin_amdgcn_make_buffer_rsrc(ws, 0, WS_END, 0x00020000);
  volatile LAS unsigned* relay = (volatile LAS unsigned*)(L + L_C + 64);
  unsigned eoff = WS_FL + FL_EPOCH * 4;
  asm volatile("" : "+v"(eoff));
  const unsigned epoch = ld32(rws, eoff);
#define TOKEN_OF(e) ((e) * 1664525u + 1013904223u)

  if (w >= 8) {
    const int j = w - 8;
#define token TOKEN_OF(epoch)
    const unsigned m0b = lds0 + j * 4096;
    const unsigned vl = lane * 16;
    unsigned so = 0;
    const char* hp = ws + WS_H + (size_t)g1dt * 64 * 3072 + j * 1536 - 2048;
    const char* w1p = w1 + (size_t)(j * 16) * 2752 + g1ft * 128;
    const unsigned u0 = (lane >> 3) * 2752 + ((((lane & 7) ^ (((lane >> 4) & 3) << 1)) & (g1ft == 21 ? 3 : 7)) << 4), u1 = u0 + 8 * 2752 - 1024;
    auto fill_g1 = [&]() { dma_g1(m0b + so, w1p, u0, u1, hp, vl); hp += 6144; w1p += 64 * 2752; NEXT_SO(); };
    auto fill_g1w = [&]() { dma_wh(m0b + so, w1p, u0, u1); w1p += 64 * 2752; NEXT_SO(); };
    auto fill_g1h4 = [&](unsigned so0) {
      const unsigned keep = so; so = so0;
#pragma unroll 1
      for (int i = 0; i < 4; ++i) { dma_hh(m0b + so, hp, vl); hp += 6144; NEXT_SO(); }
      so = keep;
    };
    const unsigned swz = ((lane & 7) ^ (((lane >> 4) & 1) << 2)) << 4;
    unsigned vw0 = (lane >> 3) * 4096 + swz, vw1 = vw0 + 8 * 4096 - 1024;
    const char* w2p = w2 + (size_t)(j * 16) * 4096 + st * 128;
    const char* up = ws + WS_U + (size_t)dt * 86 * 2048 + j * 2048 - 2048;
    const size_t w2half = (size_t)(j < 2 ? 21 * 64 : 20 * 64) * 4096;
    const int uhalf = j < 2 ? 21 * 8192 : 20 * 8192;
    auto fill_kv = [&]() {
      int t0 = qb * 8 - 8 + 4 * j; t0 = t0 < 0 ? t0 + 8 : t0;
      dma_lin4_sc1(lds0 + L_K + j * 4096, ws + WS_K + (size_t)(head * 64 + t0) * 1024, vl);
      dma_lin4_sc1(lds0 + L_V + j * 4096, ws + WS_V + (size_t)(head * 64 + t0) * 1024, vl);
    };
    int nwh = 0;
    auto fill_wh = [&]() { dma_wh(m0b + so, nwh == 0 ? w2p + w2half : w2p + (size_t)(nwh - 1) * 64 * 4096, vw0, vw1); ++nwh; NEXT_SO(); };
    unsigned so_g2 = 0;

    if (hasQ) {
      const unsigned x0 = (lane >> 3) * 1024 + (((lane & 7) ^ (lane >> 4)) << 4), x1 = (8 + (lane >> 3)) * 1024 + (((lane & 7) ^ (4 + (lane >> 4))) << 4) - 1024;
      const unsigned q0 = (lane >> 4) * 3072 + (((lane & 15) ^ ((lane >> 4) << 2)) << 4), q1 = q0 + 4 * 3072 - 1024;
      const char* xp = x + (size_t)(tm * 128 + (j >> 1) * 64 + (j & 1) * 16) * 1024;
      const char* qp = wqkv + (size_t)(j * 8) * 3072 + tn * 256 - 2048;
      int kc = QROT ? ((idx / 6 + idx % 6) % 3) * 3 : 0;
      auto fill_a = [&]() { dma_2x2(m0b + so, xp + kc * 128, x0, x1, qp + (size_t)kc * 64 * 3072, q0, q1); NEXT_SO(); };
      auto fill_b = [&]() { dma_2x2(m0b + so, xp + kc * 128 + 32 * 1024, x0, x1, qp + (size_t)kc * 64 * 3072 + 32 * 3072, q0, q1); kc = (kc + 1) & 7; NEXT_SO(); };
#pragma unroll 1
      for (int c = 0; c < 3; ++c) { fill_a(); fill_b(); }
#pragma unroll 1
      for (int c = 0; c < 5; ++c) { VBAR(20); fill_a(); VBAR(20); fill_b(); }
      if (hasG1) {
        const unsigned so0 = so;
        VBAR(20); fill_g1w(); VBAR(18); fill_g1w(); VBAR(16); fill_g1w(); VBAR(14); fill_g1w();
        VBAR(12); fill_g1h4(so0); fill_g1();
        VBAR(20); fill_g1();
      } else {
        VBAR(20); VBAR(16); VBAR(12); VBAR(8); VBAR(4); VBAR(0);
      }
    } else {
      BAR();
      VBAR(0);
#pragma unroll 1
      for (int s = 0; s < 4; ++s) fill_g1w();
      if (j == 0) spin1(rws, fh_off, token);
      else if (PREFETCH & 2) {
        const int pw = lnb * 3 + (j - 1);
#pragma unroll 1
        for (int i = pw; i < 752; i += 192) dma_touch(lds0 + L_P + 1024, i < 688 ? w2 + (size_t)i * 8192 : wo + (size_t)(i - 688) * 8192, lane * 128);
      }
      BAR();
      fill_g1h4(0); fill_g1(); fill_g1();
    }
    STAMP(1);
    if (hasG1) {
      VBAR(14); fill_g1(); VBAR(16); fill_g1(); VBAR(18); fill_g1();
#pragma unroll 1
      for (int s = 3; s < 19; ++s) { VBAR(20); fill_g1(); }
      fill_kv();
#pragma unroll 1
      for (int s = 19; s < 26; ++s) { VBAR(20); fill_g1(); }
      so_g2 = so;
      VBAR(20); fill_wh(); VBAR(18); fill_wh(); VBAR(16); fill_wh(); VBAR(14); fill_wh(); VBAR(12); fill_wh(); VBAR(10); fill_wh();
      BAR(); fill_wh();
      BAR();
      BAR();
    } else {
      BAR();
      so_g2 = so;
#pragma unroll 1
      for (int i = 0; i < 7; ++i) fill_wh();
      if (j == 0) spin1(rws, fq_off, token);
      BAR();
      fill_kv();
      VBAR(0);
    }
    STAMP(2);
    {
      if (j == 0) { spin1(rws, fu_off, token); *relay = 1u; }
      else { for (unsigned spins = 0; spins < 64 * SPIN_CAP && *relay == 0u; ++spins) __builtin_amdgcn_s_sleep(1); }
      STAMP(3);
      so = so_g2;
      dma_fh(m0b + so, up + uhalf, vl); NEXT_SO();
#pragma unroll 1
      for (int i = 0; i < 6; ++i) { dma_fh(m0b + so, up, vl); up += 8192; NEXT_SO(); }
    }
    w2p += (size_t)6 * 64 * 4096;
    unsigned wstr = 64 * 4096;
    int ci = 7;
    auto fill_g2 = [&]() {
      if (ci == 22) {
        w2p = wo + (size_t)(j * 16) * 1024 + dt * 128; up = ws + WS_O + (size_t)st * 32 * 2048 + j * 2048 - 2048; wstr = 64 * 1024;
        vw0 = (lane >> 3) * 1024 + swz; vw1 = vw0 + 8 * 1024 - 1024;
      }
      dma_wh(m0b + so, w2p, vw0, vw1); dma_fh(m0b + so, up, vl);
      w2p += wstr; up += 8192; NEXT_SO(); ++ci;
    };
    VBAR(12);
    VBAR(10); fill_g2(); VBAR(12); fill_g2(); VBAR(14); fill_g2(); VBAR(16); fill_g2(); VBAR(18); fill_g2();
#pragma unroll 1
    for (int s = 6; s < 24; ++s) { VBAR(20); fill_g2(); }
    const unsigned fa_off = WS_FL + FL_O * 4 + (j * 64 + lane) * 128;
    if (b == 0) peek_issue(lds0 + L_X + j * 1024, ws, fa_off);
    VBAR(20); VBAR(16); VBAR(12); VBAR(8); VBAR(4); VBAR(0);
    if (b == 0) peek_check(L + L_X + j * 1024 + lane * 16, rws, fa_off, token);
    BAR();
    if (b == 0 && j == 0 && lane == 0) st32(rws, WS_FL + FL_EPOCH * 4, token);
    BAR();
    STAMP(4);
    if (j == 0) STAMP_OUT(ws, 1);
    return;
  }

#undef token
  unsigned token;
  const int r = lane & 31, h = lane >> 5;
  const int g4 = lane >> 4, q4 = (lane & 15) >> 2, p4 = lane & 3, blk = (lane >> 4) & 1;
  int so = 0;
  if (CODEPF && (idx == 0 || idx == 12) && w < CODEPF) dma_touch(lds0 + L_P + 1024, pc0 + w * 8192, lane * 128);
  if (threadIdx.x == 4) *relay = 0u;
  if (threadIdx.x < 4) {
    const unsigned cv = (threadIdx.x & 1) ? 0u : 0x3f803f80u;
    *(LAS u32x4*)(L + L_C + (threadIdx.x >> 1) * 512 + (threadIdx.x & 1) * 16) = u32x4{cv, cv, cv, cv};
  }
  lptr pk = L + L_P + lane * 16;

  if (hasQ) {
    token = TOKEN_OF(epoch);
    asm volatile("" : "+v"(token));
    if (w == 7) *(LAS unsigned*)pk = ~token;
    STAMP(1);
    f32x16 acc[2] = {};
    const bool poller = w == 7 && hasG1;
    const unsigned post = poller ? (1u << 8) | (1u << 10) | (1u << 12) : 0u;
    if (PREFETCH & 1) {
      const int pi = (xcd * 24 + idx) * 4 + w;
      if (w < 4 && pi < 688) dma_touch(lds0 + L_P + 1024, w1 + (size_t)pi * 8192, lane * 128);
    }
    auto hook = [&](int s) {
      LBAR();
      if (RARE((post >> s) & 1)) peek_issue(lds0 + L_P, ws, fh_off);
      SB();
    };
    auto hook14 = [&]() { if (poller) peek_check(pk, rws, fh_off, token); LBAR(); };
    if (tn >= 8) qkv_consume<true>(L, so, w, lane, acc, hook, hook14);
    else qkv_consume<false>(L, so, w, lane, acc, hook, hook14);
    STAMP(2);
    const int wm = w >> 2, wn = w & 3;
    const int hd0 = (tn & 3) * 8 + wn * 2;
#pragma unroll
    for (int mi = 0; mi < 2; ++mi) {
      const int tb = tm * 4 + wm * 2 + mi;
      const float sc = tn < 4 ? QSCALE : 1.0f;
      const unsigned dst = tn >= 8 ? WS_V + (((hd0 + (r >> 4)) * 64 + tb) * 64 + h * 16 + (r & 15)) * 16
                                   : (tn < 4 ? WS_Q : WS_K) + ((hd0 * 64 + tb) * 64 + lane) * 16;
      const unsigned dstep = tn >= 8 ? 512 : 65536;
#pragma unroll
      for (int s = 0; s < 2; ++s) {
        u32x4 v;
#pragma unroll
        for (int jj = 0; jj < 4; ++jj) v[jj] = pk2(acc[mi][8 * s + 2 * jj] * sc, acc[mi][8 * s + 2 * jj + 1] * sc);
        st128(rws, dst + s * dstep, v);
      }
    }
  } else {
    constexpr int RS = 1056;
    u32x4 xv[4];
#pragma unroll
    for (int i = 0; i < 4; ++i) xv[i] = *(const u32x4*)(x + (size_t)(lnb * 32 + w * 4 + i) * 1024 + lane * 16);
    const u32x4 gv = *(const u32x4*)(gw + lane * 16);
    float gsel = 0.f;
#pragma unroll
    for (int i = 0; i < 4; ++i) {
      float v[8], s = 0.f, gs = 0.f;
#pragma unroll
      for (int jj = 0; jj < 4; ++jj) {
        v[2 * jj] = bflo(xv[i][jj]); v[2 * jj + 1] = bfhi(xv[i][jj]);
        s += v[2 * jj] + v[2 * jj + 1];
        gs += v[2 * jj] * bflo(gv[jj]) + v[2 * jj + 1] * bfhi(gv[jj]);
      }
      s = wave_sum(s); gs = wave_sum(gs);
      const float m = s * (1.0f / 512);
      float qq = 0.f;
#pragma unroll
      for (int jj = 0; jj < 8; ++jj) { v[jj] -= m; qq += v[jj] * v[jj]; }
      qq = wave_sum(qq);
      const float rs = __builtin_amdgcn_rsqf(qq * (1.0f / 512) + 1e-5f);
      u32x4 o;
#pragma unroll
      for (int jj = 0; jj < 4; ++jj) o[jj] = pk2(v[2 * jj] * rs, v[2 * jj + 1] * rs);
      *(LAS u32x4*)(L + (w * 4 + i) * RS + lane * 16) = o;
      gsel = lane == i ? gs : gsel;
    }
    token = TOKEN_OF(epoch);
    asm volatile("" : "+v"(token));
    if (lane < 4) st32(rws, WS_G + (lnb * 32 + w * 4 + lane) * 4, __float_as_uint(1.0f / (1.0f + __expf(-gsel))));
    STAMP(1);
    LBAR();
#pragma unroll
    for (int i = 0; i < 4; ++i) {
      const int D = w * 4 + i, d48 = D / 3;
      lptr a = L + (4 * g4 + q4) * RS + D * 32 + p4 * 8;
      const bf16x8 f = cat8(ldstr(a), ldstr(a + 16 * RS));
      st128(rws, WS_H + ((d48 * 64 + lnb) * 3 + (D - d48 * 3)) * 1024 + lane * 16, __builtin_bit_cast(u32x4, f));
      if (i == 3 && w == 7) st128(rws, WS_H + ((10 * 64 + lnb) * 3 + 2) * 1024 + lane * 16, __builtin_bit_cast(u32x4, f));
    }
    DBAR();
    if (threadIdx.x == 0) flag_st(ws, FL_H + lnb * 32, token);
    STAMP(2);
    LBAR();
  }

  bf16x8 qf;
  if (hasG1) {
    const int kh = w >> 2, fb = w & 3;
    int ha[3];
#pragma unroll
    for (int db = 0; db < 3; ++db) { const int o = (kh * 3 + db) * 1024 + lane * 16, rg = o / 1536; ha[db] = rg * 4096 + 2048 + (o - rg * 1536); }
    const int wa = kh * 8192 + (4 * g4 + q4) * 128 + ((fb ^ ((2 * g4 + (q4 >> 1)) & 3)) << 5) + p4 * 8;
    f32x4 acc[3] = {};
    bf16x8 wA, hA[3], wB, hB[3];
#define G1RD(W, H) do { lptr sl = L + so; NEXT_SO(); W = cat8(ldstr(sl + wa), ldstr(sl + wa + 4096)); \
    _Pragma("unroll") for (int db = 0; db < 3; ++db) H[db] = ldsr128(sl + ha[db]); SB(); } while (0)
#define G1MM(W, H) do { _Pragma("unroll") for (int db = 0; db < 3; ++db) acc[db] = MFMA16(W, H[db], acc[db]); SB(); } while (0)
    const unsigned pre = (1u << 6) | (w == 7 ? 1u << 18 : 0u);
    const unsigned post = (w == 0 && hasQ ? 1u << 6 : 0u) | (w == 7 ? (1u << 6) | (1u << 12) | (1u << 14) | (1u << 16) : 0u);
    auto hook = [&](int s) {
      if (RARE((pre >> s) & 1)) { if (s == 6) asm volatile("s_waitcnt vmcnt(0)" ::: "memory"); else peek_check(pk, rws, fq_off, token); }
      LBAR();
      if (RARE((post >> s) & 1)) {
        if (s == 6) { if (w == 7) *(LAS unsigned*)pk = ~token; else if (lane == 0) flag_st(ws, FL_Q + (tn * 16 + tm) * 32, token); }
        else peek_issue(lds0 + L_P, ws, fq_off);
      }
      SB();
    };
    LBAR(); G1RD(wA, hA);
#pragma unroll 1
    for (int s = 2; s < 20; s += 2) {
      LBAR();  G1RD(wB, hB); G1MM(wA, hA);
      hook(s); G1RD(wA, hA); G1MM(wB, hB);
    }
    STAMP(10);
    qf = __builtin_bit_cast(bf16x8, ld128(rws, WS_Q + (head * 64 + qb * 8 + w) * 1024 + lane * 16));
    const int fblk = g1ft * 4 + fb;
    u32x2 bb = *(const u32x2*)(b1 + min(fblk * 16 + 4 * g4, 1372) * 2);
#pragma unroll 1
    for (int s = 20; s < 32; s += 2) {
      LBAR(); G1RD(wB, hB); G1MM(wA, hA);
      LBAR(); G1RD(wA, hA); G1MM(wB, hB);
    }
    asm volatile("" : "+v"(qf), "+v"(bb));
    LBAR(); G1RD(wB, hB); G1MM(wA, hA);
    LBAR();
    G1MM(wB, hB);
    STAMP(3);
    if (kh == 1) { *(LAS f32x4*)(L + L_X + ((fb * 3 + 0) * 64 + lane) * 16) = acc[0]; *(LAS f32x4*)(L + L_X + ((fb * 3 + 1) * 64 + lane) * 16) = acc[1]; }
    else *(LAS f32x4*)(L + L_X + ((fb * 3 + 2) * 64 + lane) * 16) = acc[2];
    LBAR();
    if (fblk < 86) {
#pragma unroll
      for (int db = 0; db < 3; ++db) {
        const int D = g1dt * 3 + db;
        if ((db == 2) == (kh == 1) && D < 32) {
          const f32x4 o = *(const LAS f32x4*)(L + L_X + ((fb * 3 + db) * 64 + lane) * 16);
          const float u0 = gelu_tanh(acc[db][0] + o[0] + bflo(bb[0])), u1 = gelu_tanh(acc[db][1] + o[1] + bfhi(bb[0]));
          const float u2 = gelu_tanh(acc[db][2] + o[2] + bflo(bb[1])), u3 = gelu_tanh(acc[db][3] + o[3] + bfhi(bb[1]));
          const u32x2 v = {pk2(u0, u1), pk2(u2, u3)};
          st64(rws, WS_U + ((((D >> 2) * 86 + fblk) * 2 + ((D >> 1) & 1)) * 64 + (g4 >> 1) * 32 + (D & 1) * 16 + (lane & 15)) * 16 + (g4 & 1) * 8, v);
        }
      }
    }
  } else {
    DBAR();
    if (threadIdx.x == 0) flag_st(ws, FL_Q + (tn * 16 + tm) * 32, token);
    LBAR();
    qf = __builtin_bit_cast(bf16x8, ld128(rws, WS_Q + (head * 64 + qb * 8 + w) * 1024 + lane * 16));
    asm volatile("" : "+v"(qf));
    LBAR();
    STAMP(3);
  }

  {
    lptr kp = L + L_K + w * 1024 + lane * 16;
    const int i_lo = qb > 0 ? 1 : 8 - w;
    const f32x16 zero = {};
    f32x16 sb;
    {
      const f32x16 sd = MFMA32(ldsr128(kp + 8 * 1024), qf, zero);
      const f32x16 sf = MFMA32(ldsr128(kp), qf, zero);
#pragma unroll
      for (int i = 0; i < 16; ++i) sb[i] = (crow(i, h) <= r) ? sd[i] : (qb > 0 ? sf[i] : -INFINITY);
    }
    float m = max16(sb, -INFINITY);
    if (qb > 0) {
      float m2 = -INFINITY;
#pragma unroll
      for (int i = 1; i < 8; ++i) {
        const f32x16 s = MFMA32(ldsr128(kp + i * 1024), qf, zero);
        if (i & 1) m2 = max16(s, m2); else m = max16(s, m);
      }
      m = __builtin_fmaxf(m, m2);
    } else {
#pragma unroll 1
      for (int i = i_lo; i < 8; ++i) {
        const f32x16 s = MFMA32(ldsr128(kp + i * 1024), qf, zero);
        m = max16(s, m);
      }
    }
    {
      auto rr = __builtin_amdgcn_permlane32_swap(__float_as_uint(m), __float_as_uint(m), false, false);
      m = __builtin_fmaxf(__uint_as_float(rr[0]), __uint_as_float(rr[1]));
    }
    STAMP(11);
    const int vstr = r < 16 ? 1024 : 0;
    lptr vp0 = L + (r < 16 ? L_V + w * 1024 + h * 256 + r * 16 : (L_C + ((r == 16 || r == 20) ? 0 : 16)));
    f32x16 o = {};
    {
      u32x4 pd[2], pf[2];
#pragma unroll
      for (int i = 0; i < 16; i += 2) {
        const float p0 = __builtin_amdgcn_exp2f(sb[i] - m), p1 = __builtin_amdgcn_exp2f(sb[i + 1] - m);
        const bool v0 = crow(i, h) <= r, v1 = crow(i + 1, h) <= r;
        pd[i >> 3][(i >> 1) & 3] = pk2(v0 ? p0 : 0.f, v1 ? p1 : 0.f);
        pf[i >> 3][(i >> 1) & 3] = pk2(v0 ? 0.f : p0, v1 ? 0.f : p1);
      }
      lptr vp = vp0 + 8 * vstr;
      o = MFMA32(ldsr128(vp), __builtin_bit_cast(bf16x8, pd[0]), o);
      o = MFMA32(ldsr128(vp + 512), __builtin_bit_cast(bf16x8, pd[1]), o);
      if (qb > 0) {
        o = MFMA32(ldsr128(vp0), __builtin_bit_cast(bf16x8, pf[0]), o);
        o = MFMA32(ldsr128(vp0 + 512), __builtin_bit_cast(bf16x8, pf[1]), o);
      }
    }
    if (hasG1) {
      DBAR();
      if (threadIdx.x == 0) flag_st(ws, FL_U + (g1dt * 32 + g1ft) * 32, token);
    }
    STAMP(4);
    f32x16 cin;
#pragma unroll
    for (int i = 0; i < 16; ++i) cin[i] = -m;
    if (i_lo < 8) {
      f32x16 s = MFMA32(ldsr128(kp + i_lo * 1024), qf, cin);
#pragma unroll 1
      for (int i = i_lo; i < 8; ++i) {
        lptr vp = vp0 + i * vstr;
        const bf16x8 kn = ldsr128(kp + (i + 1) * 1024), v0 = ldsr128(vp), v1 = ldsr128(vp + 512);
        u32x4 p0, p1;
#pragma unroll
        for (int e = 0; e < 8; e += 2) p0[e >> 1] = pk2(__builtin_amdgcn_exp2f(s[e]), __builtin_amdgcn_exp2f(s[e + 1]));
        o = MFMA32(v0, __builtin_bit_cast(bf16x8, p0), o);
#pragma unroll
        for (int e = 8; e < 16; e += 2) p1[(e >> 1) & 3] = pk2(__builtin_amdgcn_exp2f(s[e]), __builtin_amdgcn_exp2f(s[e + 1]));
        s = MFMA32(kn, qf, cin);
        o = MFMA32(v1, __builtin_bit_cast(bf16x8, p1), o);
      }
    }
    const float il = 1.0f / o[8];
    u32x4 ov;
#pragma unroll
    for (int jj = 0; jj < 4; ++jj) ov[jj] = pk2(o[2 * jj] * il, o[2 * jj + 1] * il);
    const int B = qb * 8 + w;
    st128(rws, WS_O + (((B >> 1) * 32 + head) * 2 + (B & 1)) * 1024 + lane * 16, ov);
  }
  STAMP(5);

  {
    const int kh = w >> 2, mt = (w >> 1) & 1, nt = w & 1;
    const int tok = st * 64 + nt * 32 + r;
    const int ta2 = kh * 8192 + (8 * h + q4) * 128 + (((nt * 4 + blk * 2 + (p4 >> 1)) ^ ((q4 >> 1) << 2)) << 4) + (p4 & 1) * 8;
    const int tao = kh * 8192 + (4 * h + q4) * 128 + (((mt * 4 + blk * 2 + (p4 >> 1)) ^ ((q4 >> 1) << 2)) << 4) + (p4 & 1) * 8;
    const int fa2 = kh * 8192 + 2048 + mt * 1024 + lane * 16;
    const int fao = kh * 8192 + 2048 + nt * 1024 + lane * 16;
    f32x16 ay = {}, aa = {};
    bf16x8 pa0, pb0, pa1, pb1, qa0, qb0, qa1, qb1;
    float g = 0.f; unsigned b2v = 0;
#define RD2(A0, B0, A1, B1) do { lptr sl = L + so; NEXT_SO(); B0 = cat8(ldstr(sl + ta2), ldstr(sl + ta2 + 512)); A0 = ldsr128(sl + fa2); \
    B1 = cat8(ldstr(sl + ta2 + 4096), ldstr(sl + ta2 + 4096 + 512)); A1 = ldsr128(sl + fa2 + 4096); SB(); } while (0)
#define RDO(A0, B0, A1, B1) do { lptr sl = L + so; NEXT_SO(); A0 = cat8(ldstr(sl + tao), ldstr(sl + tao + 1024)); B0 = ldsr128(sl + fao); \
    A1 = cat8(ldstr(sl + tao + 4096), ldstr(sl + tao + 4096 + 1024)); B1 = ldsr128(sl + fao + 4096); SB(); } while (0)
#define MM2(ACC, A0, B0, A1, B1) do { ACC = MFMA32(A0, B0, ACC); ACC = MFMA32(A1, B1, ACC); SB(); } while (0)
    const unsigned pre = (1u << 4) | (w == 7 ? 1u << 16 : 0u);
    const unsigned post = (w == 0 ? 1u << 4 : 0u) | (w == 7 ? (1u << 4) | (1u << 10) | (1u << 12) | (1u << 14) : 0u);
    auto hook = [&](int s) {
      if (RARE((pre >> s) & 1)) { if (s == 4) asm volatile("s_waitcnt vmcnt(0)" ::: "memory"); else peek_check(pk, rws, fo_off, token); }
      LBAR();
      if (RARE((post >> s) & 1)) {
        if (s == 4) { if (w == 7) *(LAS unsigned*)pk = ~token; else if (lane == 0) flag_st(ws, FL_O + (qb * 32 + head) * 32, token); }
        else peek_issue(lds0 + L_P, ws, fo_off);
      }
      SB();
    };
    LBAR(); STAMP(6);
    RD2(pa0, pb0, pa1, pb1);
    if (kh) { pa0 = bf16x8{}; pa1 = bf16x8{}; pb0 = bf16x8{}; pb1 = bf16x8{}; }
#pragma unroll 1
    for (int s = 2; s < 22; s += 2) {
      LBAR();  RD2(qa0, qb0, qa1, qb1); MM2(ay, pa0, pb0, pa1, pb1);
      hook(s); RD2(pa0, pb0, pa1, pb1); MM2(ay, qa0, qb0, qa1, qb1);
    }
    g = __uint_as_float(ld32(rws, WS_G + tok * 4));
    b2v = ((const unsigned short*)b2)[tok];
    LBAR(); RD2(qa0, qb0, qa1, qb1); MM2(ay, pa0, pb0, pa1, pb1);
    STAMP(7);
    LBAR(); RDO(pa0, pb0, pa1, pb1); MM2(ay, qa0, qb0, qa1, qb1);
#pragma unroll 1
    for (int s = 23; s < 29; s += 2) {
      LBAR(); RDO(qa0, qb0, qa1, qb1); MM2(aa, pa0, pb0, pa1, pb1);
      LBAR(); RDO(pa0, pb0, pa1, pb1); MM2(aa, qa0, qb0, qa1, qb1);
    }
    LBAR(); RDO(qa0, qb0, qa1, qb1); MM2(aa, pa0, pb0, pa1, pb1);
    MM2(aa, qa0, qb0, qa1, qb1);
    STAMP(8);
    const float og = 1.0f - g;
    float v[16];
#pragma unroll
    for (int i = 0; i < 16; ++i) v[i] = ay[i] * og + aa[i] * g;
    LBAR();
    {
      lptr ex = L + w * 2048 + lane * 16;
      f32x4 s0, s1;
#pragma unroll
      for (int jj = 0; jj < 4; ++jj) { s0[jj] = kh ? v[jj] : v[8 + jj]; s1[jj] = kh ? v[4 + jj] : v[12 + jj]; }
      *(LAS f32x4*)(ex) = s0; *(LAS f32x4*)(ex + 1024) = s1;
    }
    LBAR();
    {
      lptr ex = L + (w ^ 4) * 2048 + lane * 16;
      const f32x4 r0 = *(const LAS f32x4*)(ex), r1 = *(const LAS f32x4*)(ex + 1024);
      const float bo = bflo(b2v) * og;
      float f[8];
#pragma unroll
      for (int jj = 0; jj < 4; ++jj) { f[jj] = (kh ? v[8 + jj] : v[jj]) + r0[jj] + bo; f[4 + jj] = (kh ? v[12 + jj] : v[4 + jj]) + r1[jj] + bo; }
      const unsigned a0 = pk2(f[0], f[1]), a1 = pk2(f[2], f[3]), c0 = pk2(f[4], f[5]), c1 = pk2(f[6], f[7]);
      auto x0 = __builtin_amdgcn_permlane32_swap(a0, c0, false, false);
      auto x1 = __builtin_amdgcn_permlane32_swap(a1, c1, false, false);
      const u32x4 ov = {x0[0], x1[0], x0[1], x1[1]};
      *(u32x4*)(out + (size_t)tok * 1024 + (dt * 64 + mt * 32 + kh * 16 + h * 8) * 2) = ov;
    }
  }
  STAMP(9);
  if (w == 0) STAMP_OUT(ws, 0);
}

extern "C" void kernel_launch(void* const* d_in, const int* in_sizes, int n_in, void* d_out, int out_size, void* d_ws, size_t ws_size,
                              hipStream_t stream) {
  (void)hipFuncSetAttribute((const void*)fused_kernel, hipFuncAttributeMaxDynamicSharedMemorySize, LDS_BYTES);
  fused_kernel<<<256, 768, LDS_BYTES, stream>>>((const char*)d_in[0], (const char*)d_in[1], (const char*)d_in[2], (const char*)d_in[4],
                                                (const char*)d_in[5], (const char*)d_in[6], (const char*)d_in[7], (const char*)d_in[8],
                                                (char*)d_ws, (char*)d_out);
}
```

```cpp
#include <hip/hip_runtime.h>
#include <hip/hip_bf16.h>
#include <stdint.h>

#define LAS __attribute__((address_space(3)))
#define GAS __attribute__((address_space(1)))
#define DI __device__ __forceinline__
typedef __attribute__((ext_vector_type(8))) __bf16 bf16x8;
typedef __attribute__((ext_vector_type(2))) __bf16 bf16x2;
typedef __attribute__((ext_vector_type(4))) short s16x4;
typedef __attribute__((ext_vector_type(2))) float f32x2;
typedef __attribute__((ext_vector_type(4))) float f32x4;
typedef __attribute__((ext_vector_type(16))) float f32x16;
typedef __attribute__((ext_vector_type(2))) unsigned u32x2;
typedef __attribute__((ext_vector_type(4))) unsigned u32x4;
typedef LAS char* lptr;
typedef __amdgpu_buffer_rsrc_t rsrc_t;

constexpr unsigned WS_Q = 0, WS_K = 2u << 20, WS_V = 4u << 20, WS_O = 6u << 20, WS_U = 8u << 20, WS_H = 10u << 20,
                   WS_G = 13u << 20, WS_FL = 14u << 20, WS_END = 15u << 20;
constexpr int FL_H = 0;
constexpr int FL_Q = 64 * 32;
constexpr int FL_U = FL_Q + 192 * 32;
constexpr int FL_O = FL_U + 352 * 32;
constexpr int FL_EPOCH = FL_O + 256 * 32;
constexpr float QSCALE = 0.25f * 1.4426950408889634f;
constexpr int NS = 7, SLOT = 16384, RING = NS * SLOT;
constexpr int L_K = RING, L_V = RING + 16384, L_X = RING + 32768  , L_C = L_X + 12288  , L_P = L_C + 1024  ;
constexpr int LDS_BYTES = 163840;
constexpr unsigned SPIN_CAP = 200000;
#define CODEPF 0
#define PREFETCH 3
#define QROT 1

#define MFMA32(a, b, c) __builtin_amdgcn_mfma_f32_32x32x16_bf16(a, b, c, 0, 0, 0)
#define MFMA16(a, b, c) __builtin_amdgcn_mfma_f32_16x16x32_bf16(a, b, c, 0, 0, 0)
#define SB() __builtin_amdgcn_sched_barrier(0)
#define LBAR() do { asm volatile("s_waitcnt lgkmcnt(0)\n\ts_barrier" ::: "memory"); SB(); } while (0)
#define DBAR() do { asm volatile("s_waitcnt vmcnt(0) lgkmcnt(0)\n\ts_barrier" ::: "memory"); SB(); } while (0)
#define VBAR(N) asm volatile("s_waitcnt vmcnt(" #N ")\n\ts_barrier" ::: "memory")
#define BAR() asm volatile("s_barrier" ::: "memory")

#define STAMP(i) do {} while (0)
#define STAMP_OUT(ws, slot) do {} while (0)

DI int rfl(int x) { return __builtin_amdgcn_readfirstlane(x); }
DI bf16x8 ldsr128(lptr p) { return *(const LAS bf16x8*)p; }
DI s16x4 ldstr(lptr p) { return __builtin_amdgcn_ds_read_tr16_b64_v4i16((LAS s16x4*)p); }
DI bf16x8 cat8(s16x4 a, s16x4 b) { return __builtin_bit_cast(bf16x8, __builtin_shufflevector(a, b, 0, 1, 2, 3, 4, 5, 6, 7)); }
DI unsigned pk2(float a, float b) { f32x2 f = {a, b}; return __builtin_bit_cast(unsigned, __builtin_convertvector(f, bf16x2)); }
DI float bflo(unsigned u) { return __uint_as_float(u << 16); }
DI float bfhi(unsigned u) { return __uint_as_float(u & 0xffff0000u); }
DI int crow(int i, int h) { return (i & 3) + 8 * (i >> 2) + 4 * h; }
DI void st128(rsrc_t r, unsigned off, u32x4 v) { __builtin_amdgcn_raw_buffer_store_b128(v, r, off, 0, 16); }
DI void st64(rsrc_t r, unsigned off, u32x2 v) { __builtin_amdgcn_raw_buffer_store_b64(v, r, off, 0, 16); }
DI void st32(rsrc_t r, unsigned off, unsigned v) { __builtin_amdgcn_raw_buffer_store_b32(v, r, off, 0, 16); }
DI u32x4 ld128(rsrc_t r, unsigned off) { return __builtin_amdgcn_raw_buffer_load_b128(r, off, 0, 16); }
DI unsigned ld32(rsrc_t r, unsigned off) { return __builtin_amdgcn_raw_buffer_load_b32(r, off, 0, 16); }
DI unsigned flag_ld(const char* ws, int idx) { return __hip_atomic_load((const GAS unsigned*)(ws + WS_FL) + idx, __ATOMIC_RELAXED, __HIP_MEMORY_SCOPE_AGENT); }
DI void flag_st(char* ws, int idx, unsigned v) { __hip_atomic_store((GAS unsigned*)(ws + WS_FL) + idx, v, __ATOMIC_RELAXED, __HIP_MEMORY_SCOPE_AGENT); }

#define M0_IN "s_mov_b32 %0, m0\n\ts_mov_b32 m0, %1\n\ts_nop 0\n\t"
#define M0_OUT "s_mov_b32 m0, %0"
DI void dma_2x2(unsigned m0v, const void* b0, unsigned v0, unsigned v1, const void* b1, unsigned v2, unsigned v3) {
  unsigned sv;
  asm volatile(M0_IN "global_load_lds_dwordx4 %4, %2\n\tglobal_load_lds_dwordx4 %5, %2 offset:1024\n\t"
               "global_load_lds_dwordx4 %6, %3 offset:2048\n\tglobal_load_lds_dwordx4 %7, %3 offset:3072\n\t" M0_OUT
               : "=&s"(sv) : "s"(m0v), "s"(b0), "s"(b1), "v"(v0), "v"(v1), "v"(v2), "v"(v3) : "memory");
}
DI void dma_lin4_sc1(unsigned m0v, const void* b, unsigned vl) {
  unsigned sv;
  asm volatile(M0_IN "global_load_lds_dwordx4 %3, %2 sc1\n\tglobal_load_lds_dwordx4 %3, %2 offset:1024 sc1\n\t"
               "global_load_lds_dwordx4 %3, %2 offset:2048 sc1\n\tglobal_load_lds_dwordx4 %3, %2 offset:3072 sc1\n\t" M0_OUT
               : "=&s"(sv) : "s"(m0v), "s"(b), "v"(vl) : "memory");
}
DI void dma_g1(unsigned m0v, const void* wb, unsigned v0, unsigned v1, const void* hb_m2048, unsigned vl) {
  unsigned sv;
  asm volatile(M0_IN "global_load_lds_dwordx4 %4, %2\n\tglobal_load_lds_dwordx4 %5, %2 offset:1024\n\t"
               "global_load_lds_dwordx4 %6, %3 offset:2048 sc1\n\t"
               "s_mov_b32 exec_hi, 0\n\tglobal_load_lds_dwordx4 %6, %3 offset:3072 sc1\n\ts_mov_b32 exec_hi, -1\n\t" M0_OUT
               : "=&s"(sv) : "s"(m0v), "s"(wb), "s"(hb_m2048), "v"(v0), "v"(v1), "v"(vl) : "memory");
}
DI void dma_hh(unsigned m0v, const void* hb_m2048, unsigned vl) {
  unsigned sv;
  asm volatile(M0_IN "global_load_lds_dwordx4 %3, %2 offset:2048 sc1\n\t"
               "s_mov_b32 exec_hi, 0\n\tglobal_load_lds_dwordx4 %3, %2 offset:3072 sc1\n\ts_mov_b32 exec_hi, -1\n\t" M0_OUT
               : "=&s"(sv) : "s"(m0v), "s"(hb_m2048), "v"(vl) : "memory");
}
DI void dma_lin4(unsigned m0v, const void* b, unsigned vl) {
  unsigned sv;
  asm volatile(M0_IN "global_load_lds_dwordx4 %3, %2\n\tglobal_load_lds_dwordx4 %3, %2 offset:1024\n\t"
               "global_load_lds_dwordx4 %3, %2 offset:2048\n\tglobal_load_lds_dwordx4 %3, %2 offset:3072\n\t" M0_OUT
               : "=&s"(sv) : "s"(m0v), "s"(b), "v"(vl) : "memory");
}
DI void dma_touch(unsigned m0v, const void* b, unsigned voff) {
  unsigned sv;
  asm volatile(M0_IN "global_load_lds_dword %3, %2\n\t" M0_OUT : "=&s"(sv) : "s"(m0v), "s"(b), "v"(voff) : "memory");
}
DI void dma_wh(unsigned m0v, const void* wb, unsigned v0, unsigned v1) {
  unsigned sv;
  asm volatile(M0_IN "global_load_lds_dwordx4 %3, %2\n\tglobal_load_lds_dwordx4 %4, %2 offset:1024\n\t" M0_OUT
               : "=&s"(sv) : "s"(m0v), "s"(wb), "v"(v0), "v"(v1) : "memory");
}
DI void dma_fh(unsigned m0v, const void* fb_m2048, unsigned vl) {
  unsigned sv;
  asm volatile(M0_IN "global_load_lds_dwordx4 %3, %2 offset:2048 sc1\n\tglobal_load_lds_dwordx4 %3, %2 offset:3072 sc1\n\t" M0_OUT
               : "=&s"(sv) : "s"(m0v), "s"(fb_m2048), "v"(vl) : "memory");
}

DI void spin1(rsrc_t r, unsigned off, unsigned tok) {
  unsigned n = 0;
#pragma nounroll
  do { const unsigned v = ld32(r, off); if (__all(v == tok)) break; __builtin_amdgcn_s_sleep(4); asm volatile("" ::: "memory"); } while (++n < SPIN_CAP);
  __builtin_amdgcn_fence(__ATOMIC_ACQUIRE, "wavefront");
}
DI void peek_issue(unsigned m0v, const void* ws, unsigned off) {
  unsigned sv;
  asm volatile(M0_IN "global_load_lds_dwordx4 %3, %2 sc1\n\t" M0_OUT : "=&s"(sv) : "s"(m0v), "s"(ws), "v"(off) : "memory");
}
DI void peek_check(lptr p, rsrc_t r, unsigned off, unsigned tok) {
  if (__all(*(const volatile LAS unsigned*)p == tok)) return;
  asm volatile("s_waitcnt vmcnt(0)" ::: "memory");
  if (__all(*(const volatile LAS unsigned*)p == tok)) return;
  spin1(r, off, tok);
}
#define RARE(c) __builtin_expect((c) != 0, 0)

DI float wave_sum(float v) {
#define DPPADD(ctrl, rm) v += __int_as_float(__builtin_amdgcn_update_dpp(0, __float_as_int(v), ctrl, rm, 0xf, false))
  DPPADD(0x111, 0xf); DPPADD(0x112, 0xf); DPPADD(0x114, 0xf); DPPADD(0x118, 0xf); DPPADD(0x142, 0xa); DPPADD(0x143, 0xc);
#undef DPPADD
  return __int_as_float(__builtin_amdgcn_readlane(__float_as_int(v), 63));
}
DI float row16_sum(float v) {
#define DPPX(ctrl) v += __int_as_float(__builtin_amdgcn_update_dpp(0, __float_as_int(v), ctrl, 0xf, 0xf, true))
  DPPX(0xB1); DPPX(0x4E); DPPX(0x141); DPPX(0x140);
#undef DPPX
  return v;
}
DI float gelu_tanh(float u) {
  const float z = u * (0.7978845608028654f + 0.7978845608028654f * 0.044715f * u * u);
  const float e = __builtin_amdgcn_exp2f(z * (-2.0f * 1.4426950408889634f));
  return u * __builtin_amdgcn_rcpf(1.0f + e);
}
constexpr unsigned long long diag_mask(int i) {
  unsigned long long mk = 0;
  for (int l = 0; l < 64; ++l) if ((i & 3) + 8 * (i >> 2) + 4 * (l >> 5) <= (l & 31)) mk |= 1ull << l;
  return mk;
}
DI float max16(const f32x16& s, float m) {
#pragma unroll
  for (int i = 0; i < 16; i += 2) m = __builtin_fmaxf(__builtin_fmaxf(s[i], s[i + 1]), m);
  return m;
}

#define NEXT_SO() so = so == (NS - 1) * SLOT ? 0 : so + SLOT

template <bool ISV, class Hook, class Hook14>
DI void qkv_consume(lptr L, int& so, int w, int lane, f32x16 (&acc)[2], Hook hook, Hook14 hook14) {
  const int r = lane & 31, h = lane >> 5, wm = w >> 2, wn = w & 3;
  int xa[4];
#pragma unroll
  for (int s = 0; s < 4; ++s) xa[s] = (wm * 2 + (r >> 4)) * 4096 + (r & 15) * 128 + (((2 * s + h) ^ ((r >> 1) & 7)) << 4);
  const int q = (lane & 15) >> 2, p = lane & 3, blk = (lane >> 4) & 1;
  const int wa = h * 4096 + 2048 + q * 256 + (((wn * 4 + blk * 2 + (p >> 1)) ^ (q << 2)) << 4) + (p & 1) * 8;
  bf16x8 xA[2][4], xB[2][4], wA[4], wB[4];
#define QM(X, W, mi, s) do { if (ISV) acc[mi] = MFMA32(X[mi][s], W[s], acc[mi]); else acc[mi] = MFMA32(W[s], X[mi][s], acc[mi]); SB(); } while (0)
#define QRX(X, mi, s) X[mi][s] = ldsr128(sl + xa[s])
#define QRW(W, mi, e) W[2 * mi + e] = cat8(ldstr(sl + wa + e * 8192), ldstr(sl + wa + e * 8192 + 1024))
#define QSTEP(mi, XN, WN, XP, WP, MM) do { lptr sl = L + so; NEXT_SO(); \
    QRX(XN, mi, 0); QRX(XN, mi, 1); QRX(XN, mi, 2); SB(); if (MM) QM(XP, WP, 0, 2 * mi); \
    QRX(XN, mi, 3); QRW(WN, mi, 0); SB(); if (MM) QM(XP, WP, 1, 2 * mi); \
    QRW(WN, mi, 1); SB(); if (MM) { QM(XP, WP, 0, 2 * mi + 1); QM(XP, WP, 1, 2 * mi + 1); } } while (0)
  LBAR(); QSTEP(0, xA, wA, xB, wB, false); LBAR(); QSTEP(1, xA, wA, xB, wB, false);
#pragma unroll 1
  for (int s = 2; s < 14; s += 4) {
    hook(s);     QSTEP(0, xB, wB, xA, wA, true); LBAR(); QSTEP(1, xB, wB, xA, wA, true);
    hook(s + 2); QSTEP(0, xA, wA, xB, wB, true); LBAR(); QSTEP(1, xA, wA, xB, wB, true);
  }
  hook14(); QSTEP(0, xB, wB, xA, wA, true); LBAR(); QSTEP(1, xB, wB, xA, wA, true);
#pragma unroll
  for (int s = 0; s < 4; ++s) { QM(xB, wB, 0, s); QM(xB, wB, 1, s); }
}

__global__ __launch_bounds__(768, 3) void fused_kernel(const char* __restrict__ x, const char* __restrict__ wqkv, const char* __restrict__ wo,
                                                       const char* __restrict__ w1, const char* __restrict__ b1, const char* __restrict__ w2,
                                                       const char* __restrict__ b2, const char* __restrict__ gw, char* __restrict__ ws,
                                                       char* __restrict__ out) {
  extern __shared__ __attribute__((aligned(16))) char smem[];
  const char* pc0;
  asm volatile("s_getpc_b64 %0" : "=s"(pc0));
  asm volatile("" :: "s"(x), "s"(wqkv), "s"(wo), "s"(w1), "s"(b1), "s"(w2), "s"(b2), "s"(gw), "s"(ws), "s"(out));
  STAMP(0);
  const int b = blockIdx.x, xcd = b & 7, idx = ((b >> 3) + 24) & 31;
  const bool lead = b == 64;
  const int lane = threadIdx.x & 63, w = rfl(threadIdx.x >> 6);
  const unsigned lds0 = (unsigned)(uintptr_t)smem;
  lptr L = (lptr)smem;
  const int Lx = xcd * 31 + (31 - idx);
  const bool hasQ = idx < 24, hasG1 = idx >= 1 && Lx < 242;
  const int tm = (xcd >> 1) * 4 + idx / 6, tn = (xcd & 1) * 6 + idx % 6;
  const int lnb = xcd * 8 + (idx - 24);
  const int g1ft = Lx < 132 ? Lx / 6 : (Lx - 132) / 5, g1dt = Lx < 132 ? Lx % 6 : 6 + (Lx - 132) % 5;
  const int head = xcd * 4 + (idx & 3), qb = idx >> 2;
  const int st = (xcd >> 1) * 8 + (idx >> 2), dt = (xcd & 1) * 4 + (idx & 3);
  unsigned fq_off;
  { const int l = lane < 12 ? lane : 3, wh = l >> 2; int tmi = l & 3; if (wh == 0 || qb == 0) tmi |= 2;
    fq_off = WS_FL + (FL_Q + ((wh * 4 + (head >> 3)) * 16 + 2 * qb - 2 + tmi) * 32) * 4; }
  const unsigned fu_off = WS_FL + (FL_U + (((lane & 32) ? (4 * dt + 3) / 3 : (4 * dt) / 3) * 32 + min(lane & 31, 21)) * 32) * 4;
  const unsigned fo_off = WS_FL + (FL_O + ((st >> 2) * 32 + (lane & 31)) * 32) * 4;
  const unsigned fh_off = WS_FL + FL_H * 4 + lane * 128;
  const rsrc_t rws = __builtin_amdgcn_make_buffer_rsrc(ws, 0, WS_END, 0x00020000);
  volatile LAS unsigned* relay = (volatile LAS unsigned*)(L + L_C + 64);
  unsigned eoff = WS_FL + FL_EPOCH * 4;
  asm volatile("" : "+v"(eoff));
  const unsigned epoch = ld32(rws, eoff);
#define TOKEN_OF(e) ((e) * 1664525u + 1013904223u)

  if (w >= 8) {
    const int j = w - 8;
#define token TOKEN_OF(epoch)
    const unsigned m0b = lds0 + j * 4096;
    const unsigned vl = lane * 16;
    unsigned so = 0;
    const char* hp = ws + WS_H + (size_t)g1dt * 64 * 3072 + j * 1536 - 2048;
    const char* w1p = w1 + (size_t)(j * 16) * 2752 + g1ft * 128;
    const unsigned u0 = (lane >> 3) * 2752 + ((((lane & 7) ^ (((lane >> 4) & 3) << 1)) & (g1ft == 21 ? 3 : 7)) << 4), u1 = u0 + 8 * 2752 - 1024;
    auto fill_g1 = [&]() { dma_g1(m0b + so, w1p, u0, u1, hp, vl); hp += 6144; w1p += 64 * 2752; NEXT_SO(); };
    auto fill_g1w = [&]() { dma_wh(m0b + so, w1p, u0, u1); w1p += 64 * 2752; NEXT_SO(); };
    auto fill_g1h4 = [&](unsigned so0) {
      const unsigned keep = so; so = so0;
#pragma unroll 1
      for (int i = 0; i < 4; ++i) { dma_hh(m0b + so, hp, vl); hp += 6144; NEXT_SO(); }
      so = keep;
    };
    const unsigned swz = ((lane & 7) ^ (((lane >> 4) & 1) << 2)) << 4;
    unsigned vw0 = (lane >> 3) * 4096 + swz, vw1 = vw0 + 8 * 4096 - 1024;
    const char* w2p = w2 + (size_t)(j * 16) * 4096 + st * 128;
    const char* up = ws + WS_U + (size_t)dt * 86 * 2048 + j * 2048 - 2048;
    const size_t w2half = (size_t)(j < 2 ? 21 * 64 : 20 * 64) * 4096;
    const int uhalf = j < 2 ? 21 * 8192 : 20 * 8192;
    auto fill_kv = [&]() {
      int t0 = qb * 8 - 8 + 4 * j; t0 = t0 < 0 ? t0 + 8 : t0;
      dma_lin4_sc1(lds0 + L_K + j * 4096, ws + WS_K + (size_t)(head * 64 + t0) * 1024, vl);
      dma_lin4_sc1(lds0 + L_V + j * 4096, ws + WS_V + (size_t)(head * 64 + t0) * 1024, vl);
    };
    int nwh = 0;
    auto fill_wh = [&]() { dma_wh(m0b + so, nwh == 0 ? w2p + w2half : w2p + (size_t)(nwh - 1) * 64 * 4096, vw0, vw1); ++nwh; NEXT_SO(); };
    unsigned so_g2 = 0;

    if (hasQ) {
      const unsigned x0 = (lane >> 3) * 1024 + (((lane & 7) ^ (lane >> 4)) << 4), x1 = (8 + (lane >> 3)) * 1024 + (((lane & 7) ^ (4 + (lane >> 4))) << 4) - 1024;
      const unsigned q0 = (lane >> 4) * 3072 + (((lane & 15) ^ ((lane >> 4) << 2)) << 4), q1 = q0 + 4 * 3072 - 1024;
      const char* xp = x + (size_t)(tm * 128 + (j >> 1) * 64 + (j & 1) * 16) * 1024;
      const char* qp = wqkv + (size_t)(j * 8) * 3072 + tn * 256 - 2048;
      int kc = QROT ? ((idx / 6 + idx % 6) % 3) * 3 : 0;
      auto fill_a = [&]() { dma_2x2(m0b + so, xp + kc * 128, x0, x1, qp + (size_t)kc * 64 * 3072, q0, q1); NEXT_SO(); };
      auto fill_b = [&]() { dma_2x2(m0b + so, xp + kc * 128 + 32 * 1024, x0, x1, qp + (size_t)kc * 64 * 3072 + 32 * 3072, q0, q1); kc = (kc + 1) & 7; NEXT_SO(); };
#pragma unroll 1
      for (int c = 0; c < 3; ++c) { fill_a(); fill_b(); }
#pragma unroll 1
      for (int c = 0; c < 5; ++c) { VBAR(20); fill_a(); VBAR(20); fill_b(); }
      if (hasG1) {
        const unsigned so0 = so;
        VBAR(20); fill_g1w(); VBAR(18); fill_g1w(); VBAR(16); fill_g1w(); VBAR(14); fill_g1w();
        VBAR(12); fill_g1h4(so0); fill_g1();
        VBAR(20); fill_g1();
      } else {
        VBAR(20); VBAR(16); VBAR(12); VBAR(8); VBAR(4); VBAR(0);
      }
    } else {
      BAR();
      VBAR(0);
#pragma unroll 1
      for (int s = 0; s < 4; ++s) fill_g1w();
      if (j == 0) spin1(rws, fh_off, token);
      else if (PREFETCH & 2) {
        const int pw = lnb * 3 + (j - 1);
#pragma unroll 1
        for (int i = pw; i < 752; i += 192) dma_touch(lds0 + L_P + 1024, i < 688 ? w2 + (size_t)i * 8192 : wo + (size_t)(i - 688) * 8192, lane * 128);
      }
      BAR();
      fill_g1h4(0); fill_g1(); fill_g1();
    }
    STAMP(1);
    if (hasG1) {
      VBAR(14); fill_g1(); VBAR(16); fill_g1(); VBAR(18); fill_g1();
#pragma unroll 1
      for (int s = 3; s < 19; ++s) { VBAR(20); fill_g1(); }
      fill_kv();
#pragma unroll 1
      for (int s = 19; s < 26; ++s) { VBAR(20); fill_g1(); }
      so_g2 = so;
      VBAR(20); fill_wh(); VBAR(18); fill_wh(); VBAR(16); fill_wh(); VBAR(14); fill_wh(); VBAR(12); fill_wh(); VBAR(10); fill_wh();
      BAR(); fill_wh();
      BAR();
      BAR();
    } else {
      BAR();
      so_g2 = so;
#pragma unroll 1
      for (int i = 0; i < 7; ++i) fill_wh();
      if (j == 0) spin1(rws, fq_off, token);
      BAR();
      fill_kv();
      VBAR(0);
    }
    STAMP(2);
    {
      if (j == 0) { spin1(rws, fu_off, token); *relay = 1u; }
      else { for (unsigned spins = 0; spins < 64 * SPIN_CAP && *relay == 0u; ++spins) __builtin_amdgcn_s_sleep(1); }
      STAMP(3);
      so = so_g2;
      dma_fh(m0b + so, up + uhalf, vl); NEXT_SO();
#pragma unroll 1
      for (int i = 0; i < 6; ++i) { dma_fh(m0b + so, up, vl); up += 8192; NEXT_SO(); }
    }
    w2p += (size_t)6 * 64 * 4096;
    unsigned wstr = 64 * 4096;
    int ci = 7;
    auto fill_g2 = [&]() {
      if (ci == 22) {
        w2p = wo + (size_t)(j * 16) * 1024 + dt * 128; up = ws + WS_O + (size_t)st * 32 * 2048 + j * 2048 - 2048; wstr = 64 * 1024;
        vw0 = (lane >> 3) * 1024 + swz; vw1 = vw0 + 8 * 1024 - 1024;
      }
      dma_wh(m0b + so, w2p, vw0, vw1); dma_fh(m0b + so, up, vl);
      w2p += wstr; up += 8192; NEXT_SO(); ++ci;
    };
    VBAR(12);
    VBAR(10); fill_g2(); VBAR(12); fill_g2(); VBAR(14); fill_g2(); VBAR(16); fill_g2(); VBAR(18); fill_g2();
#pragma unroll 1
    for (int s = 6; s < 24; ++s) { VBAR(20); fill_g2(); }
    const unsigned fa_off = WS_FL + FL_O * 4 + (j * 64 + lane) * 128;
    if (lead) peek_issue(lds0 + L_X + j * 1024, ws, fa_off);
    VBAR(20); VBAR(16); VBAR(12); VBAR(8); VBAR(4); VBAR(0);
    if (lead) peek_check(L + L_X + j * 1024 + lane * 16, rws, fa_off, token);
    BAR();
    if (lead && j == 0 && lane == 0) st32(rws, WS_FL + FL_EPOCH * 4, token);
    BAR();
    STAMP(4);
    if (j == 0) STAMP_OUT(ws, 1);
    return;
  }

#undef token
  unsigned token;
  const int r = lane & 31, h = lane >> 5;
  const int g4 = lane >> 4, q4 = (lane & 15) >> 2, p4 = lane & 3, blk = (lane >> 4) & 1;
  int so = 0;
  if (CODEPF && (idx == 0 || idx == 12) && w < CODEPF) dma_touch(lds0 + L_P + 1024, pc0 + w * 8192, lane * 128);
  if (threadIdx.x == 4) *relay = 0u;
  if (threadIdx.x < 4) {
    const unsigned cv = (threadIdx.x & 1) ? 0u : 0x3f803f80u;
    *(LAS u32x4*)(L + L_C + (threadIdx.x >> 1) * 512 + (threadIdx.x & 1) * 16) = u32x4{cv, cv, cv, cv};
  }
  lptr pk = L + L_P + lane * 16;

  if (hasQ) {
    token = TOKEN_OF(epoch);
    asm volatile("" : "+v"(token));
    if (w == 7) *(LAS unsigned*)pk = ~token;
    STAMP(1);
    f32x16 acc[2] = {};
    const bool poller = w == 7 && hasG1;
    const unsigned post = poller ? (1u << 8) | (1u << 10) | (1u << 12) : 0u;
    if (PREFETCH & 1) {
      const int pi = (xcd * 24 + idx) * 4 + w;
      if (w < 4 && pi < 688) dma_touch(lds0 + L_P + 1024, w1 + (size_t)pi * 8192, lane * 128);
    }
    auto hook = [&](int s) {
      LBAR();
      if (RARE((post >> s) & 1)) peek_issue(lds0 + L_P, ws, fh_off);
      SB();
    };
    auto hook14 = [&]() { if (poller) peek_check(pk, rws, fh_off, token); LBAR(); };
    if (tn >= 8) qkv_consume<true>(L, so, w, lane, acc, hook, hook14);
    else qkv_consume<false>(L, so, w, lane, acc, hook, hook14);
    STAMP(2);
    const int wm = w >> 2, wn = w & 3;
    const int hd0 = (tn & 3) * 8 + wn * 2;
#pragma unroll
    for (int mi = 0; mi < 2; ++mi) {
      const int tb = tm * 4 + wm * 2 + mi;
      const float sc = tn < 4 ? QSCALE : 1.0f;
      const unsigned dst = tn >= 8 ? WS_V + (((hd0 + (r >> 4)) * 64 + tb) * 64 + h * 16 + (r & 15)) * 16
                                   : (tn < 4 ? WS_Q : WS_K) + ((hd0 * 64 + tb) * 64 + lane) * 16;
      const unsigned dstep = tn >= 8 ? 512 : 65536;
#pragma unroll
      for (int s = 0; s < 2; ++s) {
        u32x4 v;
#pragma unroll
        for (int jj = 0; jj < 4; ++jj) v[jj] = pk2(acc[mi][8 * s + 2 * jj] * sc, acc[mi][8 * s + 2 * jj + 1] * sc);
        st128(rws, dst + s * dstep, v);
      }
    }
  } else {
    constexpr int RS = 1056;
    u32x4 xv[4], gv[4];
#pragma unroll
    for (int i = 0; i < 4; ++i) xv[i] = *(const u32x4*)(x + (size_t)(lnb * 32 + w * 4 + g4) * 1024 + (lane & 15) * 16 + i * 256);
#pragma unroll
    for (int i = 0; i < 4; ++i) gv[i] = *(const u32x4*)(gw + (lane & 15) * 16 + i * 256);
    f32x2 v[16], s2 = {0.f, 0.f}, g2 = {0.f, 0.f};
#pragma unroll
    for (int i = 0; i < 16; ++i) {
      v[i] = f32x2{bflo(xv[i >> 2][i & 3]), bfhi(xv[i >> 2][i & 3])};
      s2 += v[i];
      g2 += v[i] * f32x2{bflo(gv[i >> 2][i & 3]), bfhi(gv[i >> 2][i & 3])};
    }
    const float mean = row16_sum(s2[0] + s2[1]) * (1.0f / 512), gs = row16_sum(g2[0] + g2[1]);
    f32x2 q2 = {0.f, 0.f};
#pragma unroll
    for (int i = 0; i < 16; ++i) { v[i] -= f32x2{mean, mean}; q2 += v[i] * v[i]; }
    const float rs = __builtin_amdgcn_rsqf(row16_sum(q2[0] + q2[1]) * (1.0f / 512) + 1e-5f);
#pragma unroll
    for (int i = 0; i < 4; ++i) {
      u32x4 ov;
#pragma unroll
      for (int jj = 0; jj < 4; ++jj) { const f32x2 t = v[4 * i + jj] * f32x2{rs, rs}; ov[jj] = pk2(t[0], t[1]); }
      *(LAS u32x4*)(L + (w * 4 + g4) * RS + (lane & 15) * 16 + i * 256) = ov;
    }
    token = TOKEN_OF(epoch);
    asm volatile("" : "+v"(token));
    if ((lane & 15) == 0) st32(rws, WS_G + (lnb * 32 + w * 4 + g4) * 4, __float_as_uint(1.0f / (1.0f + __expf(-gs))));
    STAMP(1);
    LBAR();
#pragma unroll
    for (int i = 0; i < 4; ++i) {
      const int D = w * 4 + i, d48 = D / 3;
      lptr a = L + (4 * g4 + q4) * RS + D * 32 + p4 * 8;
      const bf16x8 f = cat8(ldstr(a), ldstr(a + 16 * RS));
      st128(rws, WS_H + ((d48 * 64 + lnb) * 3 + (D - d48 * 3)) * 1024 + lane * 16, __builtin_bit_cast(u32x4, f));
      if (i == 3 && w == 7) st128(rws, WS_H + ((10 * 64 + lnb) * 3 + 2) * 1024 + lane * 16, __builtin_bit_cast(u32x4, f));
    }
    DBAR();
    if (threadIdx.x == 0) flag_st(ws, FL_H + lnb * 32, token);
    STAMP(2);
    LBAR();
  }

  bf16x8 qf;
  if (hasG1) {
    const int kh = w >> 2, fb = w & 3;
    int ha[3];
#pragma unroll
    for (int db = 0; db < 3; ++db) { const int o = (kh * 3 + db) * 1024 + lane * 16, rg = o / 1536; ha[db] = rg * 4096 + 2048 + (o - rg * 1536); }
    const int wa = kh * 8192 + (4 * g4 + q4) * 128 + ((fb ^ ((2 * g4 + (q4 >> 1)) & 3)) << 5) + p4 * 8;
    f32x4 acc[3] = {};
    bf16x8 wA, hA[3], wB, hB[3];
#define G1RD(W, H) do { lptr sl = L + so; NEXT_SO(); W = cat8(ldstr(sl + wa), ldstr(sl + wa + 4096)); \
    _Pragma("unroll") for (int db = 0; db < 3; ++db) H[db] = ldsr128(sl + ha[db]); SB(); } while (0)
#define G1MM(W, H) do { _Pragma("unroll") for (int db = 0; db < 3; ++db) acc[db] = MFMA16(W, H[db], acc[db]); SB(); } while (0)
    const unsigned pre = (1u << 6) | (w == 7 ? 1u << 18 : 0u);
    const unsigned post = (w == 0 && hasQ ? 1u << 6 : 0u) | (w == 7 ? (1u << 6) | (1u << 12) | (1u << 14) | (1u << 16) : 0u);
    auto hook = [&](int s) {
      if (RARE((pre >> s) & 1)) { if (s == 6) asm volatile("s_waitcnt vmcnt(0)" ::: "memory"); else peek_check(pk, rws, fq_off, token); }
      LBAR();
      if (RARE((post >> s) & 1)) {
        if (s == 6) { if (w == 7) *(LAS unsigned*)pk = ~token; else if (lane == 0) flag_st(ws, FL_Q + (tn * 16 + tm) * 32, token); }
        else peek_issue(lds0 + L_P, ws, fq_off);
      }
      SB();
    };
    LBAR(); G1RD(wA, hA);
#pragma unroll 1
    for (int s = 2; s < 20; s += 2) {
      LBAR();  G1RD(wB, hB); G1MM(wA, hA);
      hook(s); G1RD(wA, hA); G1MM(wB, hB);
    }
    STAMP(10);
    qf = __builtin_bit_cast(bf16x8, ld128(rws, WS_Q + (head * 64 + qb * 8 + w) * 1024 + lane * 16));
    const int fblk = g1ft * 4 + fb;
    u32x2 bb = *(const u32x2*)(b1 + min(fblk * 16 + 4 * g4, 1372) * 2);
    unsigned ua[2];
#pragma unroll
    for (int e = 0; e < 2; ++e) {
      const int D = g1dt * 3 + (kh ? 2 : e);
      ua[e] = WS_U + ((((D >> 2) * 86 + fblk) * 2 + ((D >> 1) & 1)) * 64 + (g4 >> 1) * 32 + (D & 1) * 16 + (lane & 15)) * 16 + (g4 & 1) * 8;
      if (fblk >= 86 || D >= 32) ua[e] = WS_END;
    }
    lptr xw = L + L_X + (fb * 3 * 64 + lane) * 16;
#pragma unroll 1
    for (int s = 20; s < 32; s += 2) {
      LBAR(); G1RD(wB, hB); G1MM(wA, hA);
      LBAR(); G1RD(wA, hA); G1MM(wB, hB);
    }
    asm volatile("" : "+v"(qf), "+v"(bb), "+v"(ua[0]), "+v"(ua[1]));
    const f32x4 bv = {bflo(bb[0]), bfhi(bb[0]), bflo(bb[1]), bfhi(bb[1])};
    LBAR(); G1RD(wB, hB); G1MM(wA, hA);
    LBAR();
    G1MM(wB, hB);
    STAMP(3);
    if (kh == 1) { *(LAS f32x4*)(xw) = acc[0]; *(LAS f32x4*)(xw + 1024) = acc[1]; }
    else *(LAS f32x4*)(xw + 2048) = acc[2];
    LBAR();
    auto fin = [&](const f32x4& a, int off, unsigned addr) {
      const f32x4 t = a + *(const LAS f32x4*)(xw + off) + bv;
      st64(rws, addr, u32x2{pk2(gelu_tanh(t[0]), gelu_tanh(t[1])), pk2(gelu_tanh(t[2]), gelu_tanh(t[3]))});
    };
    if (kh == 1) fin(acc[2], 2048, ua[0]);
    else { fin(acc[0], 0, ua[0]); fin(acc[1], 1024, ua[1]); }
  } else {
    DBAR();
    if (threadIdx.x == 0) flag_st(ws, FL_Q + (tn * 16 + tm) * 32, token);
    LBAR();
    qf = __builtin_bit_cast(bf16x8, ld128(rws, WS_Q + (head * 64 + qb * 8 + w) * 1024 + lane * 16));
    asm volatile("" : "+v"(qf));
    LBAR();
    STAMP(3);
  }

  {
    lptr kp = L + L_K + w * 1024 + lane * 16;
    const int i_lo = qb > 0 ? 1 : 8 - w;
    const f32x16 zero = {};
    bf16x8 kf[9];
#pragma unroll
    for (int i = 0; i < 9; ++i) kf[i] = ldsr128(kp + i * 1024);
    f32x16 sb, sa, sc;
    float mx[4];
#define TMAX(S, i) (((i) >= i_lo) ? max16(S, -INFINITY) : -INFINITY)
    sa = MFMA32(kf[8], qf, zero); sc = MFMA32(kf[0], qf, zero);
    f32x16 s1 = MFMA32(kf[1], qf, zero), s2 = MFMA32(kf[2], qf, zero); SB();
#pragma unroll
    for (int i = 0; i < 16; ++i) sb[i] = __builtin_amdgcn_inverse_ballot_w64(diag_mask(i)) ? sa[i] : (qb > 0 ? sc[i] : -INFINITY);
    SB();
    sa = MFMA32(kf[3], qf, zero); sc = MFMA32(kf[4], qf, zero); SB();
    mx[0] = max16(sb, -INFINITY); mx[1] = TMAX(s1, 1); mx[2] = TMAX(s2, 2); SB();
    s1 = MFMA32(kf[5], qf, zero); s2 = MFMA32(kf[6], qf, zero); SB();
    mx[3] = TMAX(sa, 3); mx[0] = __builtin_fmaxf(mx[0], TMAX(sc, 4)); SB();
    sa = MFMA32(kf[7], qf, zero); SB();
    mx[1] = __builtin_fmaxf(mx[1], TMAX(s1, 5)); mx[2] = __builtin_fmaxf(mx[2], TMAX(s2, 6)); mx[3] = __builtin_fmaxf(mx[3], TMAX(sa, 7));
    float m = __builtin_fmaxf(__builtin_fmaxf(mx[0], mx[1]), __builtin_fmaxf(mx[2], mx[3]));
    {
      auto rr = __builtin_amdgcn_permlane32_swap(__float_as_uint(m), __float_as_uint(m), false, false);
      m = __builtin_fmaxf(__uint_as_float(rr[0]), __uint_as_float(rr[1]));
    }
    STAMP(11);
    if (hasG1) {
      DBAR();
      if (threadIdx.x == 0) flag_st(ws, FL_U + (g1dt * 32 + g1ft) * 32, token);
    }
    STAMP(4);
    const int vstr = r < 16 ? 1024 : 0;
    lptr vp0 = L + (r < 16 ? L_V + w * 1024 + h * 256 + r * 16 : (L_C + ((r == 16 || r == 20) ? 0 : 16)));
    f32x16 cin;
#pragma unroll
    for (int i = 0; i < 16; ++i) cin[i] = -m;
    f32x16 s = MFMA32(ldsr128(kp + i_lo * 1024), qf, cin);
    f32x16 o = {};
    {
      u32x4 pd[2], pf[2];
#pragma unroll
      for (int i = 0; i < 16; i += 2) {
        const unsigned pp = pk2(__builtin_amdgcn_exp2f(sb[i] - m), __builtin_amdgcn_exp2f(sb[i + 1] - m));
        const int dlt = r - crow(i, h);
        const unsigned mk = dlt >= 1 ? 0xffffffffu : (dlt == 0 ? 0x0000ffffu : 0u);
        pd[i >> 3][(i >> 1) & 3] = pp & mk;
        pf[i >> 3][(i >> 1) & 3] = pp & ~mk;
      }
      lptr vp = vp0 + 8 * vstr;
      o = MFMA32(ldsr128(vp), __builtin_bit_cast(bf16x8, pd[0]), o);
      o = MFMA32(ldsr128(vp + 512), __builtin_bit_cast(bf16x8, pd[1]), o);
      o = MFMA32(ldsr128(vp0), __builtin_bit_cast(bf16x8, pf[0]), o);
      o = MFMA32(ldsr128(vp0 + 512), __builtin_bit_cast(bf16x8, pf[1]), o);
    }
#pragma unroll 1
    for (int i = i_lo; i < 8; ++i) {
      lptr vp = vp0 + i * vstr;
      const bf16x8 kn = ldsr128(kp + (i + 1) * 1024), v0 = ldsr128(vp), v1 = ldsr128(vp + 512);
      u32x4 p0, p1;
#pragma unroll
      for (int e = 0; e < 8; e += 2) p0[e >> 1] = pk2(__builtin_amdgcn_exp2f(s[e]), __builtin_amdgcn_exp2f(s[e + 1]));
      o = MFMA32(v0, __builtin_bit_cast(bf16x8, p0), o);
#pragma unroll
      for (int e = 8; e < 16; e += 2) p1[(e >> 1) & 3] = pk2(__builtin_amdgcn_exp2f(s[e]), __builtin_amdgcn_exp2f(s[e + 1]));
      s = MFMA32(kn, qf, cin);
      o = MFMA32(v1, __builtin_bit_cast(bf16x8, p1), o);
    }
    const float il = __builtin_amdgcn_rcpf(o[8]);
    u32x4 ov;
#pragma unroll
    for (int jj = 0; jj < 4; ++jj) ov[jj] = pk2(o[2 * jj] * il, o[2 * jj + 1] * il);
    const int B = qb * 8 + w;
    st128(rws, WS_O + (((B >> 1) * 32 + head) * 2 + (B & 1)) * 1024 + lane * 16, ov);
  }
  STAMP(5);

  {
    const int kh = w >> 2, mt = (w >> 1) & 1, nt = w & 1;
    const int tok = st * 64 + nt * 32 + r;
    const int ta2 = kh * 8192 + (8 * h + q4) * 128 + (((nt * 4 + blk * 2 + (p4 >> 1)) ^ ((q4 >> 1) << 2)) << 4) + (p4 & 1) * 8;
    const int tao = kh * 8192 + (4 * h + q4) * 128 + (((mt * 4 + blk * 2 + (p4 >> 1)) ^ ((q4 >> 1) << 2)) << 4) + (p4 & 1) * 8;
    const int fa2 = kh * 8192 + 2048 + mt * 1024 + lane * 16;
    const int fao = kh * 8192 + 2048 + nt * 1024 + lane * 16;
    f32x16 ay = {}, aa = {};
    bf16x8 pa0, pb0, pa1, pb1, qa0, qb0, qa1, qb1;
    float g = 0.f; unsigned b2v = 0;
#define RD2(A0, B0, A1, B1) do { lptr sl = L + so; NEXT_SO(); B0 = cat8(ldstr(sl + ta2), ldstr(sl + ta2 + 512)); A0 = ldsr128(sl + fa2); \
    B1 = cat8(ldstr(sl + ta2 + 4096), ldstr(sl + ta2 + 4096 + 512)); A1 = ldsr128(sl + fa2 + 4096); SB(); } while (0)
#define RDO(A0, B0, A1, B1) do { lptr sl = L + so; NEXT_SO(); A0 = cat8(ldstr(sl + tao), ldstr(sl + tao + 1024)); B0 = ldsr128(sl + fao); \
    A1 = cat8(ldstr(sl + tao + 4096), ldstr(sl + tao + 4096 + 1024)); B1 = ldsr128(sl + fao + 4096); SB(); } while (0)
#define MM2(ACC, A0, B0, A1, B1) do { ACC = MFMA32(A0, B0, ACC); ACC = MFMA32(A1, B1, ACC); SB(); } while (0)
    const unsigned pre = (1u << 4) | (w == 7 ? 1u << 16 : 0u);
    const unsigned post = (w == 0 ? 1u << 4 : 0u) | (w == 7 ? (1u << 4) | (1u << 10) | (1u << 12) | (1u << 14) : 0u);
    auto hook = [&](int s) {
      if (RARE((pre >> s) & 1)) { if (s == 4) asm volatile("s_waitcnt vmcnt(0)" ::: "memory"); else peek_check(pk, rws, fo_off, token); }
      LBAR();
      if (RARE((post >> s) & 1)) {
        if (s == 4) { if (w == 7) *(LAS unsigned*)pk = ~token; else if (lane == 0) flag_st(ws, FL_O + (qb * 32 + head) * 32, token); }
        else peek_issue(lds0 + L_P, ws, fo_off);
      }
      SB();
    };
    LBAR(); STAMP(6);
    RD2(pa0, pb0, pa1, pb1);
    if (kh) { pa0 = bf16x8{}; pa1 = bf16x8{}; pb0 = bf16x8{}; pb1 = bf16x8{}; }
#pragma unroll 1
    for (int s = 2; s < 22; s += 2) {
      LBAR();  RD2(qa0, qb0, qa1, qb1); MM2(ay, pa0, pb0, pa1, pb1);
      hook(s); RD2(pa0, pb0, pa1, pb1); MM2(ay, qa0, qb0, qa1, qb1);
    }
    g = __uint_as_float(ld32(rws, WS_G + tok * 4));
    b2v = ((const unsigned short*)b2)[tok];
    LBAR(); RD2(qa0, qb0, qa1, qb1); MM2(ay, pa0, pb0, pa1, pb1);
    STAMP(7);
    LBAR(); RDO(pa0, pb0, pa1, pb1); MM2(ay, qa0, qb0, qa1, qb1);
#pragma unroll 1
    for (int s = 23; s < 29; s += 2) {
      LBAR(); RDO(qa0, qb0, qa1, qb1); MM2(aa, pa0, pb0, pa1, pb1);
      LBAR(); RDO(pa0, pb0, pa1, pb1); MM2(aa, qa0, qb0, qa1, qb1);
    }
    LBAR(); RDO(qa0, qb0, qa1, qb1); MM2(aa, pa0, pb0, pa1, pb1);
    MM2(aa, qa0, qb0, qa1, qb1);
    STAMP(8);
    const float og = 1.0f - g;
    float v[16];
#pragma unroll
    for (int i = 0; i < 16; ++i) v[i] = ay[i] * og + aa[i] * g;
    LBAR();
    {
      lptr ex = L + w * 2048 + lane * 16;
      f32x4 s0, s1;
#pragma unroll
      for (int jj = 0; jj < 4; ++jj) { s0[jj] = kh ? v[jj] : v[8 + jj]; s1[jj] = kh ? v[4 + jj] : v[12 + jj]; }
      *(LAS f32x4*)(ex) = s0; *(LAS f32x4*)(ex + 1024) = s1;
    }
    LBAR();
    {
      lptr ex = L + (w ^ 4) * 2048 + lane * 16;
      const f32x4 r0 = *(const LAS f32x4*)(ex), r1 = *(const LAS f32x4*)(ex + 1024);
      const float bo = bflo(b2v) * og;
      float f[8];
#pragma unroll
      for (int jj = 0; jj < 4; ++jj) { f[jj] = (kh ? v[8 + jj] : v[jj]) + r0[jj] + bo; f[4 + jj] = (kh ? v[12 + jj] : v[4 + jj]) + r1[jj] + bo; }
      const unsigned a0 = pk2(f[0], f[1]), a1 = pk2(f[2], f[3]), c0 = pk2(f[4], f[5]), c1 = pk2(f[6], f[7]);
      auto x0 = __builtin_amdgcn_permlane32_swap(a0, c0, false, false);
      auto x1 = __builtin_amdgcn_permlane32_swap(a1, c1, false, false);
      const u32x4 ov = {x0[0], x1[0], x0[1], x1[1]};
      *(u32x4*)(out + (size_t)tok * 1024 + (dt * 64 + mt * 32 + kh * 16 + h * 8) * 2) = ov;
    }
  }
  STAMP(9);
  if (w == 0) STAMP_OUT(ws, 0);
}

extern "C" void kernel_launch(void* const* d_in, const int* in_sizes, int n_in, void* d_out, int out_size, void* d_ws, size_t ws_size,
                              hipStream_t stream) {
  (void)hipFuncSetAttribute((const void*)fused_kernel, hipFuncAttributeMaxDynamicSharedMemorySize, LDS_BYTES);
  fused_kernel<<<256, 768, LDS_BYTES, stream>>>((const char*)d_in[0], (const char*)d_in[1], (const char*)d_in[2], (const char*)d_in[4],
                                                (const char*)d_in[5], (const char*)d_in[6], (const char*)d_in[7], (const char*)d_in[8],
                                                (char*)d_ws, (char*)d_out);
}
```

```cpp
#include <hip/hip_runtime.h>
#include <hip/hip_bf16.h>
#include <stdint.h>

#define LAS __attribute__((address_space(3)))
#define GAS __attribute__((address_space(1)))
#define DI __device__ __forceinline__
typedef __attribute__((ext_vector_type(8))) __bf16 bf16x8;
typedef __attribute__((ext_vector_type(2))) __bf16 bf16x2;
typedef __attribute__((ext_vector_type(4))) short s16x4;
typedef __attribute__((ext_vector_type(2))) float f32x2;
typedef __attribute__((ext_vector_type(4))) float f32x4;
typedef __attribute__((ext_vector_type(16))) float f32x16;
typedef __attribute__((ext_vector_type(2))) unsigned u32x2;
typedef __attribute__((ext_vector_type(4))) unsigned u32x4;
typedef LAS char* lptr;
typedef __amdgpu_buffer_rsrc_t rsrc_t;

constexpr unsigned WS_Q = 0, WS_K = 2u << 20, WS_V = 4u << 20, WS_O = 6u << 20, WS_U = 8u << 20, WS_H = 10u << 20,
                   WS_G = 13u << 20, WS_FL = 14u << 20, WS_END = 15u << 20;
constexpr int FL_H = 0;
constexpr int FL_Q = 64 * 32;
constexpr int FL_U = FL_Q + 192 * 32;
constexpr int FL_O = FL_U + 352 * 32;
constexpr int FL_EPOCH = FL_O + 256 * 32;
constexpr float QSCALE = 0.25f * 1.4426950408889634f;
constexpr int NS = 7, SLOT = 16384, RING = NS * SLOT;
constexpr int L_K = RING, L_V = RING + 16384, L_X = RING + 32768  , L_C = L_X + 12288  , L_P = L_C + 1024  ;
constexpr int LDS_BYTES = 163840;
constexpr unsigned SPIN_CAP = 200000;
#define CODEPF 0
#define PREFETCH 3
#define PFSTEP 1
#define QROT 1

#define MFMA32(a, b, c) __builtin_amdgcn_mfma_f32_32x32x16_bf16(a, b, c, 0, 0, 0)
#define MFMA16(a, b, c) __builtin_amdgcn_mfma_f32_16x16x32_bf16(a, b, c, 0, 0, 0)
#define SB() __builtin_amdgcn_sched_barrier(0)
#define LBAR() do { asm volatile("s_waitcnt lgkmcnt(0)\n\ts_barrier" ::: "memory"); SB(); } while (0)
#define CBAR(N) do { asm volatile("s_waitcnt lgkmcnt(" #N ")\n\ts_barrier" ::: "memory"); SB(); } while (0)
#define DBAR() do { asm volatile("s_waitcnt vmcnt(0) lgkmcnt(0)\n\ts_barrier" ::: "memory"); SB(); } while (0)
#define VBAR(N) asm volatile("s_waitcnt vmcnt(" #N ")\n\ts_barrier" ::: "memory")
#define BAR() asm volatile("s_barrier" ::: "memory")

#define STAMP(i) do {} while (0)
#define STAMP_OUT(ws, slot) do {} while (0)

DI int rfl(int x) { return __builtin_amdgcn_readfirstlane(x); }
DI bf16x8 ldsr128(lptr p) { return *(const LAS bf16x8*)p; }
DI s16x4 ldstr(lptr p) { return __builtin_amdgcn_ds_read_tr16_b64_v4i16((LAS s16x4*)p); }
DI bf16x8 cat8(s16x4 a, s16x4 b) { return __builtin_bit_cast(bf16x8, __builtin_shufflevector(a, b, 0, 1, 2, 3, 4, 5, 6, 7)); }
DI unsigned pk2(float a, float b) { f32x2 f = {a, b}; return __builtin_bit_cast(unsigned, __builtin_convertvector(f, bf16x2)); }
DI float bflo(unsigned u) { return __uint_as_float(u << 16); }
DI float bfhi(unsigned u) { return __uint_as_float(u & 0xffff0000u); }
DI int crow(int i, int h) { return (i & 3) + 8 * (i >> 2) + 4 * h; }
DI void st128(rsrc_t r, unsigned off, u32x4 v) { __builtin_amdgcn_raw_buffer_store_b128(v, r, off, 0, 16); }
DI void st64(rsrc_t r, unsigned off, u32x2 v) { __builtin_amdgcn_raw_buffer_store_b64(v, r, off, 0, 16); }
DI void st32(rsrc_t r, unsigned off, unsigned v) { __builtin_amdgcn_raw_buffer_store_b32(v, r, off, 0, 16); }
DI u32x4 ld128(rsrc_t r, unsigned off) { return __builtin_amdgcn_raw_buffer_load_b128(r, off, 0, 16); }
DI unsigned ld32(rsrc_t r, unsigned off) { return __builtin_amdgcn_raw_buffer_load_b32(r, off, 0, 16); }
DI unsigned flag_ld(const char* ws, int idx) { return __hip_atomic_load((const GAS unsigned*)(ws + WS_FL) + idx, __ATOMIC_RELAXED, __HIP_MEMORY_SCOPE_AGENT); }
DI void flag_st(char* ws, int idx, unsigned v) { __hip_atomic_store((GAS unsigned*)(ws + WS_FL) + idx, v, __ATOMIC_RELAXED, __HIP_MEMORY_SCOPE_AGENT); }

#define M0_IN "s_mov_b32 %0, m0\n\ts_mov_b32 m0, %1\n\ts_nop 0\n\t"
#define M0_OUT "s_mov_b32 m0, %0"
DI void dma_2x2(unsigned m0v, const void* b0, unsigned v0, unsigned v1, const void* b1, unsigned v2, unsigned v3) {
  unsigned sv;
  asm volatile(M0_IN "global_load_lds_dwordx4 %4, %2\n\tglobal_load_lds_dwordx4 %5, %2 offset:1024\n\t"
               "global_load_lds_dwordx4 %6, %3 offset:2048\n\tglobal_load_lds_dwordx4 %7, %3 offset:3072\n\t" M0_OUT
               : "=&s"(sv) : "s"(m0v), "s"(b0), "s"(b1), "v"(v0), "v"(v1), "v"(v2), "v"(v3) : "memory");
}
DI void dma_lin4_sc1(unsigned m0v, const void* b, unsigned vl) {
  unsigned sv;
  asm volatile(M0_IN "global_load_lds_dwordx4 %3, %2 sc1\n\tglobal_load_lds_dwordx4 %3, %2 offset:1024 sc1\n\t"
               "global_load_lds_dwordx4 %3, %2 offset:2048 sc1\n\tglobal_load_lds_dwordx4 %3, %2 offset:3072 sc1\n\t" M0_OUT
               : "=&s"(sv) : "s"(m0v), "s"(b), "v"(vl) : "memory");
}
DI void dma_g1(unsigned m0v, const void* wb, unsigned v0, unsigned v1, const void* hb_m2048, unsigned vl) {
  unsigned sv;
  asm volatile(M0_IN "global_load_lds_dwordx4 %4, %2\n\tglobal_load_lds_dwordx4 %5, %2 offset:1024\n\t"
               "global_load_lds_dwordx4 %6, %3 offset:2048 sc1\n\t"
               "s_mov_b32 exec_hi, 0\n\tglobal_load_lds_dwordx4 %6, %3 offset:3072 sc1\n\ts_mov_b32 exec_hi, -1\n\t" M0_OUT
               : "=&s"(sv) : "s"(m0v), "s"(wb), "s"(hb_m2048), "v"(v0), "v"(v1), "v"(vl) : "memory");
}
DI void dma_hh(unsigned m0v, const void* hb_m2048, unsigned vl) {
  unsigned sv;
  asm volatile(M0_IN "global_load_lds_dwordx4 %3, %2 offset:2048 sc1\n\t"
               "s_mov_b32 exec_hi, 0\n\tglobal_load_lds_dwordx4 %3, %2 offset:3072 sc1\n\ts_mov_b32 exec_hi, -1\n\t" M0_OUT
               : "=&s"(sv) : "s"(m0v), "s"(hb_m2048), "v"(vl) : "memory");
}
DI void dma_lin4(unsigned m0v, const void* b, unsigned vl) {
  unsigned sv;
  asm volatile(M0_IN "global_load_lds_dwordx4 %3, %2\n\tglobal_load_lds_dwordx4 %3, %2 offset:1024\n\t"
               "global_load_lds_dwordx4 %3, %2 offset:2048\n\tglobal_load_lds_dwordx4 %3, %2 offset:3072\n\t" M0_OUT
               : "=&s"(sv) : "s"(m0v), "s"(b), "v"(vl) : "memory");
}
DI void dma_touch(unsigned m0v, const void* b, unsigned voff) {
  unsigned sv;
  asm volatile(M0_IN "global_load_lds_dword %3, %2\n\t" M0_OUT : "=&s"(sv) : "s"(m0v), "s"(b), "v"(voff) : "memory");
}
DI void dma_wh(unsigned m0v, const void* wb, unsigned v0, unsigned v1) {
  unsigned sv;
  asm volatile(M0_IN "global_load_lds_dwordx4 %3, %2\n\tglobal_load_lds_dwordx4 %4, %2 offset:1024\n\t" M0_OUT
               : "=&s"(sv) : "s"(m0v), "s"(wb), "v"(v0), "v"(v1) : "memory");
}
DI void dma_fh(unsigned m0v, const void* fb_m2048, unsigned vl) {
  unsigned sv;
  asm volatile(M0_IN "global_load_lds_dwordx4 %3, %2 offset:2048 sc1\n\tglobal_load_lds_dwordx4 %3, %2 offset:3072 sc1\n\t" M0_OUT
               : "=&s"(sv) : "s"(m0v), "s"(fb_m2048), "v"(vl) : "memory");
}

DI void spin1(rsrc_t r, unsigned off, unsigned tok) {
  unsigned n = 0;
#pragma nounroll
  do { const unsigned v = ld32(r, off); if (__all(v == tok)) break; __builtin_amdgcn_s_sleep(4); asm volatile("" ::: "memory"); } while (++n < SPIN_CAP);
  __builtin_amdgcn_fence(__ATOMIC_ACQUIRE, "wavefront");
}
DI void peek_issue(unsigned m0v, const void* ws, unsigned off) {
  unsigned sv;
  asm volatile(M0_IN "global_load_lds_dwordx4 %3, %2 sc1\n\t" M0_OUT : "=&s"(sv) : "s"(m0v), "s"(ws), "v"(off) : "memory");
}
DI void peek_check(lptr p, rsrc_t r, unsigned off, unsigned tok) {
  if (__all(*(const volatile LAS unsigned*)p == tok)) return;
  asm volatile("s_waitcnt vmcnt(0)" ::: "memory");
  if (__all(*(const volatile LAS unsigned*)p == tok)) return;
  spin1(r, off, tok);
}
#define RARE(c) __builtin_expect((c) != 0, 0)

DI float wave_sum(float v) {
#define DPPADD(ctrl, rm) v += __int_as_float(__builtin_amdgcn_update_dpp(0, __float_as_int(v), ctrl, rm, 0xf, false))
  DPPADD(0x111, 0xf); DPPADD(0x112, 0xf); DPPADD(0x114, 0xf); DPPADD(0x118, 0xf); DPPADD(0x142, 0xa); DPPADD(0x143, 0xc);
#undef DPPADD
  return __int_as_float(__builtin_amdgcn_readlane(__float_as_int(v), 63));
}
DI float row16_sum(float v) {
#define DPPX(ctrl) v += __int_as_float(__builtin_amdgcn_update_dpp(0, __float_as_int(v), ctrl, 0xf, 0xf, true))
  DPPX(0xB1); DPPX(0x4E); DPPX(0x141); DPPX(0x140);
#undef DPPX
  return v;
}
DI float gelu_tanh(float u) {
  const float z = u * (0.7978845608028654f + 0.7978845608028654f * 0.044715f * u * u);
  const float e = __builtin_amdgcn_exp2f(z * (-2.0f * 1.4426950408889634f));
  return u * __builtin_amdgcn_rcpf(1.0f + e);
}
constexpr unsigned long long diag_mask(int i) {
  unsigned long long mk = 0;
  for (int l = 0; l < 64; ++l) if ((i & 3) + 8 * (i >> 2) + 4 * (l >> 5) <= (l & 31)) mk |= 1ull << l;
  return mk;
}
DI float max16(const f32x16& s, float m) {
#pragma unroll
  for (int i = 0; i < 16; i += 2) m = __builtin_fmaxf(__builtin_fmaxf(s[i], s[i + 1]), m);
  return m;
}

#define NEXT_SO() so = so == (NS - 1) * SLOT ? 0 : so + SLOT

template <bool ISV, class Hook, class Hook14, class Mid>
DI void qkv_consume(lptr L, int& so, int w, int lane, f32x16 (&acc)[2], Hook hook, Hook14 hook14, Mid mid) {
  const int r = lane & 31, h = lane >> 5, wm = w >> 2, wn = w & 3;
  int xa[4];
#pragma unroll
  for (int s = 0; s < 4; ++s) xa[s] = (wm * 2 + (r >> 4)) * 4096 + (r & 15) * 128 + (((2 * s + h) ^ ((r >> 1) & 7)) << 4);
  const int q = (lane & 15) >> 2, p = lane & 3, blk = (lane >> 4) & 1;
  const int wa = h * 4096 + 2048 + q * 256 + (((wn * 4 + blk * 2 + (p >> 1)) ^ (q << 2)) << 4) + (p & 1) * 8;
  bf16x8 xA[2][4], xB[2][4], wA[4], wB[4];
#define QM(X, W, mi, s) do { if (ISV) acc[mi] = MFMA32(X[mi][s], W[s], acc[mi]); else acc[mi] = MFMA32(W[s], X[mi][s], acc[mi]); SB(); } while (0)
#define QRX(X, mi, s) X[mi][s] = ldsr128(sl + xa[s])
#define QRW(W, mi, e) W[2 * mi + e] = cat8(ldstr(sl + wa + e * 8192), ldstr(sl + wa + e * 8192 + 1024))
#define QSTEP(mi, XN, WN, XP, WP, MM) do { lptr sl = L + so; NEXT_SO(); \
    if (MM) QM(XP, WP, 0, 2 * mi); \
    QRX(XN, mi, 0); QRX(XN, mi, 1); SB(); if (MM) QM(XP, WP, 1, 2 * mi); \
    QRX(XN, mi, 2); QRX(XN, mi, 3); QRW(WN, mi, 0); SB(); if (MM) QM(XP, WP, 0, 2 * mi + 1); \
    QRW(WN, mi, 1); SB(); if (MM) QM(XP, WP, 1, 2 * mi + 1); } while (0)
  LBAR(); QSTEP(0, xA, wA, xB, wB, false); QSTEP(1, xA, wA, xB, wB, false);
#pragma unroll 1
  for (int c = 1; c < 7; c += 2) {
    hook(c);     QSTEP(0, xB, wB, xA, wA, true); QSTEP(1, xB, wB, xA, wA, true);
    hook(c + 1); QSTEP(0, xA, wA, xB, wB, true); QSTEP(1, xA, wA, xB, wB, true);
  }
  hook14(); QSTEP(0, xB, wB, xA, wA, true); QSTEP(1, xB, wB, xA, wA, true);
#pragma unroll
  for (int s = 0; s < 4; ++s) QM(xB, wB, 0, s);
  mid();
#pragma unroll
  for (int s = 0; s < 4; ++s) QM(xB, wB, 1, s);
}

__global__ __launch_bounds__(768, 3) void fused_kernel(const char* __restrict__ x, const char* __restrict__ wqkv, const char* __restrict__ wo,
                                                       const char* __restrict__ w1, const char* __restrict__ b1, const char* __restrict__ w2,
                                                       const char* __restrict__ b2, const char* __restrict__ gw, char* __restrict__ ws,
                                                       char* __restrict__ out) {
  extern __shared__ __attribute__((aligned(16))) char smem[];
  const char* pc0;
  asm volatile("s_getpc_b64 %0" : "=s"(pc0));
  asm volatile("" :: "s"(x), "s"(wqkv), "s"(wo), "s"(w1), "s"(b1), "s"(w2), "s"(b2), "s"(gw), "s"(ws), "s"(out));
  STAMP(0);
  const int b = blockIdx.x, xcd = b & 7, idx = ((b >> 3) + 24) & 31;
  const bool lead = b == 64;
  const int lane = threadIdx.x & 63, w = rfl(threadIdx.x >> 6);
  const unsigned lds0 = (unsigned)(uintptr_t)smem;
  lptr L = (lptr)smem;
  const int Lx = xcd * 31 + (31 - idx);
  const bool hasQ = idx < 24, hasG1 = idx >= 1 && Lx < 242;
  const int tm = (xcd >> 1) * 4 + idx / 6, tn = (xcd & 1) * 6 + idx % 6;
  const int lnb = xcd * 8 + (idx - 24);
  const int g1ft = Lx < 132 ? Lx / 6 : (Lx - 132) / 5, g1dt = Lx < 132 ? Lx % 6 : 6 + (Lx - 132) % 5;
  const int head = xcd * 4 + (idx & 3), qb = idx >> 2;
  const int st = (xcd >> 1) * 8 + (idx >> 2), dt = (xcd & 1) * 4 + (idx & 3);
  unsigned fq_off;
  { const int l = lane < 12 ? lane : 3, wh = l >> 2; int tmi = l & 3; if (wh == 0 || qb == 0) tmi |= 2;
    fq_off = WS_FL + (FL_Q + ((wh * 4 + (head >> 3)) * 16 + 2 * qb - 2 + tmi) * 32) * 4; }
  const unsigned fu_off = WS_FL + (FL_U + (((lane & 32) ? (4 * dt + 3) / 3 : (4 * dt) / 3) * 32 + min(lane & 31, 21)) * 32) * 4;
  const unsigned fo_off = WS_FL + (FL_O + ((st >> 2) * 32 + (lane & 31)) * 32) * 4;
  const unsigned fh_off = WS_FL + FL_H * 4 + lane * 128;
  const rsrc_t rws = __builtin_amdgcn_make_buffer_rsrc(ws, 0, WS_END, 0x00020000);
  volatile LAS unsigned* relay = (volatile LAS unsigned*)(L + L_C + 64);
  unsigned eoff = WS_FL + FL_EPOCH * 4;
  asm volatile("" : "+v"(eoff));
  const unsigned epoch = ld32(rws, eoff);
#define TOKEN_OF(e) ((e) * 1664525u + 1013904223u)

  if (w >= 8) {
    const int j = w - 8;
#define token TOKEN_OF(epoch)
    const unsigned m0b = lds0 + j * 4096;
    const unsigned vl = lane * 16;
    unsigned so = 0;
    const char* hp = ws + WS_H + (size_t)g1dt * 64 * 3072 + j * 1536 - 2048;
    const char* w1p = w1 + (size_t)(j * 16) * 2752 + g1ft * 128;
    const unsigned u0 = (lane >> 3) * 2752 + ((((lane & 7) ^ (((lane >> 4) & 3) << 1)) & (g1ft == 21 ? 3 : 7)) << 4), u1 = u0 + 8 * 2752 - 1024;
    auto fill_g1 = [&]() { dma_g1(m0b + so, w1p, u0, u1, hp, vl); hp += 6144; w1p += 64 * 2752; NEXT_SO(); };
    auto fill_g1w = [&]() { dma_wh(m0b + so, w1p, u0, u1); w1p += 64 * 2752; NEXT_SO(); };
    auto fill_g1h3 = [&](unsigned so0) {
      const unsigned keep = so; so = so0;
#pragma unroll 1
      for (int i = 0; i < 3; ++i) { dma_hh(m0b + so, hp, vl); hp += 6144; NEXT_SO(); }
      so = keep;
    };
    const unsigned swz = ((lane & 7) ^ (((lane >> 4) & 1) << 2)) << 4;
    unsigned vw0 = (lane >> 3) * 4096 + swz, vw1 = vw0 + 8 * 4096 - 1024;
    const char* w2p = w2 + (size_t)(j * 16) * 4096 + st * 128;
    const char* up = ws + WS_U + (size_t)dt * 86 * 2048 + j * 2048 - 2048;
    const size_t w2half = (size_t)(j < 2 ? 21 * 64 : 20 * 64) * 4096;
    const int uhalf = j < 2 ? 21 * 8192 : 20 * 8192;
    auto fill_kv = [&]() {
      int t0 = qb * 8 - 8 + 4 * j; t0 = t0 < 0 ? t0 + 8 : t0;
      dma_lin4_sc1(lds0 + L_K + j * 4096, ws + WS_K + (size_t)(head * 64 + t0) * 1024, vl);
      dma_lin4_sc1(lds0 + L_V + j * 4096, ws + WS_V + (size_t)(head * 64 + t0) * 1024, vl);
    };
    int nwh = 0;
    auto fill_wh = [&]() { dma_wh(m0b + so, nwh == 0 ? w2p + w2half : w2p + (size_t)(nwh - 1) * 64 * 4096, vw0, vw1); ++nwh; NEXT_SO(); };
    unsigned so_g2 = 0;

    if (hasQ) {
      const unsigned x0 = (lane >> 3) * 1024 + (((lane & 7) ^ (lane >> 4)) << 4), x1 = (8 + (lane >> 3)) * 1024 + (((lane & 7) ^ (4 + (lane >> 4))) << 4) - 1024;
      const unsigned q0 = (lane >> 4) * 3072 + (((lane & 15) ^ ((lane >> 4) << 2)) << 4), q1 = q0 + 4 * 3072 - 1024;
      const char* xp = x + (size_t)(tm * 128 + (j >> 1) * 64 + (j & 1) * 16) * 1024;
      const char* qp = wqkv + (size_t)(j * 8) * 3072 + tn * 256 - 2048;
      int kc = QROT ? ((idx / 6 + idx % 6) % 3) * 3 : 0;
      auto fill_a = [&]() { dma_2x2(m0b + so, xp + kc * 128, x0, x1, qp + (size_t)kc * 64 * 3072, q0, q1); NEXT_SO(); };
      auto fill_b = [&]() { dma_2x2(m0b + so, xp + kc * 128 + 32 * 1024, x0, x1, qp + (size_t)kc * 64 * 3072 + 32 * 3072, q0, q1); kc = (kc + 1) & 7; NEXT_SO(); };
#pragma unroll 1
      for (int c = 0; c < 3; ++c) { fill_a(); fill_b(); }
      fill_a();
      VBAR(20);
#pragma unroll 1
      for (int c = 1; c < 5; ++c) { VBAR(12); fill_b(); fill_a(); }
      VBAR(12); fill_b();
      if (hasG1) {
        const unsigned so0 = so;
        fill_g1w();
        VBAR(10); fill_g1w(); fill_g1w();
        VBAR(6); fill_g1h3(so0); fill_g1(); fill_g1();
      } else {
        VBAR(8); VBAR(0);
      }
    } else {
      BAR();
      VBAR(0);
#pragma unroll 1
      for (int s = 0; s < 3; ++s) fill_g1w();
      if (j == 0) spin1(rws, fh_off, token);
      else if (PREFETCH & 2) {
        const int pw = lnb * 3 + (j - 1);
#pragma unroll 1
        for (int i = pw; i < 752; i += 192) dma_touch(lds0 + L_P + 1024, i < 688 ? w2 + (size_t)i * 8192 : wo + (size_t)(i - 688) * 8192, lane * 128);
      }
      BAR();
      fill_g1h3(0); fill_g1(); fill_g1();
    }
    STAMP(1);
    if (hasG1) {
      VBAR(12); fill_g1(); VBAR(14); fill_g1();
#pragma unroll 1
      for (int s = 2; s < 19; ++s) { VBAR(16); fill_g1(); }
      fill_kv();
#pragma unroll 1
      for (int s = 19; s < 27; ++s) { VBAR(16); fill_g1(); }
      so_g2 = so;
      VBAR(16); fill_wh(); VBAR(14); fill_wh(); VBAR(12); fill_wh(); VBAR(10); fill_wh(); VBAR(8); fill_wh();
      BAR(); fill_wh(); fill_wh();
      BAR();
      BAR();
    } else {
      BAR();
      so_g2 = so;
#pragma unroll 1
      for (int i = 0; i < 7; ++i) fill_wh();
      if (j == 0) spin1(rws, fq_off, token);
      BAR();
      fill_kv();
      VBAR(0);
    }
    STAMP(2);
    {
      if (j == 0) { spin1(rws, fu_off, token); *relay = 1u; }
      else { for (unsigned spins = 0; spins < 64 * SPIN_CAP && *relay == 0u; ++spins) __builtin_amdgcn_s_sleep(1); }
      STAMP(3);
      so = so_g2;
      dma_fh(m0b + so, up + uhalf, vl); NEXT_SO();
#pragma unroll 1
      for (int i = 0; i < 6; ++i) { dma_fh(m0b + so, up, vl); up += 8192; NEXT_SO(); }
    }
    w2p += (size_t)6 * 64 * 4096;
    unsigned wstr = 64 * 4096;
    int ci = 7;
    auto fill_g2 = [&]() {
      if (ci == 22) {
        w2p = wo + (size_t)(j * 16) * 1024 + dt * 128; up = ws + WS_O + (size_t)st * 32 * 2048 + j * 2048 - 2048; wstr = 64 * 1024;
        vw0 = (lane >> 3) * 1024 + swz; vw1 = vw0 + 8 * 1024 - 1024;
      }
      dma_wh(m0b + so, w2p, vw0, vw1); dma_fh(m0b + so, up, vl);
      w2p += wstr; up += 8192; NEXT_SO(); ++ci;
    };
    VBAR(12); VBAR(10);
    VBAR(8); fill_g2(); VBAR(10); fill_g2(); VBAR(12); fill_g2(); VBAR(14); fill_g2();
#pragma unroll 1
    for (int s = 6; s < 25; ++s) { VBAR(16); fill_g2(); }
    const unsigned fa_off = WS_FL + FL_O * 4 + (j * 64 + lane) * 128;
    if (lead) peek_issue(lds0 + L_X + j * 1024, ws, fa_off);
    VBAR(16); VBAR(12); VBAR(8); VBAR(4); VBAR(0);
    if (lead) peek_check(L + L_X + j * 1024 + lane * 16, rws, fa_off, token);
    BAR();
    if (lead && j == 0 && lane == 0) st32(rws, WS_FL + FL_EPOCH * 4, token);
    BAR();
    STAMP(4);
    if (j == 0) STAMP_OUT(ws, 1);
    return;
  }

#undef token
  unsigned token;
  const int r = lane & 31, h = lane >> 5;
  const int g4 = lane >> 4, q4 = (lane & 15) >> 2, p4 = lane & 3, blk = (lane >> 4) & 1;
  int so = 0;
  if (CODEPF && (idx == 0 || idx == 12) && w < CODEPF) dma_touch(lds0 + L_P + 1024, pc0 + w * 8192, lane * 128);
  if (threadIdx.x == 4) *relay = 0u;
  if (threadIdx.x < 4) {
    const unsigned cv = (threadIdx.x & 1) ? 0u : 0x3f803f80u;
    *(LAS u32x4*)(L + L_C + (threadIdx.x >> 1) * 512 + (threadIdx.x & 1) * 16) = u32x4{cv, cv, cv, cv};
  }
  lptr pk = L + L_P + lane * 16;

  const int kh = w >> 2, fb = w & 3;
  int ha[3];
#pragma unroll
  for (int db = 0; db < 3; ++db) { const int o = (kh * 3 + db) * 1024 + lane * 16, rg = o / 1536; ha[db] = rg * 4096 + 2048 + (o - rg * 1536); }
  const int wa = kh * 8192 + (4 * g4 + q4) * 128 + ((fb ^ ((2 * g4 + (q4 >> 1)) & 3)) << 5) + p4 * 8;
  f32x4 acc[3] = {};
  bf16x8 wA = {}, hA[3] = {}, wB = {}, hB[3] = {};
#define G1RD(W, H) do { lptr sl = L + so; NEXT_SO(); W = cat8(ldstr(sl + wa), ldstr(sl + wa + 4096)); \
  _Pragma("unroll") for (int db = 0; db < 3; ++db) H[db] = ldsr128(sl + ha[db]); SB(); } while (0)
#define G1MM(W, H) do { _Pragma("unroll") for (int db = 0; db < 3; ++db) acc[db] = MFMA16(W, H[db], acc[db]); SB(); } while (0)

  if (hasQ) {
    token = TOKEN_OF(epoch);
    asm volatile("" : "+v"(token));
    if (w == 7) *(LAS unsigned*)pk = ~token;
    STAMP(1);
    f32x16 qacc[2] = {};
    const bool poller = w == 7 && hasG1;
    const int pi = (xcd * 24 + idx) * 4 + w;
    const unsigned post = (poller ? (1u << 4) | (1u << 5) | (1u << 6) : 0u) | (((PREFETCH & 1) && w < 4 && pi < 688) ? 1u << PFSTEP : 0u);
    auto hook = [&](int s) {
      LBAR();
      if (RARE((post >> s) & 1)) { if (w == 7) peek_issue(lds0 + L_P, ws, fh_off); else dma_touch(lds0 + L_P + 1024, w1 + (size_t)pi * 8192, lane * 128); }
      SB();
    };
    auto hook14 = [&]() { if (poller) peek_check(pk, rws, fh_off, token); LBAR(); };
    auto mid = [&]() { if (hasG1) { LBAR(); G1RD(wA, hA); } };
    if (tn >= 8) qkv_consume<true>(L, so, w, lane, qacc, hook, hook14, mid);
    else qkv_consume<false>(L, so, w, lane, qacc, hook, hook14, mid);
    STAMP(2);
    const int wm = w >> 2, wn = w & 3;
    const int hd0 = (tn & 3) * 8 + wn * 2;
    const float sc = tn < 4 ? QSCALE : 1.0f;
    const unsigned dst = tn >= 8 ? WS_V + (((hd0 + (r >> 4)) * 64 + tm * 4 + wm * 2) * 64 + h * 16 + (r & 15)) * 16
                                 : (tn < 4 ? WS_Q : WS_K) + ((hd0 * 64 + tm * 4 + wm * 2) * 64 + lane) * 16;
    const unsigned dstep = tn >= 8 ? 512 : 65536;
#pragma unroll
    for (int mi = 0; mi < 2; ++mi) {
#pragma unroll
      for (int s = 0; s < 2; ++s) {
        u32x4 v;
#pragma unroll
        for (int jj = 0; jj < 4; ++jj) v[jj] = pk2(qacc[mi][8 * s + 2 * jj] * sc, qacc[mi][8 * s + 2 * jj + 1] * sc);
        st128(rws, dst + mi * 1024 + s * dstep, v);
      }
      if (mi == 0 && hasG1) { SB(); LBAR(); G1RD(wB, hB); G1MM(wA, hA); }
    }
  } else {
    constexpr int RS = 1056;
    u32x4 xv[4], gv[4];
#pragma unroll
    for (int i = 0; i < 4; ++i) xv[i] = *(const u32x4*)(x + (size_t)(lnb * 32 + w * 4 + g4) * 1024 + (lane & 15) * 16 + i * 256);
#pragma unroll
    for (int i = 0; i < 4; ++i) gv[i] = *(const u32x4*)(gw + (lane & 15) * 16 + i * 256);
    f32x2 v[16], s2 = {0.f, 0.f}, g2 = {0.f, 0.f};
#pragma unroll
    for (int i = 0; i < 16; ++i) {
      v[i] = f32x2{bflo(xv[i >> 2][i & 3]), bfhi(xv[i >> 2][i & 3])};
      s2 += v[i];
      g2 += v[i] * f32x2{bflo(gv[i >> 2][i & 3]), bfhi(gv[i >> 2][i & 3])};
    }
    const float mean = row16_sum(s2[0] + s2[1]) * (1.0f / 512), gs = row16_sum(g2[0] + g2[1]);
    f32x2 q2 = {0.f, 0.f};
#pragma unroll
    for (int i = 0; i < 16; ++i) { v[i] -= f32x2{mean, mean}; q2 += v[i] * v[i]; }
    const float rs = __builtin_amdgcn_rsqf(row16_sum(q2[0] + q2[1]) * (1.0f / 512) + 1e-5f);
#pragma unroll
    for (int i = 0; i < 4; ++i) {
      u32x4 ov;
#pragma unroll
      for (int jj = 0; jj < 4; ++jj) { const f32x2 t = v[4 * i + jj] * f32x2{rs, rs}; ov[jj] = pk2(t[0], t[1]); }
      *(LAS u32x4*)(L + (w * 4 + g4) * RS + (lane & 15) * 16 + i * 256) = ov;
    }
    token = TOKEN_OF(epoch);
    asm volatile("" : "+v"(token));
    if ((lane & 15) == 0) st32(rws, WS_G + (lnb * 32 + w * 4 + g4) * 4, __float_as_uint(1.0f / (1.0f + __expf(-gs))));
    STAMP(1);
    LBAR();
#pragma unroll
    for (int i = 0; i < 4; ++i) {
      const int D = w * 4 + i, d48 = D / 3;
      lptr a = L + (4 * g4 + q4) * RS + D * 32 + p4 * 8;
      const bf16x8 f = cat8(ldstr(a), ldstr(a + 16 * RS));
      st128(rws, WS_H + ((d48 * 64 + lnb) * 3 + (D - d48 * 3)) * 1024 + lane * 16, __builtin_bit_cast(u32x4, f));
      if (i == 3 && w == 7) st128(rws, WS_H + ((10 * 64 + lnb) * 3 + 2) * 1024 + lane * 16, __builtin_bit_cast(u32x4, f));
    }
    DBAR();
    if (threadIdx.x == 0) flag_st(ws, FL_H + lnb * 32, token);
    STAMP(2);
    LBAR();
    LBAR(); G1RD(wA, hA);
    LBAR(); G1RD(wB, hB); G1MM(wA, hA);
  }

  bf16x8 qf;
  if (hasG1) {
    const unsigned pre = (1u << 6) | (w == 7 ? 1u << 18 : 0u);
    const unsigned post = (w == 0 && hasQ ? 1u << 6 : 0u) | (w == 7 ? (1u << 6) | (1u << 12) | (1u << 14) | (1u << 16) : 0u);
    auto hook = [&](int s) {
      if (RARE((pre >> s) & 1)) { if (s == 6) asm volatile("s_waitcnt vmcnt(0)" ::: "memory"); else peek_check(pk, rws, fq_off, token); }
      CBAR(5);
      if (RARE((post >> s) & 1)) {
        if (s == 6) { if (w == 7) *(LAS unsigned*)pk = ~token; else if (lane == 0) flag_st(ws, FL_Q + (tn * 16 + tm) * 32, token); }
        else peek_issue(lds0 + L_P, ws, fq_off);
      }
      SB();
    };
#pragma unroll 1
    for (int s = 2; s < 20; s += 2) {
      hook(s); G1RD(wA, hA); G1MM(wB, hB);
      CBAR(5); G1RD(wB, hB); G1MM(wA, hA);
    }
    STAMP(10);
    qf = __builtin_bit_cast(bf16x8, ld128(rws, WS_Q + (head * 64 + qb * 8 + w) * 1024 + lane * 16));
    const int fblk = g1ft * 4 + fb;
    u32x2 bb = *(const u32x2*)(b1 + min(fblk * 16 + 4 * g4, 1372) * 2);
    unsigned ua[2];
#pragma unroll
    for (int e = 0; e < 2; ++e) {
      const int D = g1dt * 3 + (kh ? 2 : e);
      ua[e] = WS_U + ((((D >> 2) * 86 + fblk) * 2 + ((D >> 1) & 1)) * 64 + (g4 >> 1) * 32 + (D & 1) * 16 + (lane & 15)) * 16 + (g4 & 1) * 8;
      if (fblk >= 86 || D >= 32) ua[e] = WS_END;
    }
    lptr xw = L + L_X + (fb * 3 * 64 + lane) * 16;
#pragma unroll 1
    for (int s = 20; s < 32; s += 2) {
      CBAR(5); G1RD(wA, hA); G1MM(wB, hB);
      CBAR(5); G1RD(wB, hB); G1MM(wA, hA);
    }
    asm volatile("" : "+v"(qf), "+v"(bb), "+v"(ua[0]), "+v"(ua[1]));
    const f32x4 bv = {bflo(bb[0]), bfhi(bb[0]), bflo(bb[1]), bfhi(bb[1])};
    LBAR();
    G1MM(wB, hB);
    STAMP(3);
    if (kh == 1) { *(LAS f32x4*)(xw) = acc[0]; *(LAS f32x4*)(xw + 1024) = acc[1]; }
    else *(LAS f32x4*)(xw + 2048) = acc[2];
    LBAR();
    auto fin = [&](const f32x4& a, int off, unsigned addr) {
      const f32x4 t = a + *(const LAS f32x4*)(xw + off) + bv;
      st64(rws, addr, u32x2{pk2(gelu_tanh(t[0]), gelu_tanh(t[1])), pk2(gelu_tanh(t[2]), gelu_tanh(t[3]))});
    };
    if (kh == 1) fin(acc[2], 2048, ua[0]);
    else { fin(acc[0], 0, ua[0]); fin(acc[1], 1024, ua[1]); }
  } else {
    DBAR();
    if (threadIdx.x == 0) flag_st(ws, FL_Q + (tn * 16 + tm) * 32, token);
    LBAR();
    qf = __builtin_bit_cast(bf16x8, ld128(rws, WS_Q + (head * 64 + qb * 8 + w) * 1024 + lane * 16));
    asm volatile("" : "+v"(qf));
    LBAR();
    STAMP(3);
  }

  {
    lptr kp = L + L_K + w * 1024 + lane * 16;
    const int i_lo = qb > 0 ? 1 : 8 - w;
    const f32x16 zero = {};
    bf16x8 kf[9];
#pragma unroll
    for (int i = 0; i < 9; ++i) kf[i] = ldsr128(kp + i * 1024);
    f32x16 sb, sa, sc;
    float mx[4];
#define TMAX(S, i) (((i) >= i_lo) ? max16(S, -INFINITY) : -INFINITY)
    sa = MFMA32(kf[8], qf, zero); sc = MFMA32(kf[0], qf, zero);
    f32x16 s1 = MFMA32(kf[1], qf, zero), s2 = MFMA32(kf[2], qf, zero); SB();
#pragma unroll
    for (int i = 0; i < 16; ++i) sb[i] = __builtin_amdgcn_inverse_ballot_w64(diag_mask(i)) ? sa[i] : (qb > 0 ? sc[i] : -INFINITY);
    SB();
    sa = MFMA32(kf[3], qf, zero); sc = MFMA32(kf[4], qf, zero); SB();
    mx[0] = max16(sb, -INFINITY); mx[1] = TMAX(s1, 1); mx[2] = TMAX(s2, 2); SB();
    s1 = MFMA32(kf[5], qf, zero); s2 = MFMA32(kf[6], qf, zero); SB();
    mx[3] = TMAX(sa, 3); mx[0] = __builtin_fmaxf(mx[0], TMAX(sc, 4)); SB();
    sa = MFMA32(kf[7], qf, zero); SB();
    mx[1] = __builtin_fmaxf(mx[1], TMAX(s1, 5)); mx[2] = __builtin_fmaxf(mx[2], TMAX(s2, 6)); mx[3] = __builtin_fmaxf(mx[3], TMAX(sa, 7));
    float m = __builtin_fmaxf(__builtin_fmaxf(mx[0], mx[1]), __builtin_fmaxf(mx[2], mx[3]));
    {
      auto rr = __builtin_amdgcn_permlane32_swap(__float_as_uint(m), __float_as_uint(m), false, false);
      m = __builtin_fmaxf(__uint_as_float(rr[0]), __uint_as_float(rr[1]));
    }
    STAMP(11);
    if (hasG1) {
      DBAR();
      if (threadIdx.x == 0) flag_st(ws, FL_U + (g1dt * 32 + g1ft) * 32, token);
    }
    STAMP(4);
    const int vstr = r < 16 ? 1024 : 0;
    lptr vp0 = L + (r < 16 ? L_V + w * 1024 + h * 256 + r * 16 : (L_C + ((r == 16 || r == 20) ? 0 : 16)));
    f32x16 cin;
#pragma unroll
    for (int i = 0; i < 16; ++i) cin[i] = -m;
    f32x16 s = MFMA32(ldsr128(kp + i_lo * 1024), qf, cin);
    f32x16 o = {};
    {
      u32x4 pd[2], pf[2];
#pragma unroll
      for (int i = 0; i < 16; i += 2) {
        const unsigned pp = pk2(__builtin_amdgcn_exp2f(sb[i] - m), __builtin_amdgcn_exp2f(sb[i + 1] - m));
        const int dlt = r - crow(i, h);
        const unsigned mk = dlt >= 1 ? 0xffffffffu : (dlt == 0 ? 0x0000ffffu : 0u);
        pd[i >> 3][(i >> 1) & 3] = pp & mk;
        pf[i >> 3][(i >> 1) & 3] = pp & ~mk;
      }
      lptr vp = vp0 + 8 * vstr;
      o = MFMA32(ldsr128(vp), __builtin_bit_cast(bf16x8, pd[0]), o);
      o = MFMA32(ldsr128(vp + 512), __builtin_bit_cast(bf16x8, pd[1]), o);
      o = MFMA32(ldsr128(vp0), __builtin_bit_cast(bf16x8, pf[0]), o);
      o = MFMA32(ldsr128(vp0 + 512), __builtin_bit_cast(bf16x8, pf[1]), o);
    }
#pragma unroll 1
    for (int i = i_lo; i < 8; ++i) {
      lptr vp = vp0 + i * vstr;
      const bf16x8 kn = ldsr128(kp + (i + 1) * 1024), v0 = ldsr128(vp), v1 = ldsr128(vp + 512);
      u32x4 p0, p1;
#pragma unroll
      for (int e = 0; e < 8; e += 2) p0[e >> 1] = pk2(__builtin_amdgcn_exp2f(s[e]), __builtin_amdgcn_exp2f(s[e + 1]));
      o = MFMA32(v0, __builtin_bit_cast(bf16x8, p0), o);
#pragma unroll
      for (int e = 8; e < 16; e += 2) p1[(e >> 1) & 3] = pk2(__builtin_amdgcn_exp2f(s[e]), __builtin_amdgcn_exp2f(s[e + 1]));
      s = MFMA32(kn, qf, cin);
      o = MFMA32(v1, __builtin_bit_cast(bf16x8, p1), o);
    }
    const float il = __builtin_amdgcn_rcpf(o[8]);
    u32x4 ov;
#pragma unroll
    for (int jj = 0; jj < 4; ++jj) ov[jj] = pk2(o[2 * jj] * il, o[2 * jj + 1] * il);
    const int B = qb * 8 + w;
    st128(rws, WS_O + (((B >> 1) * 32 + head) * 2 + (B & 1)) * 1024 + lane * 16, ov);
  }
  STAMP(5);

  {
    const int mt = (w >> 1) & 1, nt = w & 1;
    const int tok = st * 64 + nt * 32 + r;
    const int ta2 = kh * 8192 + (8 * h + q4) * 128 + (((nt * 4 + blk * 2 + (p4 >> 1)) ^ ((q4 >> 1) << 2)) << 4) + (p4 & 1) * 8;
    const int tao = kh * 8192 + (4 * h + q4) * 128 + (((mt * 4 + blk * 2 + (p4 >> 1)) ^ ((q4 >> 1) << 2)) << 4) + (p4 & 1) * 8;
    const int fa2 = kh * 8192 + 2048 + mt * 1024 + lane * 16;
    const int fao = kh * 8192 + 2048 + nt * 1024 + lane * 16;
    f32x16 ay = {}, aa = {};
    bf16x8 pa0, pb0, pa1, pb1, qa0, qb0, qa1, qb1;
    float g = 0.f; unsigned b2v = 0;
#define RD2(A0, B0, A1, B1) do { lptr sl = L + so; NEXT_SO(); B0 = cat8(ldstr(sl + ta2), ldstr(sl + ta2 + 512)); A0 = ldsr128(sl + fa2); \
    B1 = cat8(ldstr(sl + ta2 + 4096), ldstr(sl + ta2 + 4096 + 512)); A1 = ldsr128(sl + fa2 + 4096); SB(); } while (0)
#define RDO(A0, B0, A1, B1) do { lptr sl = L + so; NEXT_SO(); A0 = cat8(ldstr(sl + tao), ldstr(sl + tao + 1024)); B0 = ldsr128(sl + fao); \
    A1 = cat8(ldstr(sl + tao + 4096), ldstr(sl + tao + 4096 + 1024)); B1 = ldsr128(sl + fao + 4096); SB(); } while (0)
#define MM2(ACC, A0, B0, A1, B1) do { ACC = MFMA32(A0, B0, ACC); ACC = MFMA32(A1, B1, ACC); SB(); } while (0)
    const unsigned pre = (1u << 4) | (w == 7 ? 1u << 16 : 0u);
    const unsigned post = (w == 0 ? 1u << 4 : 0u) | (w == 7 ? (1u << 4) | (1u << 10) | (1u << 12) | (1u << 14) : 0u);
    auto hook = [&](int s) {
      if (RARE((pre >> s) & 1)) { if (s == 4) asm volatile("s_waitcnt vmcnt(0)" ::: "memory"); else peek_check(pk, rws, fo_off, token); }
      CBAR(6);
      if (RARE((post >> s) & 1)) {
        if (s == 4) { if (w == 7) *(LAS unsigned*)pk = ~token; else if (lane == 0) flag_st(ws, FL_O + (qb * 32 + head) * 32, token); }
        else peek_issue(lds0 + L_P, ws, fo_off);
      }
      SB();
    };
    LBAR(); STAMP(6);
    RD2(pa0, pb0, pa1, pb1);
    if (kh) { pa0 = bf16x8{}; pa1 = bf16x8{}; pb0 = bf16x8{}; pb1 = bf16x8{}; }
#pragma unroll 1
    for (int s = 2; s < 22; s += 2) {
      CBAR(6); RD2(qa0, qb0, qa1, qb1); MM2(ay, pa0, pb0, pa1, pb1);
      hook(s); RD2(pa0, pb0, pa1, pb1); MM2(ay, qa0, qb0, qa1, qb1);
    }
    g = __uint_as_float(ld32(rws, WS_G + tok * 4));
    b2v = ((const unsigned short*)b2)[tok];
    CBAR(6); RD2(qa0, qb0, qa1, qb1); MM2(ay, pa0, pb0, pa1, pb1);
    STAMP(7);
    CBAR(6); RDO(pa0, pb0, pa1, pb1); MM2(ay, qa0, qb0, qa1, qb1);
#pragma unroll 1
    for (int s = 23; s < 29; s += 2) {
      CBAR(6); RDO(qa0, qb0, qa1, qb1); MM2(aa, pa0, pb0, pa1, pb1);
      CBAR(6); RDO(pa0, pb0, pa1, pb1); MM2(aa, qa0, qb0, qa1, qb1);
    }
    CBAR(6); RDO(qa0, qb0, qa1, qb1); MM2(aa, pa0, pb0, pa1, pb1);
    MM2(aa, qa0, qb0, qa1, qb1);
    STAMP(8);
    const float og = 1.0f - g;
    float v[16];
#pragma unroll
    for (int i = 0; i < 16; ++i) v[i] = ay[i] * og + aa[i] * g;
    LBAR();
    {
      lptr ex = L + w * 2048 + lane * 16;
      f32x4 s0, s1;
#pragma unroll
      for (int jj = 0; jj < 4; ++jj) { s0[jj] = kh ? v[jj] : v[8 + jj]; s1[jj] = kh ? v[4 + jj] : v[12 + jj]; }
      *(LAS f32x4*)(ex) = s0; *(LAS f32x4*)(ex + 1024) = s1;
    }
    LBAR();
    {
      lptr ex = L + (w ^ 4) * 2048 + lane * 16;
      const f32x4 r0 = *(const LAS f32x4*)(ex), r1 = *(const LAS f32x4*)(ex + 1024);
      const float bo = bflo(b2v) * og;
      float f[8];
#pragma unroll
      for (int jj = 0; jj < 4; ++jj) { f[jj] = (kh ? v[8 + jj] : v[jj]) + r0[jj] + bo; f[4 + jj] = (kh ? v[12 + jj] : v[4 + jj]) + r1[jj] + bo; }
      const unsigned a0 = pk2(f[0], f[1]), a1 = pk2(f[2], f[3]), c0 = pk2(f[4], f[5]), c1 = pk2(f[6], f[7]);
      auto x0 = __builtin_amdgcn_permlane32_swap(a0, c0, false, false);
      auto x1 = __builtin_amdgcn_permlane32_swap(a1, c1, false, false);
      const u32x4 ov = {x0[0], x1[0], x0[1], x1[1]};
      *(u32x4*)(out + (size_t)tok * 1024 + (dt * 64 + mt * 32 + kh * 16 + h * 8) * 2) = ov;
    }
  }
  STAMP(9);
  if (w == 0) STAMP_OUT(ws, 0);
}

extern "C" void kernel_launch(void* const* d_in, const int* in_sizes, int n_in, void* d_out, int out_size, void* d_ws, size_t ws_size,
                              hipStream_t stream) {
  (void)hipFuncSetAttribute((const void*)fused_kernel, hipFuncAttributeMaxDynamicSharedMemorySize, LDS_BYTES);
  fused_kernel<<<256, 768, LDS_BYTES, stream>>>((const char*)d_in[0], (const char*)d_in[1], (const char*)d_in[2], (const char*)d_in[4],
                                                (const char*)d_in[5], (const char*)d_in[6], (const char*)d_in[7], (const char*)d_in[8],
                                                (char*)d_ws, (char*)d_out);
}
```

```cpp
#include <hip/hip_runtime.h>
#include <hip/hip_bf16.h>
#include <stdint.h>

#define LAS __attribute__((address_space(3)))
#define GAS __attribute__((address_space(1)))
#define DI __device__ __forceinline__
typedef __attribute__((ext_vector_type(8))) __bf16 bf16x8;
typedef __attribute__((ext_vector_type(2))) __bf16 bf16x2;
typedef __attribute__((ext_vector_type(4))) short s16x4;
typedef __attribute__((ext_vector_type(2))) float f32x2;
typedef __attribute__((ext_vector_type(4))) float f32x4;
typedef __attribute__((ext_vector_type(16))) float f32x16;
typedef __attribute__((ext_vector_type(2))) unsigned u32x2;
typedef __attribute__((ext_vector_type(4))) unsigned u32x4;
typedef LAS char* lptr;
typedef __amdgpu_buffer_rsrc_t rsrc_t;

constexpr unsigned WS_Q = 0, WS_K = 2u << 20, WS_V = 4u << 20, WS_O = 6u << 20, WS_U = 8u << 20, WS_H = 10u << 20,
                   WS_G = 13u << 20, WS_FL = 14u << 20, WS_END = 15u << 20;
constexpr int FL_H = 0;
constexpr int FL_Q = 64 * 32;
constexpr int FL_U = FL_Q + 192 * 32;
constexpr int FL_O = FL_U + 352 * 32;
constexpr int FL_EPOCH = FL_O + 256 * 32;
constexpr float QSCALE = 0.25f * 1.4426950408889634f;
constexpr int NS = 7, SLOT = 16384, RING = NS * SLOT;
constexpr int L_K = RING, L_V = RING + 16384, L_X = RING + 32768  , L_C = L_X + 12288  , L_P = L_C + 1024  ;
constexpr int LDS_BYTES = 163840;
constexpr unsigned SPIN_CAP = 200000;
#define CODEPF 0
#define PREFETCH 32
#define PFSTEP 1
#define G1ROT 1
#define G2ROT 1
#define KVS 22
#define QDS 10
#define ODS 4
#define QROT 1

#define MFMA32(a, b, c) __builtin_amdgcn_mfma_f32_32x32x16_bf16(a, b, c, 0, 0, 0)
#define MFMA16(a, b, c) __builtin_amdgcn_mfma_f32_16x16x32_bf16(a, b, c, 0, 0, 0)
#define SB() __builtin_amdgcn_sched_barrier(0)
#define LBAR() do { asm volatile("s_waitcnt lgkmcnt(0)\n\ts_barrier" ::: "memory"); SB(); } while (0)
#define CBAR(N) do { asm volatile("s_waitcnt lgkmcnt(" #N ")\n\ts_barrier" ::: "memory"); SB(); } while (0)
#define DBAR() do { asm volatile("s_waitcnt vmcnt(0) lgkmcnt(0)\n\ts_barrier" ::: "memory"); SB(); } while (0)
#define VBAR(N) asm volatile("s_waitcnt vmcnt(" #N ")\n\ts_barrier" ::: "memory")
#define BAR() asm volatile("s_barrier" ::: "memory")

#define STAMP(i) do {} while (0)
#define STAMP_OUT(ws, slot) do {} while (0)

DI int rfl(int x) { return __builtin_amdgcn_readfirstlane(x); }
DI bf16x8 ldsr128(lptr p) { return *(const LAS bf16x8*)p; }
DI s16x4 ldstr(lptr p) { return __builtin_amdgcn_ds_read_tr16_b64_v4i16((LAS s16x4*)p); }
DI bf16x8 cat8(s16x4 a, s16x4 b) { return __builtin_bit_cast(bf16x8, __builtin_shufflevector(a, b, 0, 1, 2, 3, 4, 5, 6, 7)); }
DI unsigned pk2(float a, float b) { f32x2 f = {a, b}; return __builtin_bit_cast(unsigned, __builtin_convertvector(f, bf16x2)); }
DI float bflo(unsigned u) { return __uint_as_float(u << 16); }
DI float bfhi(unsigned u) { return __uint_as_float(u & 0xffff0000u); }
DI int crow(int i, int h) { return (i & 3) + 8 * (i >> 2) + 4 * h; }
DI void st128(rsrc_t r, unsigned off, u32x4 v) { __builtin_amdgcn_raw_buffer_store_b128(v, r, off, 0, 16); }
DI void st64(rsrc_t r, unsigned off, u32x2 v) { __builtin_amdgcn_raw_buffer_store_b64(v, r, off, 0, 16); }
DI void st32(rsrc_t r, unsigned off, unsigned v) { __builtin_amdgcn_raw_buffer_store_b32(v, r, off, 0, 16); }
DI u32x4 ld128(rsrc_t r, unsigned off) { return __builtin_amdgcn_raw_buffer_load_b128(r, off, 0, 16); }
DI unsigned ld32(rsrc_t r, unsigned off) { return __builtin_amdgcn_raw_buffer_load_b32(r, off, 0, 16); }
DI unsigned flag_ld(const char* ws, int idx) { return __hip_atomic_load((const GAS unsigned*)(ws + WS_FL) + idx, __ATOMIC_RELAXED, __HIP_MEMORY_SCOPE_AGENT); }
DI void flag_st(char* ws, int idx, unsigned v) { __hip_atomic_store((GAS unsigned*)(ws + WS_FL) + idx, v, __ATOMIC_RELAXED, __HIP_MEMORY_SCOPE_AGENT); }

#define M0_IN "s_mov_b32 %0, m0\n\ts_mov_b32 m0, %1\n\ts_nop 0\n\t"
#define M0_OUT "s_mov_b32 m0, %0"
DI void dma_2x2(unsigned m0v, const void* b0, unsigned v0, unsigned v1, const void* b1, unsigned v2, unsigned v3) {
  unsigned sv;
  asm volatile(M0_IN "global_load_lds_dwordx4 %4, %2\n\tglobal_load_lds_dwordx4 %5, %2 offset:1024\n\t"
               "global_load_lds_dwordx4 %6, %3 offset:2048\n\tglobal_load_lds_dwordx4 %7, %3 offset:3072\n\t" M0_OUT
               : "=&s"(sv) : "s"(m0v), "s"(b0), "s"(b1), "v"(v0), "v"(v1), "v"(v2), "v"(v3) : "memory");
}
DI void dma_lin4_sc1(unsigned m0v, const void* b, unsigned vl) {
  unsigned sv;
  asm volatile(M0_IN "global_load_lds_dwordx4 %3, %2 sc1\n\tglobal_load_lds_dwordx4 %3, %2 offset:1024 sc1\n\t"
               "global_load_lds_dwordx4 %3, %2 offset:2048 sc1\n\tglobal_load_lds_dwordx4 %3, %2 offset:3072 sc1\n\t" M0_OUT
               : "=&s"(sv) : "s"(m0v), "s"(b), "v"(vl) : "memory");
}
DI void dma_g1(unsigned m0v, const void* wb, unsigned v0, unsigned v1, const void* hb_m2048, unsigned vl) {
  unsigned sv;
  asm volatile(M0_IN "global_load_lds_dwordx4 %4, %2\n\tglobal_load_lds_dwordx4 %5, %2 offset:1024\n\t"
               "global_load_lds_dwordx4 %6, %3 offset:2048 sc1\n\t"
               "s_mov_b32 exec_hi, 0\n\tglobal_load_lds_dwordx4 %6, %3 offset:3072 sc1\n\ts_mov_b32 exec_hi, -1\n\t" M0_OUT
               : "=&s"(sv) : "s"(m0v), "s"(wb), "s"(hb_m2048), "v"(v0), "v"(v1), "v"(vl) : "memory");
}
DI void dma_hh(unsigned m0v, const void* hb_m2048, unsigned vl) {
  unsigned sv;
  asm volatile(M0_IN "global_load_lds_dwordx4 %3, %2 offset:2048 sc1\n\t"
               "s_mov_b32 exec_hi, 0\n\tglobal_load_lds_dwordx4 %3, %2 offset:3072 sc1\n\ts_mov_b32 exec_hi, -1\n\t" M0_OUT
               : "=&s"(sv) : "s"(m0v), "s"(hb_m2048), "v"(vl) : "memory");
}
DI void dma_lin4(unsigned m0v, const void* b, unsigned vl) {
  unsigned sv;
  asm volatile(M0_IN "global_load_lds_dwordx4 %3, %2\n\tglobal_load_lds_dwordx4 %3, %2 offset:1024\n\t"
               "global_load_lds_dwordx4 %3, %2 offset:2048\n\tglobal_load_lds_dwordx4 %3, %2 offset:3072\n\t" M0_OUT
               : "=&s"(sv) : "s"(m0v), "s"(b), "v"(vl) : "memory");
}
DI void dma_touch(unsigned m0v, const void* b, unsigned voff) {
  unsigned sv;
  asm volatile(M0_IN "global_load_lds_dword %3, %2\n\t" M0_OUT : "=&s"(sv) : "s"(m0v), "s"(b), "v"(voff) : "memory");
}
DI void dma_wh(unsigned m0v, const void* wb, unsigned v0, unsigned v1) {
  unsigned sv;
  asm volatile(M0_IN "global_load_lds_dwordx4 %3, %2\n\tglobal_load_lds_dwordx4 %4, %2 offset:1024\n\t" M0_OUT
               : "=&s"(sv) : "s"(m0v), "s"(wb), "v"(v0), "v"(v1) : "memory");
}
DI void dma_fh(unsigned m0v, const void* fb_m2048, unsigned vl) {
  unsigned sv;
  asm volatile(M0_IN "global_load_lds_dwordx4 %3, %2 offset:2048 sc1\n\tglobal_load_lds_dwordx4 %3, %2 offset:3072 sc1\n\t" M0_OUT
               : "=&s"(sv) : "s"(m0v), "s"(fb_m2048), "v"(vl) : "memory");
}

DI void spin1(rsrc_t r, unsigned off, unsigned tok) {
  unsigned n = 0;
#pragma nounroll
  do { const unsigned v = ld32(r, off); if (__all(v == tok)) break; __builtin_amdgcn_s_sleep(4); asm volatile("" ::: "memory"); } while (++n < SPIN_CAP);
  __builtin_amdgcn_fence(__ATOMIC_ACQUIRE, "wavefront");
}
DI void peek_issue(unsigned m0v, const void* ws, unsigned off) {
  unsigned sv;
  asm volatile(M0_IN "global_load_lds_dwordx4 %3, %2 sc1\n\t" M0_OUT : "=&s"(sv) : "s"(m0v), "s"(ws), "v"(off) : "memory");
}
DI void peek_check(lptr p, rsrc_t r, unsigned off, unsigned tok) {
  if (__all(*(const volatile LAS unsigned*)p == tok)) return;
  asm volatile("s_waitcnt vmcnt(0)" ::: "memory");
  if (__all(*(const volatile LAS unsigned*)p == tok)) return;
  spin1(r, off, tok);
}
#define RARE(c) __builtin_expect((c) != 0, 0)

DI float wave_sum(float v) {
#define DPPADD(ctrl, rm) v += __int_as_float(__builtin_amdgcn_update_dpp(0, __float_as_int(v), ctrl, rm, 0xf, false))
  DPPADD(0x111, 0xf); DPPADD(0x112, 0xf); DPPADD(0x114, 0xf); DPPADD(0x118, 0xf); DPPADD(0x142, 0xa); DPPADD(0x143, 0xc);
#undef DPPADD
  return __int_as_float(__builtin_amdgcn_readlane(__float_as_int(v), 63));
}
DI float row16_sum(float v) {
#define DPPX(ctrl) v += __int_as_float(__builtin_amdgcn_update_dpp(0, __float_as_int(v), ctrl, 0xf, 0xf, true))
  DPPX(0xB1); DPPX(0x4E); DPPX(0x141); DPPX(0x140);
#undef DPPX
  return v;
}
DI float gelu_tanh(float u) {
  const float z = u * (0.7978845608028654f + 0.7978845608028654f * 0.044715f * u * u);
  const float e = __builtin_amdgcn_exp2f(z * (-2.0f * 1.4426950408889634f));
  return u * __builtin_amdgcn_rcpf(1.0f + e);
}
constexpr unsigned long long diag_mask(int i) {
  unsigned long long mk = 0;
  for (int l = 0; l < 64; ++l) if ((i & 3) + 8 * (i >> 2) + 4 * (l >> 5) <= (l & 31)) mk |= 1ull << l;
  return mk;
}
DI float max16(const f32x16& s, float m) {
#pragma unroll
  for (int i = 0; i < 16; i += 2) m = __builtin_fmaxf(__builtin_fmaxf(s[i], s[i + 1]), m);
  return m;
}

#define NEXT_SO() so = so == (NS - 1) * SLOT ? 0 : so + SLOT

template <bool ISV, class Hook, class Hook14, class Mid>
DI void qkv_consume(lptr L, int& so, int w, int lane, f32x16 (&acc)[2], Hook hook, Hook14 hook14, Mid mid) {
  const int r = lane & 31, h = lane >> 5, wm = w >> 2, wn = w & 3;
  int xa[4];
#pragma unroll
  for (int s = 0; s < 4; ++s) xa[s] = (wm * 2 + (r >> 4)) * 4096 + (r & 15) * 128 + (((2 * s + h) ^ ((r >> 1) & 7)) << 4);
  const int q = (lane & 15) >> 2, p = lane & 3, blk = (lane >> 4) & 1;
  const int wa = h * 4096 + 2048 + q * 256 + (((wn * 4 + blk * 2 + (p >> 1)) ^ (q << 2)) << 4) + (p & 1) * 8;
  bf16x8 xA[2][4], xB[2][4], wA[4], wB[4];
#define QM(X, W, mi, s) do { if (ISV) acc[mi] = MFMA32(X[mi][s], W[s], acc[mi]); else acc[mi] = MFMA32(W[s], X[mi][s], acc[mi]); SB(); } while (0)
#define QRX(X, mi, s) X[mi][s] = ldsr128(sl + xa[s])
#define QRW(W, mi, e) W[2 * mi + e] = cat8(ldstr(sl + wa + e * 8192), ldstr(sl + wa + e * 8192 + 1024))
#define QSTEP(mi, XN, WN, XP, WP, MM) do { lptr sl = L + so; NEXT_SO(); \
    if (MM) QM(XP, WP, 0, 2 * mi); \
    QRX(XN, mi, 0); QRX(XN, mi, 1); SB(); if (MM) QM(XP, WP, 1, 2 * mi); \
    QRX(XN, mi, 2); QRX(XN, mi, 3); QRW(WN, mi, 0); SB(); if (MM) QM(XP, WP, 0, 2 * mi + 1); \
    QRW(WN, mi, 1); SB(); if (MM) QM(XP, WP, 1, 2 * mi + 1); } while (0)
  LBAR(); QSTEP(0, xA, wA, xB, wB, false); QSTEP(1, xA, wA, xB, wB, false);
#pragma unroll 1
  for (int c = 1; c < 7; c += 2) {
    hook(c);     QSTEP(0, xB, wB, xA, wA, true); QSTEP(1, xB, wB, xA, wA, true);
    hook(c + 1); QSTEP(0, xA, wA, xB, wB, true); QSTEP(1, xA, wA, xB, wB, true);
  }
  hook14(); QSTEP(0, xB, wB, xA, wA, true); QSTEP(1, xB, wB, xA, wA, true);
#pragma unroll
  for (int s = 0; s < 4; ++s) QM(xB, wB, 0, s);
  mid();
#pragma unroll
  for (int s = 0; s < 4; ++s) QM(xB, wB, 1, s);
}

__global__ __launch_bounds__(768, 3) void fused_kernel(const char* __restrict__ x, const char* __restrict__ wqkv, const char* __restrict__ wo,
                                                       const char* __restrict__ w1, const char* __restrict__ b1, const char* __restrict__ w2,
                                                       const char* __restrict__ b2, const char* __restrict__ gw, char* __restrict__ ws,
                                                       char* __restrict__ out) {
  extern __shared__ __attribute__((aligned(16))) char smem[];
  const char* pc0;
  asm volatile("s_getpc_b64 %0" : "=s"(pc0));
  asm volatile("" :: "s"(x), "s"(wqkv), "s"(wo), "s"(w1), "s"(b1), "s"(w2), "s"(b2), "s"(gw), "s"(ws), "s"(out));
  STAMP(0);
  const int b = blockIdx.x, xcd = b & 7, idx = ((b >> 3) + 24) & 31;
  const bool lead = b == 65;
  const int lane = threadIdx.x & 63, w = rfl(threadIdx.x >> 6);
  const unsigned lds0 = (unsigned)(uintptr_t)smem;
  lptr L = (lptr)smem;
  const int gi = 31 - idx;
  const bool hasQ = idx < 24, hasG1 = xcd == 0 || gi < 30;
  const int tm = (xcd >> 1) * 4 + idx / 6, tn = (xcd & 1) * 6 + idx % 6;
  const int lnb = xcd * 8 + (idx - 24);
  const bool lowd = xcd == 0 ? gi < 12 : xcd < 5;
  const int gj = xcd == 0 && !lowd ? gi - 12 : gi;
  const int g1ft = lowd ? (xcd == 0 ? 20 : (xcd - 1) * 5) + gj / 6 : (xcd == 0 ? 18 : (xcd - 5) * 6) + gj / 5;
  const int g1dt = lowd ? gj % 6 : 6 + gj % 5;
  const int head = xcd * 4 + (idx & 3), qb = idx >> 2;
  const int st = (xcd >> 1) * 8 + (idx >> 2), dt = (xcd & 1) * 4 + (idx & 3);
  unsigned fq_off;
  { const int l = lane < 12 ? lane : 3, wh = l >> 2; int tmi = l & 3; if (wh == 0 || qb == 0) tmi |= 2;
    fq_off = WS_FL + (FL_Q + ((wh * 4 + (head >> 3)) * 16 + 2 * qb - 2 + tmi) * 32) * 4; }
  const unsigned fu_off = WS_FL + (FL_U + (((lane & 32) ? (4 * dt + 3) / 3 : (4 * dt) / 3) * 32 + min(lane & 31, 21)) * 32) * 4;
  const unsigned fo_off = WS_FL + (FL_O + ((st >> 2) * 32 + (lane & 31)) * 32) * 4;
  const unsigned fh_off = WS_FL + FL_H * 4 + lane * 128;
  const rsrc_t rws = __builtin_amdgcn_make_buffer_rsrc(ws, 0, WS_END, 0x00020000);
  volatile LAS unsigned* relay = (volatile LAS unsigned*)(L + L_C + 64);
  unsigned eoff = WS_FL + FL_EPOCH * 4;
  asm volatile("" : "+v"(eoff));
  const unsigned epoch = ld32(rws, eoff);
#define TOKEN_OF(e) ((e) * 1664525u + 1013904223u)

  if (w >= 8) {
    const int j = w - 8;
#define token TOKEN_OF(epoch)
    const unsigned m0b = lds0 + j * 4096;
    const unsigned vl = lane * 16;
    unsigned so = 0;
    const char* hp = ws + WS_H + (size_t)g1dt * 64 * 3072 + j * 1536 - 2048;
    const char* w1p = w1 + (size_t)(j * 16) * 2752 + g1ft * 128;
    const unsigned u0 = (lane >> 3) * 2752 + ((((lane & 7) ^ (((lane >> 4) & 3) << 1)) & (g1ft == 21 ? 3 : 7)) << 4), u1 = u0 + 8 * 2752 - 1024;
    int cw = G1ROT ? (lowd ? g1dt * 6 : (g1dt - 6) * 6 + 3) : 0, ch = cw;
    auto fill_g1 = [&]() { dma_g1(m0b + so, w1p + (size_t)cw * (64 * 2752), u0, u1, hp + cw * 6144, vl); cw = (cw + 1) & 31; NEXT_SO(); };
    auto fill_g1w = [&]() { dma_wh(m0b + so, w1p + (size_t)cw * (64 * 2752), u0, u1); cw = (cw + 1) & 31; NEXT_SO(); };
    auto fill_g1h3 = [&](unsigned so0) {
      const unsigned keep = so; so = so0;
#pragma unroll 1
      for (int i = 0; i < 3; ++i) { dma_hh(m0b + so, hp + ch * 6144, vl); ch = (ch + 1) & 31; NEXT_SO(); }
      so = keep;
    };
    const unsigned swz = ((lane & 7) ^ (((lane >> 4) & 1) << 2)) << 4;
    unsigned vw0 = (lane >> 3) * 4096 + swz, vw1 = vw0 + 8 * 4096 - 1024;
    const char* w2p = w2 + (size_t)(j * 16) * 4096 + st * 128;
    const char* up = ws + WS_U + (size_t)dt * 86 * 2048 + j * 2048 - 2048;
    const size_t w2half = (size_t)(j < 2 ? 21 * 64 : 20 * 64) * 4096;
    const int uhalf = j < 2 ? 21 * 8192 : 20 * 8192;
    auto fill_kv = [&]() {
      int t0 = qb * 8 - 8 + 4 * j; t0 = t0 < 0 ? t0 + 8 : t0;
      dma_lin4_sc1(lds0 + L_K + j * 4096, ws + WS_K + (size_t)(head * 64 + t0) * 1024, vl);
      dma_lin4_sc1(lds0 + L_V + j * 4096, ws + WS_V + (size_t)(head * 64 + t0) * 1024, vl);
    };
    int nwh = 0;
    const int r2 = G2ROT ? (((dt & 3) * 2 + (dt >> 2)) * 21) >> 3 : 0;
    int c2 = r2;
    auto fill_wh = [&]() {
      dma_wh(m0b + so, nwh == 0 ? w2p + w2half : w2p + (size_t)c2 * (64 * 4096), vw0, vw1);
      if (nwh) c2 = c2 == 20 ? 0 : c2 + 1;
      ++nwh; NEXT_SO();
    };
    unsigned so_g2 = 0;
    auto warm_g2 = [&]() {
      if (PREFETCH & 48) {
        const int t = idx * 4 + j;
        if ((PREFETCH & 16) && t < 120) dma_touch(lds0 + L_P + 1024, w2 + (size_t)(384 + t * 8) * 4096 + (xcd >> 1) * 1024, (lane >> 3) * 4096 + (lane & 7) * 128);
        if (j == 1) dma_touch(lds0 + L_P + 1024, wo + (size_t)(idx * 16) * 1024 + (xcd & 1) * 512, (lane >> 2) * 1024 + (lane & 3) * 128);
      }
    };

    if (hasQ) {
      const unsigned x0 = (lane >> 3) * 1024 + (((lane & 7) ^ (lane >> 4)) << 4), x1 = (8 + (lane >> 3)) * 1024 + (((lane & 7) ^ (4 + (lane >> 4))) << 4) - 1024;
      const unsigned q0 = (lane >> 4) * 3072 + (((lane & 15) ^ ((lane >> 4) << 2)) << 4), q1 = q0 + 4 * 3072 - 1024;
      const char* xp = x + (size_t)(tm * 128 + (j >> 1) * 64 + (j & 1) * 16) * 1024;
      const char* qp = wqkv + (size_t)(j * 8) * 3072 + tn * 256 - 2048;
      int kc = QROT ? ((idx / 6 + idx % 6) % 3) * 3 : 0;
      auto fill_a = [&]() { dma_2x2(m0b + so, xp + kc * 128, x0, x1, qp + (size_t)kc * 64 * 3072, q0, q1); NEXT_SO(); };
      auto fill_b = [&]() { dma_2x2(m0b + so, xp + kc * 128 + 32 * 1024, x0, x1, qp + (size_t)kc * 64 * 3072 + 32 * 3072, q0, q1); kc = (kc + 1) & 7; NEXT_SO(); };
#pragma unroll 1
      for (int c = 0; c < 3; ++c) { fill_a(); fill_b(); }
      fill_a();
      VBAR(20);
#pragma unroll 1
      for (int c = 1; c < 5; ++c) { VBAR(12); fill_b(); fill_a(); }
      VBAR(12); fill_b();
      if (hasG1) {
        const unsigned so0 = so;
        fill_g1w();
        VBAR(10); fill_g1w(); fill_g1w();
        VBAR(6); fill_g1h3(so0); fill_g1(); fill_g1();
      } else {
        VBAR(8); VBAR(0);
      }
    } else {
      BAR();
      VBAR(0);
#pragma unroll 1
      for (int s = 0; s < 3; ++s) fill_g1w();
      if (j == 0) spin1(rws, fh_off, token);
      else if (PREFETCH & 2) {
        const int pw = lnb * 3 + (j - 1);
#pragma unroll 1
        for (int i = pw; i < 752; i += 192) dma_touch(lds0 + L_P + 1024, i < 688 ? w2 + (size_t)i * 8192 : wo + (size_t)(i - 688) * 8192, lane * 128);
      } else if (PREFETCH & 4) {
        const int pw = lnb * 3 + (j - 1);
#pragma unroll 1
        for (int i = pw; i < 688; i += 192) dma_touch(lds0 + L_P + 1024, w1 + (size_t)i * 8192, lane * 128);
      }
      BAR();
      fill_g1h3(0); fill_g1(); fill_g1();
    }
    STAMP(1);
    if (hasG1) {
      VBAR(12); fill_g1(); VBAR(14); fill_g1();
#pragma unroll 1
      for (int s = 2; s <= KVS; ++s) { VBAR(16); fill_g1(); }
      fill_kv();
#pragma unroll 1
      for (int s = KVS + 1; s < 27; ++s) { VBAR(16); fill_g1(); }
      so_g2 = so;
      VBAR(16); fill_wh(); VBAR(14); fill_wh(); VBAR(12); fill_wh(); VBAR(10); fill_wh(); VBAR(8); fill_wh();
      BAR(); fill_wh(); fill_wh();
      warm_g2();
      BAR();
      BAR();
    } else {
      BAR();
      so_g2 = so;
#pragma unroll 1
      for (int i = 0; i < 7; ++i) fill_wh();
      warm_g2();
      if (j == 0) spin1(rws, fq_off, token);
      BAR();
      fill_kv();
      VBAR(0);
    }
    STAMP(2);
    {
      if (j == 0) { spin1(rws, fu_off, token); *relay = 1u; }
      else { for (unsigned spins = 0; spins < 64 * SPIN_CAP && *relay == 0u; ++spins) __builtin_amdgcn_s_sleep(1); }
      STAMP(3);
      so = so_g2;
      dma_fh(m0b + so, up + uhalf, vl); NEXT_SO();
#pragma unroll 1
      for (int i = 0, c = r2; i < 6; ++i) { dma_fh(m0b + so, up + c * 8192, vl); c = c == 20 ? 0 : c + 1; NEXT_SO(); }
    }
    unsigned wsh = 18;
    int ci = 7;
    auto fill_g2 = [&]() {
      if (ci == 22) {
        w2p = wo + (size_t)(j * 16) * 1024 + dt * 128; up = ws + WS_O + (size_t)st * 32 * 2048 + j * 2048 - 2048; wsh = 16; c2 = 0;
        vw0 = (lane >> 3) * 1024 + swz; vw1 = vw0 + 8 * 1024 - 1024;
      }
      dma_wh(m0b + so, w2p + ((size_t)c2 << wsh), vw0, vw1); dma_fh(m0b + so, up + c2 * 8192, vl);
      c2 = c2 == 20 ? 0 : c2 + 1; NEXT_SO(); ++ci;
    };
    VBAR(12); VBAR(10);
    VBAR(8); fill_g2(); VBAR(10); fill_g2(); VBAR(12); fill_g2(); VBAR(14); fill_g2();
#pragma unroll 1
    for (int s = 6; s < 25; ++s) { VBAR(16); fill_g2(); }
    const unsigned fa_off = WS_FL + FL_O * 4 + (j * 64 + lane) * 128;
    if (lead) peek_issue(lds0 + L_X + j * 1024, ws, fa_off);
    VBAR(16); VBAR(12); VBAR(8); VBAR(4); VBAR(0);
    if (lead) peek_check(L + L_X + j * 1024 + lane * 16, rws, fa_off, token);
    BAR();
    if (lead && j == 0 && lane == 0) st32(rws, WS_FL + FL_EPOCH * 4, token);
    BAR();
    STAMP(4);
    if (j == 0) STAMP_OUT(ws, 1);
    return;
  }

#undef token
  unsigned token;
  const int r = lane & 31, h = lane >> 5;
  const int g4 = lane >> 4, q4 = (lane & 15) >> 2, p4 = lane & 3, blk = (lane >> 4) & 1;
  int so = 0;
  if (CODEPF && (idx == 0 || idx == 12) && w < CODEPF) dma_touch(lds0 + L_P + 1024, pc0 + w * 8192, lane * 128);
  if (threadIdx.x == 4) *relay = 0u;
  if (threadIdx.x < 4) {
    const unsigned cv = (threadIdx.x & 1) ? 0u : 0x3f803f80u;
    *(LAS u32x4*)(L + L_C + (threadIdx.x >> 1) * 512 + (threadIdx.x & 1) * 16) = u32x4{cv, cv, cv, cv};
  }
  lptr pk = L + L_P + lane * 16;

  const int kh = w >> 2, fb = w & 3;
  int ha[3];
#pragma unroll
  for (int db = 0; db < 3; ++db) { const int o = (kh * 3 + db) * 1024 + lane * 16, rg = o / 1536; ha[db] = rg * 4096 + 2048 + (o - rg * 1536); }
  const int wa = kh * 8192 + (4 * g4 + q4) * 128 + ((fb ^ ((2 * g4 + (q4 >> 1)) & 3)) << 5) + p4 * 8;
  f32x4 acc[3] = {};
  bf16x8 wA = {}, hA[3] = {}, wB = {}, hB[3] = {};
#define G1RD(W, H) do { lptr sl = L + so; NEXT_SO(); W = cat8(ldstr(sl + wa), ldstr(sl + wa + 4096)); \
  _Pragma("unroll") for (int db = 0; db < 3; ++db) H[db] = ldsr128(sl + ha[db]); SB(); } while (0)
#define G1MM(W, H) do { _Pragma("unroll") for (int db = 0; db < 3; ++db) acc[db] = MFMA16(W, H[db], acc[db]); SB(); } while (0)

  if (hasQ) {
    token = TOKEN_OF(epoch);
    asm volatile("" : "+v"(token));
    if (w == 7) *(LAS unsigned*)pk = ~token;
    STAMP(1);
    f32x16 qacc[2] = {};
    const bool poller = w == 7 && hasG1;
    const int pi = (xcd * 24 + idx) * 4 + w;
    const unsigned post = (poller ? (1u << 4) | (1u << 5) | (1u << 6) : 0u) | (((PREFETCH & 1) && w < 4 && pi < 688) ? 1u << PFSTEP : 0u);
    if (post & 1) dma_touch(lds0 + L_P + 1024, w1 + (size_t)pi * 8192, lane * 128);
    auto hook = [&](int s) {
      LBAR();
      if (RARE((post >> s) & 1)) { if (w == 7) peek_issue(lds0 + L_P, ws, fh_off); else dma_touch(lds0 + L_P + 1024, w1 + (size_t)pi * 8192, lane * 128); }
      SB();
    };
    auto hook14 = [&]() { if (poller) peek_check(pk, rws, fh_off, token); LBAR(); };
    auto mid = [&]() { if (hasG1) { LBAR(); G1RD(wA, hA); } };
    if (tn >= 8) qkv_consume<true>(L, so, w, lane, qacc, hook, hook14, mid);
    else qkv_consume<false>(L, so, w, lane, qacc, hook, hook14, mid);
    STAMP(2);
    const int wm = w >> 2, wn = w & 3;
    const int hd0 = (tn & 3) * 8 + wn * 2;
    const float sc = tn < 4 ? QSCALE : 1.0f;
    const unsigned dst = tn >= 8 ? WS_V + (((hd0 + (r >> 4)) * 64 + tm * 4 + wm * 2) * 64 + h * 16 + (r & 15)) * 16
                                 : (tn < 4 ? WS_Q : WS_K) + ((hd0 * 64 + tm * 4 + wm * 2) * 64 + lane) * 16;
    const unsigned dstep = tn >= 8 ? 512 : 65536;
#pragma unroll
    for (int mi = 0; mi < 2; ++mi) {
#pragma unroll
      for (int s = 0; s < 2; ++s) {
        u32x4 v;
#pragma unroll
        for (int jj = 0; jj < 4; ++jj) v[jj] = pk2(qacc[mi][8 * s + 2 * jj] * sc, qacc[mi][8 * s + 2 * jj + 1] * sc);
        st128(rws, dst + mi * 1024 + s * dstep, v);
      }
      if (mi == 0 && hasG1) { SB(); LBAR(); G1RD(wB, hB); G1MM(wA, hA); }
    }
  } else {
    constexpr int RS = 1056;
    u32x4 xv[4], gv[4];
#pragma unroll
    for (int i = 0; i < 4; ++i) xv[i] = *(const u32x4*)(x + (size_t)(lnb * 32 + w * 4 + g4) * 1024 + (lane & 15) * 16 + i * 256);
#pragma unroll
    for (int i = 0; i < 4; ++i) gv[i] = *(const u32x4*)(gw + (lane & 15) * 16 + i * 256);
    f32x2 v[16], s2 = {0.f, 0.f}, g2 = {0.f, 0.f};
#pragma unroll
    for (int i = 0; i < 16; ++i) {
      v[i] = f32x2{bflo(xv[i >> 2][i & 3]), bfhi(xv[i >> 2][i & 3])};
      s2 += v[i];
      g2 += v[i] * f32x2{bflo(gv[i >> 2][i & 3]), bfhi(gv[i >> 2][i & 3])};
    }
    const float mean = row16_sum(s2[0] + s2[1]) * (1.0f / 512), gs = row16_sum(g2[0] + g2[1]);
    f32x2 q2 = {0.f, 0.f};
#pragma unroll
    for (int i = 0; i < 16; ++i) { v[i] -= f32x2{mean, mean}; q2 += v[i] * v[i]; }
    const float rs = __builtin_amdgcn_rsqf(row16_sum(q2[0] + q2[1]) * (1.0f / 512) + 1e-5f);
#pragma unroll
    for (int i = 0; i < 4; ++i) {
      u32x4 ov;
#pragma unroll
      for (int jj = 0; jj < 4; ++jj) { const f32x2 t = v[4 * i + jj] * f32x2{rs, rs}; ov[jj] = pk2(t[0], t[1]); }
      *(LAS u32x4*)(L + (w * 4 + g4) * RS + (lane & 15) * 16 + i * 256) = ov;
    }
    token = TOKEN_OF(epoch);
    asm volatile("" : "+v"(token));
    if ((lane & 15) == 0) st32(rws, WS_G + (lnb * 32 + w * 4 + g4) * 4, __float_as_uint(1.0f / (1.0f + __expf(-gs))));
    STAMP(1);
    LBAR();
#pragma unroll
    for (int i = 0; i < 4; ++i) {
      const int D = w * 4 + i, d48 = D / 3;
      lptr a = L + (4 * g4 + q4) * RS + D * 32 + p4 * 8;
      const bf16x8 f = cat8(ldstr(a), ldstr(a + 16 * RS));
      st128(rws, WS_H + ((d48 * 64 + lnb) * 3 + (D - d48 * 3)) * 1024 + lane * 16, __builtin_bit_cast(u32x4, f));
      if (i == 3 && w == 7) st128(rws, WS_H + ((10 * 64 + lnb) * 3 + 2) * 1024 + lane * 16, __builtin_bit_cast(u32x4, f));
    }
    DBAR();
    if (threadIdx.x == 0) flag_st(ws, FL_H + lnb * 32, token);
    STAMP(2);
    LBAR();
    LBAR(); G1RD(wA, hA);
    LBAR(); G1RD(wB, hB); G1MM(wA, hA);
  }

  bf16x8 qf;
  if (hasG1) {
    const unsigned pre = (1u << QDS) | (w == 7 ? 1u << KVS : 0u);
    const unsigned post = (w == 0 && hasQ ? 1u << QDS : 0u) | (w == 7 ? (1u << QDS) | (1u << (KVS - 6)) | (1u << (KVS - 4)) | (1u << (KVS - 2)) : 0u);
    auto hook = [&](int s) {
      if (RARE((pre >> s) & 1)) { if (s == QDS) asm volatile("s_waitcnt vmcnt(0)" ::: "memory"); else peek_check(pk, rws, fq_off, token); }
      CBAR(5);
      if (RARE((post >> s) & 1)) {
        if (s == QDS) { if (w == 7) *(LAS unsigned*)pk = ~token; else if (lane == 0) flag_st(ws, FL_Q + (tn * 16 + tm) * 32, token); }
        else peek_issue(lds0 + L_P, ws, fq_off);
      }
      SB();
    };
#pragma unroll 1
    for (int s = 2; s < KVS + 2; s += 2) {
      hook(s); G1RD(wA, hA); G1MM(wB, hB);
      CBAR(5); G1RD(wB, hB); G1MM(wA, hA);
    }
    STAMP(10);
    qf = __builtin_bit_cast(bf16x8, ld128(rws, WS_Q + (head * 64 + qb * 8 + w) * 1024 + lane * 16));
    const int fblk = g1ft * 4 + fb;
    u32x2 bb = *(const u32x2*)(b1 + min(fblk * 16 + 4 * g4, 1372) * 2);
    unsigned ua[2];
#pragma unroll
    for (int e = 0; e < 2; ++e) {
      const int D = g1dt * 3 + (kh ? 2 : e);
      ua[e] = WS_U + ((((D >> 2) * 86 + fblk) * 2 + ((D >> 1) & 1)) * 64 + (g4 >> 1) * 32 + (D & 1) * 16 + (lane & 15)) * 16 + (g4 & 1) * 8;
      if (fblk >= 86 || D >= 32) ua[e] = WS_END;
    }
    lptr xw = L + L_X + (fb * 3 * 64 + lane) * 16;
#pragma unroll 1
    for (int s = KVS + 2; s < 32; s += 2) {
      CBAR(5); G1RD(wA, hA); G1MM(wB, hB);
      CBAR(5); G1RD(wB, hB); G1MM(wA, hA);
    }
    asm volatile("" : "+v"(qf), "+v"(bb), "+v"(ua[0]), "+v"(ua[1]));
    const f32x4 bv = {bflo(bb[0]), bfhi(bb[0]), bflo(bb[1]), bfhi(bb[1])};
    LBAR();
    G1MM(wB, hB);
    STAMP(3);
    if (kh == 1) { *(LAS f32x4*)(xw) = acc[0]; *(LAS f32x4*)(xw + 1024) = acc[1]; }
    else *(LAS f32x4*)(xw + 2048) = acc[2];
    LBAR();
    auto fin = [&](const f32x4& a, int off, unsigned addr) {
      const f32x4 t = a + *(const LAS f32x4*)(xw + off) + bv;
      st64(rws, addr, u32x2{pk2(gelu_tanh(t[0]), gelu_tanh(t[1])), pk2(gelu_tanh(t[2]), gelu_tanh(t[3]))});
    };
    if (kh == 1) fin(acc[2], 2048, ua[0]);
    else { fin(acc[0], 0, ua[0]); fin(acc[1], 1024, ua[1]); }
  } else {
    DBAR();
    if (threadIdx.x == 0) flag_st(ws, FL_Q + (tn * 16 + tm) * 32, token);
    LBAR();
    qf = __builtin_bit_cast(bf16x8, ld128(rws, WS_Q + (head * 64 + qb * 8 + w) * 1024 + lane * 16));
    asm volatile("" : "+v"(qf));
    LBAR();
    STAMP(3);
  }

  {
    lptr kp = L + L_K + w * 1024 + lane * 16;
    const int i_lo = qb > 0 ? 1 : 8 - w;
    const f32x16 zero = {};
    bf16x8 kf[9];
#pragma unroll
    for (int i = 0; i < 9; ++i) kf[i] = ldsr128(kp + i * 1024);
    f32x16 sb, sa, sc;
    float mx[4];
#define TMAX(S, i) (((i) >= i_lo) ? max16(S, -INFINITY) : -INFINITY)
    sa = MFMA32(kf[8], qf, zero); sc = MFMA32(kf[0], qf, zero);
    f32x16 s1 = MFMA32(kf[1], qf, zero), s2 = MFMA32(kf[2], qf, zero); SB();
#pragma unroll
    for (int i = 0; i < 16; ++i) sb[i] = __builtin_amdgcn_inverse_ballot_w64(diag_mask(i)) ? sa[i] : (qb > 0 ? sc[i] : -INFINITY);
    SB();
    sa = MFMA32(kf[3], qf, zero); sc = MFMA32(kf[4], qf, zero); SB();
    mx[0] = max16(sb, -INFINITY); mx[1] = TMAX(s1, 1); mx[2] = TMAX(s2, 2); SB();
    s1 = MFMA32(kf[5], qf, zero); s2 = MFMA32(kf[6], qf, zero); SB();
    mx[3] = TMAX(sa, 3); mx[0] = __builtin_fmaxf(mx[0], TMAX(sc, 4)); SB();
    sa = MFMA32(kf[7], qf, zero); SB();
    mx[1] = __builtin_fmaxf(mx[1], TMAX(s1, 5)); mx[2] = __builtin_fmaxf(mx[2], TMAX(s2, 6)); mx[3] = __builtin_fmaxf(mx[3], TMAX(sa, 7));
    float m = __builtin_fmaxf(__builtin_fmaxf(mx[0], mx[1]), __builtin_fmaxf(mx[2], mx[3]));
    {
      auto rr = __builtin_amdgcn_permlane32_swap(__float_as_uint(m), __float_as_uint(m), false, false);
      m = __builtin_fmaxf(__uint_as_float(rr[0]), __uint_as_float(rr[1]));
    }
    STAMP(11);
    if (hasG1) {
      DBAR();
      if (threadIdx.x == 0) flag_st(ws, FL_U + (g1dt * 32 + g1ft) * 32, token);
    }
    STAMP(4);
    const int vstr = r < 16 ? 1024 : 0;
    lptr vp0 = L + (r < 16 ? L_V + w * 1024 + h * 256 + r * 16 : (L_C + ((r == 16 || r == 20) ? 0 : 16)));
    f32x16 cin;
#pragma unroll
    for (int i = 0; i < 16; ++i) cin[i] = -m;
    f32x16 s = MFMA32(ldsr128(kp + i_lo * 1024), qf, cin);
    f32x16 o = {};
    {
      u32x4 pd[2], pf[2];
#pragma unroll
      for (int i = 0; i < 16; i += 2) {
        const unsigned pp = pk2(__builtin_amdgcn_exp2f(sb[i] - m), __builtin_amdgcn_exp2f(sb[i + 1] - m));
        const int dlt = r - crow(i, h);
        const unsigned mk = dlt >= 1 ? 0xffffffffu : (dlt == 0 ? 0x0000ffffu : 0u);
        pd[i >> 3][(i >> 1) & 3] = pp & mk;
        pf[i >> 3][(i >> 1) & 3] = pp & ~mk;
      }
      lptr vp = vp0 + 8 * vstr;
      o = MFMA32(ldsr128(vp), __builtin_bit_cast(bf16x8, pd[0]), o);
      o = MFMA32(ldsr128(vp + 512), __builtin_bit_cast(bf16x8, pd[1]), o);
      o = MFMA32(ldsr128(vp0), __builtin_bit_cast(bf16x8, pf[0]), o);
      o = MFMA32(ldsr128(vp0 + 512), __builtin_bit_cast(bf16x8, pf[1]), o);
    }
#pragma unroll 1
    for (int i = i_lo; i < 8; ++i) {
      lptr vp = vp0 + i * vstr;
      const bf16x8 kn = ldsr128(kp + (i + 1) * 1024), v0 = ldsr128(vp), v1 = ldsr128(vp + 512);
      u32x4 p0, p1;
#pragma unroll
      for (int e = 0; e < 8; e += 2) p0[e >> 1] = pk2(__builtin_amdgcn_exp2f(s[e]), __builtin_amdgcn_exp2f(s[e + 1]));
      o = MFMA32(v0, __builtin_bit_cast(bf16x8, p0), o);
#pragma unroll
      for (int e = 8; e < 16; e += 2) p1[(e >> 1) & 3] = pk2(__builtin_amdgcn_exp2f(s[e]), __builtin_amdgcn_exp2f(s[e + 1]));
      s = MFMA32(kn, qf, cin);
      o = MFMA32(v1, __builtin_bit_cast(bf16x8, p1), o);
    }
    const float il = __builtin_amdgcn_rcpf(o[8]);
    u32x4 ov;
#pragma unroll
    for (int jj = 0; jj < 4; ++jj) ov[jj] = pk2(o[2 * jj] * il, o[2 * jj + 1] * il);
    const int B = qb * 8 + w;
    st128(rws, WS_O + (((B >> 1) * 32 + head) * 2 + (B & 1)) * 1024 + lane * 16, ov);
  }
  STAMP(5);

  {
    const int mt = (w >> 1) & 1, nt = w & 1;
    const int tok = st * 64 + nt * 32 + r;
    const int ta2 = kh * 8192 + (8 * h + q4) * 128 + (((nt * 4 + blk * 2 + (p4 >> 1)) ^ ((q4 >> 1) << 2)) << 4) + (p4 & 1) * 8;
    const int tao = kh * 8192 + (4 * h + q4) * 128 + (((mt * 4 + blk * 2 + (p4 >> 1)) ^ ((q4 >> 1) << 2)) << 4) + (p4 & 1) * 8;
    const int fa2 = kh * 8192 + 2048 + mt * 1024 + lane * 16;
    const int fao = kh * 8192 + 2048 + nt * 1024 + lane * 16;
    f32x16 ay = {}, aa = {};
    bf16x8 pa0, pb0, pa1, pb1, qa0, qb0, qa1, qb1;
    float g = 0.f; unsigned b2v = 0;
#define RD2(A0, B0, A1, B1) do { lptr sl = L + so; NEXT_SO(); B0 = cat8(ldstr(sl + ta2), ldstr(sl + ta2 + 512)); A0 = ldsr128(sl + fa2); \
    B1 = cat8(ldstr(sl + ta2 + 4096), ldstr(sl + ta2 + 4096 + 512)); A1 = ldsr128(sl + fa2 + 4096); SB(); } while (0)
#define RDO(A0, B0, A1, B1) do { lptr sl = L + so; NEXT_SO(); A0 = cat8(ldstr(sl + tao), ldstr(sl + tao + 1024)); B0 = ldsr128(sl + fao); \
    A1 = cat8(ldstr(sl + tao + 4096), ldstr(sl + tao + 4096 + 1024)); B1 = ldsr128(sl + fao + 4096); SB(); } while (0)
#define MM2(ACC, A0, B0, A1, B1) do { ACC = MFMA32(A0, B0, ACC); ACC = MFMA32(A1, B1, ACC); SB(); } while (0)
    const unsigned pre = (1u << ODS) | (w == 7 ? 1u << 16 : 0u);
    const unsigned post = (w == 0 ? 1u << ODS : 0u) | (w == 7 ? (1u << ODS) | (1u << 10) | (1u << 12) | (1u << 14) : 0u);
    auto hook = [&](int s) {
      if (RARE((pre >> s) & 1)) { if (s == ODS) asm volatile("s_waitcnt vmcnt(0)" ::: "memory"); else peek_check(pk, rws, fo_off, token); }
      CBAR(6);
      if (RARE((post >> s) & 1)) {
        if (s == ODS) { if (w == 7) *(LAS unsigned*)pk = ~token; else if (lane == 0) flag_st(ws, FL_O + (qb * 32 + head) * 32, token); }
        else peek_issue(lds0 + L_P, ws, fo_off);
      }
      SB();
    };
    LBAR(); STAMP(6);
    RD2(pa0, pb0, pa1, pb1);
    if (kh) { pa0 = bf16x8{}; pa1 = bf16x8{}; pb0 = bf16x8{}; pb1 = bf16x8{}; }
#pragma unroll 1
    for (int s = 2; s < 22; s += 2) {
      CBAR(6); RD2(qa0, qb0, qa1, qb1); MM2(ay, pa0, pb0, pa1, pb1);
      hook(s); RD2(pa0, pb0, pa1, pb1); MM2(ay, qa0, qb0, qa1, qb1);
    }
    g = __uint_as_float(ld32(rws, WS_G + tok * 4));
    b2v = ((const unsigned short*)b2)[tok];
    CBAR(6); RD2(qa0, qb0, qa1, qb1); MM2(ay, pa0, pb0, pa1, pb1);
    STAMP(7);
    CBAR(6); RDO(pa0, pb0, pa1, pb1); MM2(ay, qa0, qb0, qa1, qb1);
#pragma unroll 1
    for (int s = 23; s < 29; s += 2) {
      CBAR(6); RDO(qa0, qb0, qa1, qb1); MM2(aa, pa0, pb0, pa1, pb1);
      CBAR(6); RDO(pa0, pb0, pa1, pb1); MM2(aa, qa0, qb0, qa1, qb1);
    }
    CBAR(6); RDO(qa0, qb0, qa1, qb1); MM2(aa, pa0, pb0, pa1, pb1);
    MM2(aa, qa0, qb0, qa1, qb1);
    STAMP(8);
    const float og = 1.0f - g;
    float v[16];
#pragma unroll
    for (int i = 0; i < 16; ++i) v[i] = ay[i] * og + aa[i] * g;
    LBAR();
    {
      lptr ex = L + w * 2048 + lane * 16;
      f32x4 s0, s1;
#pragma unroll
      for (int jj = 0; jj < 4; ++jj) { s0[jj] = kh ? v[jj] : v[8 + jj]; s1[jj] = kh ? v[4 + jj] : v[12 + jj]; }
      *(LAS f32x4*)(ex) = s0; *(LAS f32x4*)(ex + 1024) = s1;
    }
    LBAR();
    {
      lptr ex = L + (w ^ 4) * 2048 + lane * 16;
      const f32x4 r0 = *(const LAS f32x4*)(ex), r1 = *(const LAS f32x4*)(ex + 1024);
      const float bo = bflo(b2v) * og;
      float f[8];
#pragma unroll
      for (int jj = 0; jj < 4; ++jj) { f[jj] = (kh ? v[8 + jj] : v[jj]) + r0[jj] + bo; f[4 + jj] = (kh ? v[12 + jj] : v[4 + jj]) + r1[jj] + bo; }
      const unsigned a0 = pk2(f[0], f[1]), a1 = pk2(f[2], f[3]), c0 = pk2(f[4], f[5]), c1 = pk2(f[6], f[7]);
      auto x0 = __builtin_amdgcn_permlane32_swap(a0, c0, false, false);
      auto x1 = __builtin_amdgcn_permlane32_swap(a1, c1, false, false);
      const u32x4 ov = {x0[0], x1[0], x0[1], x1[1]};
      __builtin_amdgcn_raw_buffer_store_b128(ov, __builtin_amdgcn_make_buffer_rsrc(out, 0, 2048 * 1024, 0x00020000), tok * 1024 + (dt * 64 + mt * 32 + kh * 16 + h * 8) * 2, 0, 17);
    }
  }
  STAMP(9);
  if (w == 0) STAMP_OUT(ws, 0);
}

extern "C" void kernel_launch(void* const* d_in, const int* in_sizes, int n_in, void* d_out, int out_size, void* d_ws, size_t ws_size,
                              hipStream_t stream) {
  (void)hipFuncSetAttribute((const void*)fused_kernel, hipFuncAttributeMaxDynamicSharedMemorySize, LDS_BYTES);
  fused_kernel<<<256, 768, LDS_BYTES, stream>>>((const char*)d_in[0], (const char*)d_in[1], (const char*)d_in[2], (const char*)d_in[4],
                                                (const char*)d_in[5], (const char*)d_in[6], (const char*)d_in[7], (const char*)d_in[8],
                                                (char*)d_ws, (char*)d_out);
}
```

```cpp
#include <hip/hip_runtime.h>
#include <hip/hip_bf16.h>
#include <stdint.h>

#define LAS __attribute__((address_space(3)))
#define GAS __attribute__((address_space(1)))
#define DI __device__ __forceinline__
typedef __attribute__((ext_vector_type(8))) __bf16 bf16x8;
typedef __attribute__((ext_vector_type(2))) __bf16 bf16x2;
typedef __attribute__((ext_vector_type(4))) short s16x4;
typedef __attribute__((ext_vector_type(2))) float f32x2;
typedef __attribute__((ext_vector_type(4))) float f32x4;
typedef __attribute__((ext_vector_type(16))) float f32x16;
typedef __attribute__((ext_vector_type(2))) unsigned u32x2;
typedef __attribute__((ext_vector_type(4))) unsigned u32x4;
typedef LAS char* lptr;
typedef __amdgpu_buffer_rsrc_t rsrc_t;

constexpr unsigned WS_Q = 0, WS_K = 2u << 20, WS_V = 4u << 20, WS_O = 6u << 20, WS_U = 8u << 20, WS_H = 10u << 20,
                   WS_G = 13u << 20, WS_FL = 14u << 20, WS_END = 15u << 20;
constexpr int FL_H = 0;
constexpr int FL_Q = 64 * 32;
constexpr int FL_U = FL_Q + 192 * 32;
constexpr int FL_O = FL_U + 352 * 32;
constexpr int FL_EPOCH = FL_O + 256 * 32;
constexpr float QSCALE = 0.25f * 1.4426950408889634f;
constexpr int NS = 7, SLOT = 16384, RING = NS * SLOT;
constexpr int L_K = RING, L_V = RING + 16384, L_X = RING + 32768  , L_C = L_X + 12288  , L_P = L_C + 1024  ;
constexpr int LDS_BYTES = 163840;
constexpr unsigned SPIN_CAP = 200000;
#define CODEPF 7
#define PREFETCH 32
#define PFSTEP 1
#define G1ROT 1
#define G2ROT 1
#define KVS 22
#define QDS 10
#define ODS 4
#define QROT 1

#define MFMA32(a, b, c) __builtin_amdgcn_mfma_f32_32x32x16_bf16(a, b, c, 0, 0, 0)
#define MFMA16(a, b, c) __builtin_amdgcn_mfma_f32_16x16x32_bf16(a, b, c, 0, 0, 0)
#define SB() __builtin_amdgcn_sched_barrier(0)
#define LBAR() do { asm volatile("s_waitcnt lgkmcnt(0)\n\ts_barrier" ::: "memory"); SB(); } while (0)
#define CBAR(N) do { asm volatile("s_waitcnt lgkmcnt(" #N ")\n\ts_barrier" ::: "memory"); SB(); } while (0)
#define DBAR() do { asm volatile("s_waitcnt vmcnt(0) lgkmcnt(0)\n\ts_barrier" ::: "memory"); SB(); } while (0)
#define VBAR(N) asm volatile("s_waitcnt vmcnt(" #N ")\n\ts_barrier" ::: "memory")
#define BAR() asm volatile("s_barrier" ::: "memory")

#define STAMP(i) do {} while (0)
#define STAMP_OUT(ws, slot) do {} while (0)

DI int rfl(int x) { return __builtin_amdgcn_readfirstlane(x); }
DI bf16x8 ldsr128(lptr p) { return *(const LAS bf16x8*)p; }
DI s16x4 ldstr(lptr p) { return __builtin_amdgcn_ds_read_tr16_b64_v4i16((LAS s16x4*)p); }
DI bf16x8 cat8(s16x4 a, s16x4 b) { return __builtin_bit_cast(bf16x8, __builtin_shufflevector(a, b, 0, 1, 2, 3, 4, 5, 6, 7)); }
DI unsigned pk2(float a, float b) { f32x2 f = {a, b}; return __builtin_bit_cast(unsigned, __builtin_convertvector(f, bf16x2)); }
DI float bflo(unsigned u) { return __uint_as_float(u << 16); }
DI float bfhi(unsigned u) { return __uint_as_float(u & 0xffff0000u); }
DI int crow(int i, int h) { return (i & 3) + 8 * (i >> 2) + 4 * h; }
DI void st128(rsrc_t r, unsigned off, u32x4 v) { __builtin_amdgcn_raw_buffer_store_b128(v, r, off, 0, 16); }
DI void st64(rsrc_t r, unsigned off, u32x2 v) { __builtin_amdgcn_raw_buffer_store_b64(v, r, off, 0, 16); }
DI void st32(rsrc_t r, unsigned off, unsigned v) { __builtin_amdgcn_raw_buffer_store_b32(v, r, off, 0, 16); }
DI u32x4 ld128(rsrc_t r, unsigned off) { return __builtin_amdgcn_raw_buffer_load_b128(r, off, 0, 16); }
DI unsigned ld32(rsrc_t r, unsigned off) { return __builtin_amdgcn_raw_buffer_load_b32(r, off, 0, 16); }
DI unsigned flag_ld(const char* ws, int idx) { return __hip_atomic_load((const GAS unsigned*)(ws + WS_FL) + idx, __ATOMIC_RELAXED, __HIP_MEMORY_SCOPE_AGENT); }
DI void flag_st(char* ws, int idx, unsigned v) { __hip_atomic_store((GAS unsigned*)(ws + WS_FL) + idx, v, __ATOMIC_RELAXED, __HIP_MEMORY_SCOPE_AGENT); }

#define M0_IN "s_mov_b32 %0, m0\n\ts_mov_b32 m0, %1\n\ts_nop 0\n\t"
#define M0_OUT "s_mov_b32 m0, %0"
DI void dma_2x2(unsigned m0v, const void* b0, unsigned v0, unsigned v1, const void* b1, unsigned v2, unsigned v3) {
  unsigned sv;
  asm volatile(M0_IN "global_load_lds_dwordx4 %4, %2\n\tglobal_load_lds_dwordx4 %5, %2 offset:1024\n\t"
               "global_load_lds_dwordx4 %6, %3 offset:2048\n\tglobal_load_lds_dwordx4 %7, %3 offset:3072\n\t" M0_OUT
               : "=&s"(sv) : "s"(m0v), "s"(b0), "s"(b1), "v"(v0), "v"(v1), "v"(v2), "v"(v3) : "memory");
}
DI void dma_lin4_sc1(unsigned m0v, const void* b, unsigned vl) {
  unsigned sv;
  asm volatile(M0_IN "global_load_lds_dwordx4 %3, %2 sc1\n\tglobal_load_lds_dwordx4 %3, %2 offset:1024 sc1\n\t"
               "global_load_lds_dwordx4 %3, %2 offset:2048 sc1\n\tglobal_load_lds_dwordx4 %3, %2 offset:3072 sc1\n\t" M0_OUT
               : "=&s"(sv) : "s"(m0v), "s"(b), "v"(vl) : "memory");
}
DI void dma_g1(unsigned m0v, const void* wb, unsigned v0, unsigned v1, const void* hb_m2048, unsigned vl) {
  unsigned sv;
  asm volatile(M0_IN "global_load_lds_dwordx4 %4, %2\n\tglobal_load_lds_dwordx4 %5, %2 offset:1024\n\t"
               "global_load_lds_dwordx4 %6, %3 offset:2048 sc1\n\t"
               "s_mov_b32 exec_hi, 0\n\tglobal_load_lds_dwordx4 %6, %3 offset:3072 sc1\n\ts_mov_b32 exec_hi, -1\n\t" M0_OUT
               : "=&s"(sv) : "s"(m0v), "s"(wb), "s"(hb_m2048), "v"(v0), "v"(v1), "v"(vl) : "memory");
}
DI void dma_hh(unsigned m0v, const void* hb_m2048, unsigned vl) {
  unsigned sv;
  asm volatile(M0_IN "global_load_lds_dwordx4 %3, %2 offset:2048 sc1\n\t"
               "s_mov_b32 exec_hi, 0\n\tglobal_load_lds_dwordx4 %3, %2 offset:3072 sc1\n\ts_mov_b32 exec_hi, -1\n\t" M0_OUT
               : "=&s"(sv) : "s"(m0v), "s"(hb_m2048), "v"(vl) : "memory");
}
DI void dma_lin4(unsigned m0v, const void* b, unsigned vl) {
  unsigned sv;
  asm volatile(M0_IN "global_load_lds_dwordx4 %3, %2\n\tglobal_load_lds_dwordx4 %3, %2 offset:1024\n\t"
               "global_load_lds_dwordx4 %3, %2 offset:2048\n\tglobal_load_lds_dwordx4 %3, %2 offset:3072\n\t" M0_OUT
               : "=&s"(sv) : "s"(m0v), "s"(b), "v"(vl) : "memory");
}
DI void dma_touch(unsigned m0v, const void* b, unsigned voff) {
  unsigned sv;
  asm volatile(M0_IN "global_load_lds_dword %3, %2\n\t" M0_OUT : "=&s"(sv) : "s"(m0v), "s"(b), "v"(voff) : "memory");
}
DI void dma_wh(unsigned m0v, const void* wb, unsigned v0, unsigned v1) {
  unsigned sv;
  asm volatile(M0_IN "global_load_lds_dwordx4 %3, %2\n\tglobal_load_lds_dwordx4 %4, %2 offset:1024\n\t" M0_OUT
               : "=&s"(sv) : "s"(m0v), "s"(wb), "v"(v0), "v"(v1) : "memory");
}
DI void dma_fh(unsigned m0v, const void* fb_m2048, unsigned vl) {
  unsigned sv;
  asm volatile(M0_IN "global_load_lds_dwordx4 %3, %2 offset:2048 sc1\n\tglobal_load_lds_dwordx4 %3, %2 offset:3072 sc1\n\t" M0_OUT
               : "=&s"(sv) : "s"(m0v), "s"(fb_m2048), "v"(vl) : "memory");
}

DI void spin1(rsrc_t r, unsigned off, unsigned tok) {
  unsigned n = 0;
#pragma nounroll
  do { const unsigned v = ld32(r, off); if (__all(v == tok)) break; __builtin_amdgcn_s_sleep(4); asm volatile("" ::: "memory"); } while (++n < SPIN_CAP);
  __builtin_amdgcn_fence(__ATOMIC_ACQUIRE, "wavefront");
}
DI void peek_issue(unsigned m0v, const void* ws, unsigned off) {
  unsigned sv;
  asm volatile(M0_IN "global_load_lds_dwordx4 %3, %2 sc1\n\t" M0_OUT : "=&s"(sv) : "s"(m0v), "s"(ws), "v"(off) : "memory");
}
DI void peek_check(lptr p, rsrc_t r, unsigned off, unsigned tok) {
  if (__all(*(const volatile LAS unsigned*)p == tok)) return;
  asm volatile("s_waitcnt vmcnt(0)" ::: "memory");
  if (__all(*(const volatile LAS unsigned*)p == tok)) return;
  spin1(r, off, tok);
}
#define RARE(c) __builtin_expect((c) != 0, 0)

DI float wave_sum(float v) {
#define DPPADD(ctrl, rm) v += __int_as_float(__builtin_amdgcn_update_dpp(0, __float_as_int(v), ctrl, rm, 0xf, false))
  DPPADD(0x111, 0xf); DPPADD(0x112, 0xf); DPPADD(0x114, 0xf); DPPADD(0x118, 0xf); DPPADD(0x142, 0xa); DPPADD(0x143, 0xc);
#undef DPPADD
  return __int_as_float(__builtin_amdgcn_readlane(__float_as_int(v), 63));
}
DI float row16_sum(float v) {
#define DPPX(ctrl) v += __int_as_float(__builtin_amdgcn_update_dpp(0, __float_as_int(v), ctrl, 0xf, 0xf, true))
  DPPX(0xB1); DPPX(0x4E); DPPX(0x141); DPPX(0x140);
#undef DPPX
  return v;
}
DI float gelu_tanh(float u) {
  const float z = u * (0.7978845608028654f + 0.7978845608028654f * 0.044715f * u * u);
  const float e = __builtin_amdgcn_exp2f(z * (-2.0f * 1.4426950408889634f));
  return u * __builtin_amdgcn_rcpf(1.0f + e);
}
constexpr unsigned long long diag_mask(int i) {
  unsigned long long mk = 0;
  for (int l = 0; l < 64; ++l) if ((i & 3) + 8 * (i >> 2) + 4 * (l >> 5) <= (l & 31)) mk |= 1ull << l;
  return mk;
}
DI float max16(const f32x16& s, float m) {
#pragma unroll
  for (int i = 0; i < 16; i += 2) m = __builtin_fmaxf(__builtin_fmaxf(s[i], s[i + 1]), m);
  return m;
}

#define NEXT_SO() so = so == (NS - 1) * SLOT ? 0 : so + SLOT

template <bool ISV, class Hook, class Hook14, class Mid>
DI void qkv_consume(lptr L, int& so, int w, int lane, f32x16 (&acc)[2], Hook hook, Hook14 hook14, Mid mid) {
  const int r = lane & 31, h = lane >> 5, wm = w >> 2, wn = w & 3;
  int xa[4];
#pragma unroll
  for (int s = 0; s < 4; ++s) xa[s] = (wm * 2 + (r >> 4)) * 4096 + (r & 15) * 128 + (((2 * s + h) ^ ((r >> 1) & 7)) << 4);
  const int q = (lane & 15) >> 2, p = lane & 3, blk = (lane >> 4) & 1;
  const int wa = h * 4096 + 2048 + q * 256 + (((wn * 4 + blk * 2 + (p >> 1)) ^ (q << 2)) << 4) + (p & 1) * 8;
  bf16x8 xA[2][4], xB[2][4], wA[4], wB[4];
#define QM(X, W, mi, s) do { if (ISV) acc[mi] = MFMA32(X[mi][s], W[s], acc[mi]); else acc[mi] = MFMA32(W[s], X[mi][s], acc[mi]); SB(); } while (0)
#define QRX(X, mi, s) X[mi][s] = ldsr128(sl + xa[s])
#define QRW(W, mi, e) W[2 * mi + e] = cat8(ldstr(sl + wa + e * 8192), ldstr(sl + wa + e * 8192 + 1024))
#define QSTEP(mi, XN, WN, XP, WP, MM) do { lptr sl = L + so; NEXT_SO(); \
    if (MM) QM(XP, WP, 0, 2 * mi); \
    QRX(XN, mi, 0); QRX(XN, mi, 1); SB(); if (MM) QM(XP, WP, 1, 2 * mi); \
    QRX(XN, mi, 2); QRX(XN, mi, 3); QRW(WN, mi, 0); SB(); if (MM) QM(XP, WP, 0, 2 * mi + 1); \
    QRW(WN, mi, 1); SB(); if (MM) QM(XP, WP, 1, 2 * mi + 1); } while (0)
  LBAR(); QSTEP(0, xA, wA, xB, wB, false); QSTEP(1, xA, wA, xB, wB, false);
#pragma unroll 1
  for (int c = 1; c < 7; c += 2) {
    hook(c);     QSTEP(0, xB, wB, xA, wA, true); QSTEP(1, xB, wB, xA, wA, true);
    hook(c + 1); QSTEP(0, xA, wA, xB, wB, true); QSTEP(1, xA, wA, xB, wB, true);
  }
  hook14(); QSTEP(0, xB, wB, xA, wA, true); QSTEP(1, xB, wB, xA, wA, true);
#pragma unroll
  for (int s = 0; s < 4; ++s) QM(xB, wB, 0, s);
  mid();
#pragma unroll
  for (int s = 0; s < 4; ++s) QM(xB, wB, 1, s);
}

__global__ __launch_bounds__(768, 3) void fused_kernel(const char* __restrict__ x, const char* __restrict__ wqkv, const char* __restrict__ wo,
                                                       const char* __restrict__ w1, const char* __restrict__ b1, const char* __restrict__ w2,
                                                       const char* __restrict__ b2, const char* __restrict__ gw, char* __restrict__ ws,
                                                       char* __restrict__ out) {
  extern __shared__ __attribute__((aligned(16))) char smem[];
  asm volatile("" :: "s"(x), "s"(wqkv), "s"(wo), "s"(w1), "s"(b1), "s"(w2), "s"(b2), "s"(gw), "s"(ws), "s"(out));
  STAMP(0);
  const int b = blockIdx.x, xcd = b & 7, idx = ((b >> 3) + 24) & 31;
  const bool lead = b == 65;
  const int lane = threadIdx.x & 63, w = rfl(threadIdx.x >> 6);
  const unsigned lds0 = (unsigned)(uintptr_t)smem;
  lptr L = (lptr)smem;
  if (CODEPF && w < 4 && (idx & CODEPF) == 0) {
    const char* pc0;
    asm volatile("s_getpc_b64 %0" : "=s"(pc0));
    dma_touch(lds0 + L_P + 1024, pc0, min(w * 8192 + lane * 128, 26112));
  }
  const int tm = (xcd >> 1) * 4 + idx / 6, tn = (xcd & 1) * 6 + idx % 6;
  const int lnb = xcd * 8 + (idx - 24);
  const unsigned x0 = (lane >> 3) * 1024 + (((lane & 7) ^ (lane >> 4)) << 4), x1 = (8 + (lane >> 3)) * 1024 + (((lane & 7) ^ (4 + (lane >> 4))) << 4) - 1024;
  const unsigned q0 = (lane >> 4) * 3072 + (((lane & 15) ^ ((lane >> 4) << 2)) << 4), q1 = q0 + 4 * 3072 - 1024;
  const char* xp = x + (size_t)(tm * 128 + ((w >> 1) & 1) * 64 + (w & 1) * 16) * 1024;
  const char* qp = wqkv + (size_t)((w & 3) * 8) * 3072 + tn * 256 - 2048;
  int kc = QROT ? ((idx / 6 + idx % 6) % 3) * 3 : 0;
  if (w >= 8) {
    if (idx < 24) {
#pragma unroll 1
      for (int f = 0; f < 7; ++f) {
        dma_2x2(lds0 + (w & 3) * 4096 + f * SLOT, xp + kc * 128 + (f & 1) * 32 * 1024, x0, x1, qp + (size_t)(kc * 2 + (f & 1)) * (32 * 3072), q0, q1);
        kc = (kc + (f & 1)) & 7;
      }
    }
  }
  asm volatile("" ::: "memory");
  SB();
  const int gi = 31 - idx;
  const bool hasQ = idx < 24, hasG1 = xcd == 0 || gi < 30;
  const bool lowd = xcd == 0 ? gi < 12 : xcd < 5;
  const int gj = xcd == 0 && !lowd ? gi - 12 : gi;
  const int g1ft = lowd ? (xcd == 0 ? 20 : (xcd - 1) * 5) + gj / 6 : (xcd == 0 ? 18 : (xcd - 5) * 6) + gj / 5;
  const int g1dt = lowd ? gj % 6 : 6 + gj % 5;
  const int head = xcd * 4 + (idx & 3), qb = idx >> 2;
  const int st = (xcd >> 1) * 8 + (idx >> 2), dt = (xcd & 1) * 4 + (idx & 3);
  unsigned fq_off;
  { const int l = lane < 12 ? lane : 3, wh = l >> 2; int tmi = l & 3; if (wh == 0 || qb == 0) tmi |= 2;
    fq_off = WS_FL + (FL_Q + ((wh * 4 + (head >> 3)) * 16 + 2 * qb - 2 + tmi) * 32) * 4; }
  const unsigned fu_off = WS_FL + (FL_U + (((lane & 32) ? (4 * dt + 3) / 3 : (4 * dt) / 3) * 32 + min(lane & 31, 21)) * 32) * 4;
  const unsigned fo_off = WS_FL + (FL_O + ((st >> 2) * 32 + (lane & 31)) * 32) * 4;
  const unsigned fh_off = WS_FL + FL_H * 4 + lane * 128;
  const rsrc_t rws = __builtin_amdgcn_make_buffer_rsrc(ws, 0, WS_END, 0x00020000);
  volatile LAS unsigned* relay = (volatile LAS unsigned*)(L + L_C + 64);
  unsigned eoff = WS_FL + FL_EPOCH * 4;
  asm volatile("" : "+v"(eoff));
  const unsigned epoch = ld32(rws, eoff);
#define TOKEN_OF(e) ((e) * 1664525u + 1013904223u)

  if (w >= 8) {
    const int j = w - 8;
#define token TOKEN_OF(epoch)
    const unsigned m0b = lds0 + j * 4096;
    const unsigned vl = lane * 16;
    unsigned so = 0;
    const char* hp = ws + WS_H + (size_t)g1dt * 64 * 3072 + j * 1536 - 2048;
    const char* w1p = w1 + (size_t)(j * 16) * 2752 + g1ft * 128;
    const unsigned u0 = (lane >> 3) * 2752 + ((((lane & 7) ^ (((lane >> 4) & 3) << 1)) & (g1ft == 21 ? 3 : 7)) << 4), u1 = u0 + 8 * 2752 - 1024;
    int cw = G1ROT ? (lowd ? g1dt * 6 : (g1dt - 6) * 6 + 3) : 0, ch = cw;
    auto fill_g1 = [&]() { dma_g1(m0b + so, w1p + (size_t)cw * (64 * 2752), u0, u1, hp + cw * 6144, vl); cw = (cw + 1) & 31; NEXT_SO(); };
    auto fill_g1w = [&]() { dma_wh(m0b + so, w1p + (size_t)cw * (64 * 2752), u0, u1); cw = (cw + 1) & 31; NEXT_SO(); };
    auto fill_g1h3 = [&](unsigned so0) {
      const unsigned keep = so; so = so0;
#pragma unroll 1
      for (int i = 0; i < 3; ++i) { dma_hh(m0b + so, hp + ch * 6144, vl); ch = (ch + 1) & 31; NEXT_SO(); }
      so = keep;
    };
    const unsigned swz = ((lane & 7) ^ (((lane >> 4) & 1) << 2)) << 4;
    unsigned vw0 = (lane >> 3) * 4096 + swz, vw1 = vw0 + 8 * 4096 - 1024;
    const char* w2p = w2 + (size_t)(j * 16) * 4096 + st * 128;
    const char* up = ws + WS_U + (size_t)dt * 86 * 2048 + j * 2048 - 2048;
    const size_t w2half = (size_t)(j < 2 ? 21 * 64 : 20 * 64) * 4096;
    const int uhalf = j < 2 ? 21 * 8192 : 20 * 8192;
    auto fill_kv = [&]() {
      int t0 = qb * 8 - 8 + 4 * j; t0 = t0 < 0 ? t0 + 8 : t0;
      dma_lin4_sc1(lds0 + L_K + j * 4096, ws + WS_K + (size_t)(head * 64 + t0) * 1024, vl);
      dma_lin4_sc1(lds0 + L_V + j * 4096, ws + WS_V + (size_t)(head * 64 + t0) * 1024, vl);
    };
    int nwh = 0;
    const int r2 = G2ROT ? (((dt & 3) * 2 + (dt >> 2)) * 21) >> 3 : 0;
    int c2 = r2;
    auto fill_wh = [&]() {
      dma_wh(m0b + so, nwh == 0 ? w2p + w2half : w2p + (size_t)c2 * (64 * 4096), vw0, vw1);
      if (nwh) c2 = c2 == 20 ? 0 : c2 + 1;
      ++nwh; NEXT_SO();
    };
    unsigned so_g2 = 0;
    auto warm_g2 = [&]() {
      if (PREFETCH & 48) {
        const int t = idx * 4 + j;
        if ((PREFETCH & 16) && t < 120) dma_touch(lds0 + L_P + 1024, w2 + (size_t)(384 + t * 8) * 4096 + (xcd >> 1) * 1024, (lane >> 3) * 4096 + (lane & 7) * 128);
        if (j == 1) dma_touch(lds0 + L_P + 1024, wo + (size_t)(idx * 16) * 1024 + (xcd & 1) * 512, (lane >> 2) * 1024 + (lane & 3) * 128);
      }
    };

    if (hasQ) {
      auto fill_a = [&]() { dma_2x2(m0b + so, xp + kc * 128, x0, x1, qp + (size_t)kc * 64 * 3072, q0, q1); NEXT_SO(); };
      auto fill_b = [&]() { dma_2x2(m0b + so, xp + kc * 128 + 32 * 1024, x0, x1, qp + (size_t)kc * 64 * 3072 + 32 * 3072, q0, q1); kc = (kc + 1) & 7; NEXT_SO(); };
      VBAR(20);
#pragma unroll 1
      for (int c = 1; c < 5; ++c) { VBAR(12); fill_b(); fill_a(); }
      VBAR(12); fill_b();
      if (hasG1) {
        const unsigned so0 = so;
        fill_g1w();
        VBAR(10); fill_g1w(); fill_g1w();
        VBAR(6); fill_g1h3(so0); fill_g1(); fill_g1();
      } else {
        VBAR(8); VBAR(0);
      }
    } else {
      BAR();
      VBAR(0);
#pragma unroll 1
      for (int s = 0; s < 3; ++s) fill_g1w();
      if (j == 0) spin1(rws, fh_off, token);
      else if (PREFETCH & 2) {
        const int pw = lnb * 3 + (j - 1);
#pragma unroll 1
        for (int i = pw; i < 752; i += 192) dma_touch(lds0 + L_P + 1024, i < 688 ? w2 + (size_t)i * 8192 : wo + (size_t)(i - 688) * 8192, lane * 128);
      } else if (PREFETCH & 4) {
        const int pw = lnb * 3 + (j - 1);
#pragma unroll 1
        for (int i = pw; i < 688; i += 192) dma_touch(lds0 + L_P + 1024, w1 + (size_t)i * 8192, lane * 128);
      }
      BAR();
      fill_g1h3(0); fill_g1(); fill_g1();
    }
    STAMP(1);
    if (hasG1) {
      VBAR(12); fill_g1(); VBAR(14); fill_g1();
#pragma unroll 1
      for (int s = 2; s <= KVS; ++s) { VBAR(16); fill_g1(); }
      fill_kv();
#pragma unroll 1
      for (int s = KVS + 1; s < 27; ++s) { VBAR(16); fill_g1(); }
      so_g2 = so;
      VBAR(16); fill_wh(); VBAR(14); fill_wh(); VBAR(12); fill_wh(); VBAR(10); fill_wh(); VBAR(8); fill_wh();
      BAR(); fill_wh(); fill_wh();
      warm_g2();
      BAR();
      BAR();
    } else {
      BAR();
      so_g2 = so;
#pragma unroll 1
      for (int i = 0; i < 7; ++i) fill_wh();
      warm_g2();
      if (j == 0) spin1(rws, fq_off, token);
      BAR();
      fill_kv();
      VBAR(0);
    }
    STAMP(2);
    {
      if (j == 0) { spin1(rws, fu_off, token); *relay = 1u; }
      else { for (unsigned spins = 0; spins < 64 * SPIN_CAP && *relay == 0u; ++spins) __builtin_amdgcn_s_sleep(1); }
      STAMP(3);
      so = so_g2;
      dma_fh(m0b + so, up + uhalf, vl); NEXT_SO();
#pragma unroll 1
      for (int i = 0, c = r2; i < 6; ++i) { dma_fh(m0b + so, up + c * 8192, vl); c = c == 20 ? 0 : c + 1; NEXT_SO(); }
    }
    unsigned wsh = 18;
    int ci = 7;
    auto fill_g2 = [&]() {
      if (ci == 22) {
        w2p = wo + (size_t)(j * 16) * 1024 + dt * 128; up = ws + WS_O + (size_t)st * 32 * 2048 + j * 2048 - 2048; wsh = 16; c2 = 0;
        vw0 = (lane >> 3) * 1024 + swz; vw1 = vw0 + 8 * 1024 - 1024;
      }
      dma_wh(m0b + so, w2p + ((size_t)c2 << wsh), vw0, vw1); dma_fh(m0b + so, up + c2 * 8192, vl);
      c2 = c2 == 20 ? 0 : c2 + 1; NEXT_SO(); ++ci;
    };
    VBAR(12); VBAR(10);
    VBAR(8); fill_g2(); VBAR(10); fill_g2(); VBAR(12); fill_g2(); VBAR(14); fill_g2();
#pragma unroll 1
    for (int s = 6; s < 25; ++s) { VBAR(16); fill_g2(); }
    const unsigned fa_off = WS_FL + FL_O * 4 + (j * 64 + lane) * 128;
    if (lead) peek_issue(lds0 + L_X + j * 1024, ws, fa_off);
    VBAR(16); VBAR(12); VBAR(8); VBAR(4); VBAR(0);
    if (lead) peek_check(L + L_X + j * 1024 + lane * 16, rws, fa_off, token);
    BAR();
    if (lead && j == 0 && lane == 0) st32(rws, WS_FL + FL_EPOCH * 4, token);
    BAR();
    STAMP(4);
    if (j == 0) STAMP_OUT(ws, 1);
    return;
  }

#undef token
  unsigned token;
  u32x4 xv[4], gv[4];
  if (!hasQ) {
#pragma unroll
    for (int i = 0; i < 4; ++i) xv[i] = *(const u32x4*)(x + (size_t)(lnb * 32 + w * 4 + (lane >> 4)) * 1024 + (lane & 15) * 16 + i * 256);
#pragma unroll
    for (int i = 0; i < 4; ++i) gv[i] = *(const u32x4*)(gw + (lane & 15) * 16 + i * 256);
  }
  asm volatile("" ::: "memory");
  SB();
  const int r = lane & 31, h = lane >> 5;
  const int g4 = lane >> 4, q4 = (lane & 15) >> 2, p4 = lane & 3, blk = (lane >> 4) & 1;
  int so = 0;
  if (threadIdx.x == 4) *relay = 0u;
  if (threadIdx.x < 4) {
    const unsigned cv = (threadIdx.x & 1) ? 0u : 0x3f803f80u;
    *(LAS u32x4*)(L + L_C + (threadIdx.x >> 1) * 512 + (threadIdx.x & 1) * 16) = u32x4{cv, cv, cv, cv};
  }
  lptr pk = L + L_P + lane * 16;

  const int kh = w >> 2, fb = w & 3;
  int ha[3];
#pragma unroll
  for (int db = 0; db < 3; ++db) { const int o = (kh * 3 + db) * 1024 + lane * 16, rg = o / 1536; ha[db] = rg * 4096 + 2048 + (o - rg * 1536); }
  const int wa = kh * 8192 + (4 * g4 + q4) * 128 + ((fb ^ ((2 * g4 + (q4 >> 1)) & 3)) << 5) + p4 * 8;
  f32x4 acc[3] = {};
  bf16x8 wA = {}, hA[3] = {}, wB = {}, hB[3] = {};
#define G1RD(W, H) do { lptr sl = L + so; NEXT_SO(); W = cat8(ldstr(sl + wa), ldstr(sl + wa + 4096)); \
  _Pragma("unroll") for (int db = 0; db < 3; ++db) H[db] = ldsr128(sl + ha[db]); SB(); } while (0)
#define G1MM(W, H) do { _Pragma("unroll") for (int db = 0; db < 3; ++db) acc[db] = MFMA16(W, H[db], acc[db]); SB(); } while (0)

  token = TOKEN_OF(epoch);
  asm volatile("" : "+v"(token), "+v"(gv[3]));
  if (hasQ) {
    if (w == 7) *(LAS unsigned*)pk = ~token;
    STAMP(1);
    f32x16 qacc[2] = {};
    const bool poller = w == 7 && hasG1;
    const int pi = (xcd * 24 + idx) * 4 + w;
    const unsigned post = (poller ? (1u << 4) | (1u << 5) | (1u << 6) : 0u) | (((PREFETCH & 1) && w < 4 && pi < 688) ? 1u << PFSTEP : 0u);
    if (post & 1) dma_touch(lds0 + L_P + 1024, w1 + (size_t)pi * 8192, lane * 128);
    auto hook = [&](int s) {
      LBAR();
      if (RARE((post >> s) & 1)) { if (w == 7) peek_issue(lds0 + L_P, ws, fh_off); else dma_touch(lds0 + L_P + 1024, w1 + (size_t)pi * 8192, lane * 128); }
      SB();
    };
    auto hook14 = [&]() { if (poller) peek_check(pk, rws, fh_off, token); LBAR(); };
    auto mid = [&]() { if (hasG1) { LBAR(); G1RD(wA, hA); } };
    if (tn >= 8) qkv_consume<true>(L, so, w, lane, qacc, hook, hook14, mid);
    else qkv_consume<false>(L, so, w, lane, qacc, hook, hook14, mid);
    STAMP(2);
    const int wm = w >> 2, wn = w & 3;
    const int hd0 = (tn & 3) * 8 + wn * 2;
    const float sc = tn < 4 ? QSCALE : 1.0f;
    const unsigned dst = tn >= 8 ? WS_V + (((hd0 + (r >> 4)) * 64 + tm * 4 + wm * 2) * 64 + h * 16 + (r & 15)) * 16
                                 : (tn < 4 ? WS_Q : WS_K) + ((hd0 * 64 + tm * 4 + wm * 2) * 64 + lane) * 16;
    const unsigned dstep = tn >= 8 ? 512 : 65536;
#pragma unroll
    for (int mi = 0; mi < 2; ++mi) {
#pragma unroll
      for (int s = 0; s < 2; ++s) {
        u32x4 v;
#pragma unroll
        for (int jj = 0; jj < 4; ++jj) v[jj] = pk2(qacc[mi][8 * s + 2 * jj] * sc, qacc[mi][8 * s + 2 * jj + 1] * sc);
        st128(rws, dst + mi * 1024 + s * dstep, v);
      }
      if (mi == 0 && hasG1) { SB(); LBAR(); G1RD(wB, hB); G1MM(wA, hA); }
    }
  } else {
    constexpr int RS = 1056;
    f32x2 v[16], s2 = {0.f, 0.f}, g2 = {0.f, 0.f};
#pragma unroll
    for (int i = 0; i < 16; ++i) {
      v[i] = f32x2{bflo(xv[i >> 2][i & 3]), bfhi(xv[i >> 2][i & 3])};
      s2 += v[i];
      g2 += v[i] * f32x2{bflo(gv[i >> 2][i & 3]), bfhi(gv[i >> 2][i & 3])};
    }
    const float mean = row16_sum(s2[0] + s2[1]) * (1.0f / 512), gs = row16_sum(g2[0] + g2[1]);
    f32x2 q2 = {0.f, 0.f};
#pragma unroll
    for (int i = 0; i < 16; ++i) { v[i] -= f32x2{mean, mean}; q2 += v[i] * v[i]; }
    const float rs = __builtin_amdgcn_rsqf(row16_sum(q2[0] + q2[1]) * (1.0f / 512) + 1e-5f);
#pragma unroll
    for (int i = 0; i < 4; ++i) {
      u32x4 ov;
#pragma unroll
      for (int jj = 0; jj < 4; ++jj) { const f32x2 t = v[4 * i + jj] * f32x2{rs, rs}; ov[jj] = pk2(t[0], t[1]); }
      *(LAS u32x4*)(L + (w * 4 + g4) * RS + (lane & 15) * 16 + i * 256) = ov;
    }
    if ((lane & 15) == 0) st32(rws, WS_G + (lnb * 32 + w * 4 + g4) * 4, __float_as_uint(1.0f / (1.0f + __expf(-gs))));
    STAMP(1);
    LBAR();
#pragma unroll
    for (int i = 0; i < 4; ++i) {
      const int D = w * 4 + i, d48 = D / 3;
      lptr a = L + (4 * g4 + q4) * RS + D * 32 + p4 * 8;
      const bf16x8 f = cat8(ldstr(a), ldstr(a + 16 * RS));
      st128(rws, WS_H + ((d48 * 64 + lnb) * 3 + (D - d48 * 3)) * 1024 + lane * 16, __builtin_bit_cast(u32x4, f));
      if (i == 3 && w == 7) st128(rws, WS_H + ((10 * 64 + lnb) * 3 + 2) * 1024 + lane * 16, __builtin_bit_cast(u32x4, f));
    }
    DBAR();
    if (threadIdx.x == 0) flag_st(ws, FL_H + lnb * 32, token);
    STAMP(2);
    LBAR();
    LBAR(); G1RD(wA, hA);
    LBAR(); G1RD(wB, hB); G1MM(wA, hA);
  }

  bf16x8 qf;
  if (hasG1) {
    const unsigned pre = (1u << QDS) | (w == 7 ? 1u << KVS : 0u);
    const unsigned post = (w == 0 && hasQ ? 1u << QDS : 0u) | (w == 7 ? (1u << QDS) | (1u << (KVS - 6)) | (1u << (KVS - 4)) | (1u << (KVS - 2)) : 0u);
    auto hook = [&](int s) {
      if (RARE((pre >> s) & 1)) { if (s == QDS) asm volatile("s_waitcnt vmcnt(0)" ::: "memory"); else peek_check(pk, rws, fq_off, token); }
      CBAR(5);
      if (RARE((post >> s) & 1)) {
        if (s == QDS) { if (w == 7) *(LAS unsigned*)pk = ~token; else if (lane == 0) flag_st(ws, FL_Q + (tn * 16 + tm) * 32, token); }
        else peek_issue(lds0 + L_P, ws, fq_off);
      }
      SB();
    };
#pragma unroll 1
    for (int s = 2; s < KVS + 2; s += 2) {
      hook(s); G1RD(wA, hA); G1MM(wB, hB);
      CBAR(5); G1RD(wB, hB); G1MM(wA, hA);
    }
    STAMP(10);
    qf = __builtin_bit_cast(bf16x8, ld128(rws, WS_Q + (head * 64 + qb * 8 + w) * 1024 + lane * 16));
    const int fblk = g1ft * 4 + fb;
    u32x2 bb = *(const u32x2*)(b1 + min(fblk * 16 + 4 * g4, 1372) * 2);
    unsigned ua[2];
#pragma unroll
    for (int e = 0; e < 2; ++e) {
      const int D = g1dt * 3 + (kh ? 2 : e);
      ua[e] = WS_U + ((((D >> 2) * 86 + fblk) * 2 + ((D >> 1) & 1)) * 64 + (g4 >> 1) * 32 + (D & 1) * 16 + (lane & 15)) * 16 + (g4 & 1) * 8;
      if (fblk >= 86 || D >= 32) ua[e] = WS_END;
    }
    lptr xw = L + L_X + (fb * 3 * 64 + lane) * 16;
#pragma unroll 1
    for (int s = KVS + 2; s < 32; s += 2) {
      CBAR(5); G1RD(wA, hA); G1MM(wB, hB);
      CBAR(5); G1RD(wB, hB); G1MM(wA, hA);
    }
    asm volatile("" : "+v"(qf), "+v"(bb), "+v"(ua[0]), "+v"(ua[1]));
    const f32x4 bv = {bflo(bb[0]), bfhi(bb[0]), bflo(bb[1]), bfhi(bb[1])};
    LBAR();
    G1MM(wB, hB);
    STAMP(3);
    if (kh == 1) { *(LAS f32x4*)(xw) = acc[0]; *(LAS f32x4*)(xw + 1024) = acc[1]; }
    else *(LAS f32x4*)(xw + 2048) = acc[2];
    LBAR();
    auto fin = [&](const f32x4& a, int off, unsigned addr) {
      const f32x4 t = a + *(const LAS f32x4*)(xw + off) + bv;
      st64(rws, addr, u32x2{pk2(gelu_tanh(t[0]), gelu_tanh(t[1])), pk2(gelu_tanh(t[2]), gelu_tanh(t[3]))});
    };
    if (kh == 1) fin(acc[2], 2048, ua[0]);
    else { fin(acc[0], 0, ua[0]); fin(acc[1], 1024, ua[1]); }
  } else {
    DBAR();
    if (threadIdx.x == 0) flag_st(ws, FL_Q + (tn * 16 + tm) * 32, token);
    LBAR();
    qf = __builtin_bit_cast(bf16x8, ld128(rws, WS_Q + (head * 64 + qb * 8 + w) * 1024 + lane * 16));
    asm volatile("" : "+v"(qf));
    LBAR();
    STAMP(3);
  }

  {
    lptr kp = L + L_K + w * 1024 + lane * 16;
    const int i_lo = qb > 0 ? 1 : 8 - w;
    const f32x16 zero = {};
    bf16x8 kf[9];
#pragma unroll
    for (int i = 0; i < 9; ++i) kf[i] = ldsr128(kp + i * 1024);
    f32x16 sb, sa, sc;
    float mx[4];
#define TMAX(S, i) (((i) >= i_lo) ? max16(S, -INFINITY) : -INFINITY)
    sa = MFMA32(kf[8], qf, zero); sc = MFMA32(kf[0], qf, zero);
    f32x16 s1 = MFMA32(kf[1], qf, zero), s2 = MFMA32(kf[2], qf, zero); SB();
#pragma unroll
    for (int i = 0; i < 16; ++i) sb[i] = __builtin_amdgcn_inverse_ballot_w64(diag_mask(i)) ? sa[i] : (qb > 0 ? sc[i] : -INFINITY);
    SB();
    sa = MFMA32(kf[3], qf, zero); sc = MFMA32(kf[4], qf, zero); SB();
    mx[0] = max16(sb, -INFINITY); mx[1] = TMAX(s1, 1); mx[2] = TMAX(s2, 2); SB();
    s1 = MFMA32(kf[5], qf, zero); s2 = MFMA32(kf[6], qf, zero); SB();
    mx[3] = TMAX(sa, 3); mx[0] = __builtin_fmaxf(mx[0], TMAX(sc, 4)); SB();
    sa = MFMA32(kf[7], qf, zero); SB();
    mx[1] = __builtin_fmaxf(mx[1], TMAX(s1, 5)); mx[2] = __builtin_fmaxf(mx[2], TMAX(s2, 6)); mx[3] = __builtin_fmaxf(mx[3], TMAX(sa, 7));
    float m = __builtin_fmaxf(__builtin_fmaxf(mx[0], mx[1]), __builtin_fmaxf(mx[2], mx[3]));
    {
      auto rr = __builtin_amdgcn_permlane32_swap(__float_as_uint(m), __float_as_uint(m), false, false);
      m = __builtin_fmaxf(__uint_as_float(rr[0]), __uint_as_float(rr[1]));
    }
    STAMP(11);
    if (hasG1) {
      DBAR();
      if (threadIdx.x == 0) flag_st(ws, FL_U + (g1dt * 32 + g1ft) * 32, token);
    }
    STAMP(4);
    const int vstr = r < 16 ? 1024 : 0;
    lptr vp0 = L + (r < 16 ? L_V + w * 1024 + h * 256 + r * 16 : (L_C + ((r == 16 || r == 20) ? 0 : 16)));
    f32x16 cin;
#pragma unroll
    for (int i = 0; i < 16; ++i) cin[i] = -m;
    f32x16 s = MFMA32(ldsr128(kp + i_lo * 1024), qf, cin);
    f32x16 o = {};
    {
      u32x4 pd[2], pf[2];
#pragma unroll
      for (int i = 0; i < 16; i += 2) {
        const unsigned pp = pk2(__builtin_amdgcn_exp2f(sb[i] - m), __builtin_amdgcn_exp2f(sb[i + 1] - m));
        const int dlt = r - crow(i, h);
        const unsigned mk = dlt >= 1 ? 0xffffffffu : (dlt == 0 ? 0x0000ffffu : 0u);
        pd[i >> 3][(i >> 1) & 3] = pp & mk;
        pf[i >> 3][(i >> 1) & 3] = pp & ~mk;
      }
      lptr vp = vp0 + 8 * vstr;
      o = MFMA32(ldsr128(vp), __builtin_bit_cast(bf16x8, pd[0]), o);
      o = MFMA32(ldsr128(vp + 512), __builtin_bit_cast(bf16x8, pd[1]), o);
      o = MFMA32(ldsr128(vp0), __builtin_bit_cast(bf16x8, pf[0]), o);
      o = MFMA32(ldsr128(vp0 + 512), __builtin_bit_cast(bf16x8, pf[1]), o);
    }
#pragma unroll 1
    for (int i = i_lo; i < 8; ++i) {
      lptr vp = vp0 + i * vstr;
      const bf16x8 kn = ldsr128(kp + (i + 1) * 1024), v0 = ldsr128(vp), v1 = ldsr128(vp + 512);
      u32x4 p0, p1;
#pragma unroll
      for (int e = 0; e < 8; e += 2) p0[e >> 1] = pk2(__builtin_amdgcn_exp2f(s[e]), __builtin_amdgcn_exp2f(s[e + 1]));
      o = MFMA32(v0, __builtin_bit_cast(bf16x8, p0), o);
#pragma unroll
      for (int e = 8; e < 16; e += 2) p1[(e >> 1) & 3] = pk2(__builtin_amdgcn_exp2f(s[e]), __builtin_amdgcn_exp2f(s[e + 1]));
      s = MFMA32(kn, qf, cin);
      o = MFMA32(v1, __builtin_bit_cast(bf16x8, p1), o);
    }
    const float il = __builtin_amdgcn_rcpf(o[8]);
    u32x4 ov;
#pragma unroll
    for (int jj = 0; jj < 4; ++jj) ov[jj] = pk2(o[2 * jj] * il, o[2 * jj + 1] * il);
    const int B = qb * 8 + w;
    st128(rws, WS_O + (((B >> 1) * 32 + head) * 2 + (B & 1)) * 1024 + lane * 16, ov);
  }
  STAMP(5);

  {
    const int mt = (w >> 1) & 1, nt = w & 1;
    const int tok = st * 64 + nt * 32 + r;
    const int ta2 = kh * 8192 + (8 * h + q4) * 128 + (((nt * 4 + blk * 2 + (p4 >> 1)) ^ ((q4 >> 1) << 2)) << 4) + (p4 & 1) * 8;
    const int tao = kh * 8192 + (4 * h + q4) * 128 + (((mt * 4 + blk * 2 + (p4 >> 1)) ^ ((q4 >> 1) << 2)) << 4) + (p4 & 1) * 8;
    const int fa2 = kh * 8192 + 2048 + mt * 1024 + lane * 16;
    const int fao = kh * 8192 + 2048 + nt * 1024 + lane * 16;
    f32x16 ay = {}, aa = {};
    bf16x8 pa0, pb0, pa1, pb1, qa0, qb0, qa1, qb1;
    float g = 0.f; unsigned b2v = 0;
#define RD2(A0, B0, A1, B1) do { lptr sl = L + so; NEXT_SO(); B0 = cat8(ldstr(sl + ta2), ldstr(sl + ta2 + 512)); A0 = ldsr128(sl + fa2); \
    B1 = cat8(ldstr(sl + ta2 + 4096), ldstr(sl + ta2 + 4096 + 512)); A1 = ldsr128(sl + fa2 + 4096); SB(); } while (0)
#define RDO(A0, B0, A1, B1) do { lptr sl = L + so; NEXT_SO(); A0 = cat8(ldstr(sl + tao), ldstr(sl + tao + 1024)); B0 = ldsr128(sl + fao); \
    A1 = cat8(ldstr(sl + tao + 4096), ldstr(sl + tao + 4096 + 1024)); B1 = ldsr128(sl + fao + 4096); SB(); } while (0)
#define MM2(ACC, A0, B0, A1, B1) do { ACC = MFMA32(A0, B0, ACC); ACC = MFMA32(A1, B1, ACC); SB(); } while (0)
    const unsigned pre = (1u << ODS) | (w == 7 ? 1u << 16 : 0u);
    const unsigned post = (w == 0 ? 1u << ODS : 0u) | (w == 7 ? (1u << ODS) | (1u << 10) | (1u << 12) | (1u << 14) : 0u);
    auto hook = [&](int s) {
      if (RARE((pre >> s) & 1)) { if (s == ODS) asm volatile("s_waitcnt vmcnt(0)" ::: "memory"); else peek_check(pk, rws, fo_off, token); }
      CBAR(6);
      if (RARE((post >> s) & 1)) {
        if (s == ODS) { if (w == 7) *(LAS unsigned*)pk = ~token; else if (lane == 0) flag_st(ws, FL_O + (qb * 32 + head) * 32, token); }
        else peek_issue(lds0 + L_P, ws, fo_off);
      }
      SB();
    };
    LBAR(); STAMP(6);
    RD2(pa0, pb0, pa1, pb1);
    if (kh) { pa0 = bf16x8{}; pa1 = bf16x8{}; pb0 = bf16x8{}; pb1 = bf16x8{}; }
#pragma unroll 1
    for (int s = 2; s < 22; s += 2) {
      CBAR(6); RD2(qa0, qb0, qa1, qb1); MM2(ay, pa0, pb0, pa1, pb1);
      hook(s); RD2(pa0, pb0, pa1, pb1); MM2(ay, qa0, qb0, qa1, qb1);
    }
    g = __uint_as_float(ld32(rws, WS_G + tok * 4));
    b2v = ((const unsigned short*)b2)[tok];
    CBAR(6); RD2(qa0, qb0, qa1, qb1); MM2(ay, pa0, pb0, pa1, pb1);
    STAMP(7);
    CBAR(6); RDO(pa0, pb0, pa1, pb1); MM2(ay, qa0, qb0, qa1, qb1);
#pragma unroll 1
    for (int s = 23; s < 29; s += 2) {
      CBAR(6); RDO(qa0, qb0, qa1, qb1); MM2(aa, pa0, pb0, pa1, pb1);
      CBAR(6); RDO(pa0, pb0, pa1, pb1); MM2(aa, qa0, qb0, qa1, qb1);
    }
    CBAR(6); RDO(qa0, qb0, qa1, qb1); MM2(aa, pa0, pb0, pa1, pb1);
    MM2(aa, qa0, qb0, qa1, qb1);
    STAMP(8);
    const float og = 1.0f - g;
    float v[16];
#pragma unroll
    for (int i = 0; i < 16; ++i) v[i] = ay[i] * og + aa[i] * g;
    LBAR();
    {
      lptr ex = L + w * 2048 + lane * 16;
      f32x4 s0, s1;
#pragma unroll
      for (int jj = 0; jj < 4; ++jj) { s0[jj] = kh ? v[jj] : v[8 + jj]; s1[jj] = kh ? v[4 + jj] : v[12 + jj]; }
      *(LAS f32x4*)(ex) = s0; *(LAS f32x4*)(ex + 1024) = s1;
    }
    LBAR();
    {
      lptr ex = L + (w ^ 4) * 2048 + lane * 16;
      const f32x4 r0 = *(const LAS f32x4*)(ex), r1 = *(const LAS f32x4*)(ex + 1024);
      const float bo = bflo(b2v) * og;
      float f[8];
#pragma unroll
      for (int jj = 0; jj < 4; ++jj) { f[jj] = (kh ? v[8 + jj] : v[jj]) + r0[jj] + bo; f[4 + jj] = (kh ? v[12 + jj] : v[4 + jj]) + r1[jj] + bo; }
      const unsigned a0 = pk2(f[0], f[1]), a1 = pk2(f[2], f[3]), c0 = pk2(f[4], f[5]), c1 = pk2(f[6], f[7]);
      auto x0 = __builtin_amdgcn_permlane32_swap(a0, c0, false, false);
      auto x1 = __builtin_amdgcn_permlane32_swap(a1, c1, false, false);
      const u32x4 ov = {x0[0], x1[0], x0[1], x1[1]};
      __builtin_amdgcn_raw_buffer_store_b128(ov, __builtin_amdgcn_make_buffer_rsrc(out, 0, 2048 * 1024, 0x00020000), tok * 1024 + (dt * 64 + mt * 32 + kh * 16 + h * 8) * 2, 0, 2);
    }
  }
  STAMP(9);
  if (w == 0) STAMP_OUT(ws, 0);
}

extern "C" void kernel_launch(void* const* d_in, const int* in_sizes, int n_in, void* d_out, int out_size, void* d_ws, size_t ws_size,
                              hipStream_t stream) {
  (void)hipFuncSetAttribute((const void*)fused_kernel, hipFuncAttributeMaxDynamicSharedMemorySize, LDS_BYTES);
  fused_kernel<<<256, 768, LDS_BYTES, stream>>>((const char*)d_in[0], (const char*)d_in[1], (const char*)d_in[2], (const char*)d_in[4],
                                                (const char*)d_in[5], (const char*)d_in[6], (const char*)d_in[7], (const char*)d_in[8],
                                                (char*)d_ws, (char*)d_out);
}
```

```cpp
#include <hip/hip_runtime.h>
#include <hip/hip_bf16.h>
#include <stdint.h>

#define LAS __attribute__((address_space(3)))
#define GAS __attribute__((address_space(1)))
#define DI __device__ __forceinline__
typedef __attribute__((ext_vector_type(8))) __bf16 bf16x8;
typedef __attribute__((ext_vector_type(2))) __bf16 bf16x2;
typedef __attribute__((ext_vector_type(4))) short s16x4;
typedef __attribute__((ext_vector_type(2))) float f32x2;
typedef __attribute__((ext_vector_type(4))) float f32x4;
typedef __attribute__((ext_vector_type(16))) float f32x16;
typedef __attribute__((ext_vector_type(2))) unsigned u32x2;
typedef __attribute__((ext_vector_type(4))) unsigned u32x4;
typedef LAS char* lptr;
typedef __amdgpu_buffer_rsrc_t rsrc_t;

constexpr unsigned WS_Q = 0, WS_K = 2u << 20, WS_V = 4u << 20, WS_O = 6u << 20, WS_U = 8u << 20, WS_H = 10u << 20,
                   WS_G = 13u << 20, WS_FL = 14u << 20, WS_END = 15u << 20;
constexpr int FL_H = 0;
constexpr int FL_Q = 64 * 32;
constexpr int FL_U = FL_Q + 192 * 32;
constexpr int FL_O = FL_U + 352 * 32;
constexpr int FL_EPOCH = FL_O + 256 * 32;
constexpr float QSCALE = 0.25f * 1.4426950408889634f;
constexpr int NS = 7, SLOT = 16384, RING = NS * SLOT;
constexpr int L_K = RING, L_V = RING + 16384, L_X = RING + 32768  , L_C = L_X + 12288  , L_P = L_C + 1024  ;
constexpr int LDS_BYTES = 163840;
constexpr unsigned SPIN_CAP = 200000;
#define CODEPF 0
#define PREFETCH 32
#define PFSTEP 1
#define G1ROT 1
#define G2ROT 1
#define KVS 22
#define QDS 10
#define ODS 4
#define QROT 1

#define MFMA32(a, b, c) __builtin_amdgcn_mfma_f32_32x32x16_bf16(a, b, c, 0, 0, 0)
#define MFMA16(a, b, c) __builtin_amdgcn_mfma_f32_16x16x32_bf16(a, b, c, 0, 0, 0)
#define SB() __builtin_amdgcn_sched_barrier(0)
#define LBAR() do { asm volatile("s_waitcnt lgkmcnt(0)\n\ts_barrier" ::: "memory"); SB(); } while (0)
#define CBAR(N) do { asm volatile("s_waitcnt lgkmcnt(" #N ")\n\ts_barrier" ::: "memory"); SB(); } while (0)
#define DBAR() do { asm volatile("s_waitcnt vmcnt(0) lgkmcnt(0)\n\ts_barrier" ::: "memory"); SB(); } while (0)
#define VBAR(N) asm volatile("s_waitcnt vmcnt(" #N ")\n\ts_barrier" ::: "memory")
#define BAR() asm volatile("s_barrier" ::: "memory")

#define STAMP(i) do {} while (0)
#define STAMP_OUT(ws, slot) do {} while (0)

DI int rfl(int x) { return __builtin_amdgcn_readfirstlane(x); }
DI bf16x8 ldsr128(lptr p) { return *(const LAS bf16x8*)p; }
DI s16x4 ldstr(lptr p) { return __builtin_amdgcn_ds_read_tr16_b64_v4i16((LAS s16x4*)p); }
DI bf16x8 cat8(s16x4 a, s16x4 b) { return __builtin_bit_cast(bf16x8, __builtin_shufflevector(a, b, 0, 1, 2, 3, 4, 5, 6, 7)); }
DI unsigned pk2(float a, float b) { f32x2 f = {a, b}; return __builtin_bit_cast(unsigned, __builtin_convertvector(f, bf16x2)); }
DI float bflo(unsigned u) { return __uint_as_float(u << 16); }
DI float bfhi(unsigned u) { return __uint_as_float(u & 0xffff0000u); }
DI int crow(int i, int h) { return (i & 3) + 8 * (i >> 2) + 4 * h; }
DI void st128(rsrc_t r, unsigned off, u32x4 v) { __builtin_amdgcn_raw_buffer_store_b128(v, r, off, 0, 16); }
DI void st64(rsrc_t r, unsigned off, u32x2 v) { __builtin_amdgcn_raw_buffer_store_b64(v, r, off, 0, 16); }
DI void st32(rsrc_t r, unsigned off, unsigned v) { __builtin_amdgcn_raw_buffer_store_b32(v, r, off, 0, 16); }
DI u32x4 ld128(rsrc_t r, unsigned off) { return __builtin_amdgcn_raw_buffer_load_b128(r, off, 0, 16); }
DI unsigned ld32(rsrc_t r, unsigned off) { return __builtin_amdgcn_raw_buffer_load_b32(r, off, 0, 16); }
DI unsigned flag_ld(const char* ws, int idx) { return __hip_atomic_load((const GAS unsigned*)(ws + WS_FL) + idx, __ATOMIC_RELAXED, __HIP_MEMORY_SCOPE_AGENT); }
DI void flag_st(char* ws, int idx, unsigned v) { __hip_atomic_store((GAS unsigned*)(ws + WS_FL) + idx, v, __ATOMIC_RELAXED, __HIP_MEMORY_SCOPE_AGENT); }

#define M0_IN "s_mov_b32 %0, m0\n\ts_mov_b32 m0, %1\n\ts_nop 0\n\t"
#define M0_OUT "s_mov_b32 m0, %0"
DI void dma_2x2(unsigned m0v, const void* b0, unsigned v0, unsigned v1, const void* b1, unsigned v2, unsigned v3) {
  unsigned sv;
  asm volatile(M0_IN "global_load_lds_dwordx4 %4, %2\n\tglobal_load_lds_dwordx4 %5, %2 offset:1024\n\t"
               "global_load_lds_dwordx4 %6, %3 offset:2048\n\tglobal_load_lds_dwordx4 %7, %3 offset:3072\n\t" M0_OUT
               : "=&s"(sv) : "s"(m0v), "s"(b0), "s"(b1), "v"(v0), "v"(v1), "v"(v2), "v"(v3) : "memory");
}
DI void dma_lin4_sc1(unsigned m0v, const void* b, unsigned vl) {
  unsigned sv;
  asm volatile(M0_IN "global_load_lds_dwordx4 %3, %2 sc1\n\tglobal_load_lds_dwordx4 %3, %2 offset:1024 sc1\n\t"
               "global_load_lds_dwordx4 %3, %2 offset:2048 sc1\n\tglobal_load_lds_dwordx4 %3, %2 offset:3072 sc1\n\t" M0_OUT
               : "=&s"(sv) : "s"(m0v), "s"(b), "v"(vl) : "memory");
}
DI void dma_g1(unsigned m0v, const void* wb, unsigned v0, unsigned v1, const void* hb_m2048, unsigned vl) {
  unsigned sv;
  asm volatile(M0_IN "global_load_lds_dwordx4 %4, %2\n\tglobal_load_lds_dwordx4 %5, %2 offset:1024\n\t"
               "global_load_lds_dwordx4 %6, %3 offset:2048 sc1\n\t"
               "s_mov_b32 exec_hi, 0\n\tglobal_load_lds_dwordx4 %6, %3 offset:3072 sc1\n\ts_mov_b32 exec_hi, -1\n\t" M0_OUT
               : "=&s"(sv) : "s"(m0v), "s"(wb), "s"(hb_m2048), "v"(v0), "v"(v1), "v"(vl) : "memory");
}
DI void dma_hh(unsigned m0v, const void* hb_m2048, unsigned vl) {
  unsigned sv;
  asm volatile(M0_IN "global_load_lds_dwordx4 %3, %2 offset:2048 sc1\n\t"
               "s_mov_b32 exec_hi, 0\n\tglobal_load_lds_dwordx4 %3, %2 offset:3072 sc1\n\ts_mov_b32 exec_hi, -1\n\t" M0_OUT
               : "=&s"(sv) : "s"(m0v), "s"(hb_m2048), "v"(vl) : "memory");
}
DI void dma_lin4(unsigned m0v, const void* b, unsigned vl) {
  unsigned sv;
  asm volatile(M0_IN "global_load_lds_dwordx4 %3, %2\n\tglobal_load_lds_dwordx4 %3, %2 offset:1024\n\t"
               "global_load_lds_dwordx4 %3, %2 offset:2048\n\tglobal_load_lds_dwordx4 %3, %2 offset:3072\n\t" M0_OUT
               : "=&s"(sv) : "s"(m0v), "s"(b), "v"(vl) : "memory");
}
DI void dma_touch(unsigned m0v, const void* b, unsigned voff) {
  unsigned sv;
  asm volatile(M0_IN "global_load_lds_dword %3, %2\n\t" M0_OUT : "=&s"(sv) : "s"(m0v), "s"(b), "v"(voff) : "memory");
}
DI void dma_wh(unsigned m0v, const void* wb, unsigned v0, unsigned v1) {
  unsigned sv;
  asm volatile(M0_IN "global_load_lds_dwordx4 %3, %2\n\tglobal_load_lds_dwordx4 %4, %2 offset:1024\n\t" M0_OUT
               : "=&s"(sv) : "s"(m0v), "s"(wb), "v"(v0), "v"(v1) : "memory");
}
DI void dma_fh(unsigned m0v, const void* fb_m2048, unsigned vl) {
  unsigned sv;
  asm volatile(M0_IN "global_load_lds_dwordx4 %3, %2 offset:2048 sc1\n\tglobal_load_lds_dwordx4 %3, %2 offset:3072 sc1\n\t" M0_OUT
               : "=&s"(sv) : "s"(m0v), "s"(fb_m2048), "v"(vl) : "memory");
}

DI void spin1(rsrc_t r, unsigned off, unsigned tok) {
  unsigned n = 0;
#pragma nounroll
  do { const unsigned v = ld32(r, off); if (__all(v == tok)) break; __builtin_amdgcn_s_sleep(4); asm volatile("" ::: "memory"); } while (++n < SPIN_CAP);
  __builtin_amdgcn_fence(__ATOMIC_ACQUIRE, "wavefront");
}
DI void peek_issue(unsigned m0v, const void* ws, unsigned off) {
  unsigned sv;
  asm volatile(M0_IN "global_load_lds_dwordx4 %3, %2 sc1\n\t" M0_OUT : "=&s"(sv) : "s"(m0v), "s"(ws), "v"(off) : "memory");
}
DI void peek_check(lptr p, rsrc_t r, unsigned off, unsigned tok) {
  if (__all(*(const volatile LAS unsigned*)p == tok)) return;
  asm volatile("s_waitcnt vmcnt(0)" ::: "memory");
  if (__all(*(const volatile LAS unsigned*)p == tok)) return;
  spin1(r, off, tok);
}
#define RARE(c) __builtin_expect((c) != 0, 0)

DI float wave_sum(float v) {
#define DPPADD(ctrl, rm) v += __int_as_float(__builtin_amdgcn_update_dpp(0, __float_as_int(v), ctrl, rm, 0xf, false))
  DPPADD(0x111, 0xf); DPPADD(0x112, 0xf); DPPADD(0x114, 0xf); DPPADD(0x118, 0xf); DPPADD(0x142, 0xa); DPPADD(0x143, 0xc);
#undef DPPADD
  return __int_as_float(__builtin_amdgcn_readlane(__float_as_int(v), 63));
}
DI float row16_sum(float v) {
#define DPPX(ctrl) v += __int_as_float(__builtin_amdgcn_update_dpp(0, __float_as_int(v), ctrl, 0xf, 0xf, true))
  DPPX(0xB1); DPPX(0x4E); DPPX(0x141); DPPX(0x140);
#undef DPPX
  return v;
}
DI float gelu_tanh(float u) {
  const float z = u * (0.7978845608028654f + 0.7978845608028654f * 0.044715f * u * u);
  const float e = __builtin_amdgcn_exp2f(z * (-2.0f * 1.4426950408889634f));
  return u * __builtin_amdgcn_rcpf(1.0f + e);
}
constexpr unsigned long long diag_mask(int i) {
  unsigned long long mk = 0;
  for (int l = 0; l < 64; ++l) if ((i & 3) + 8 * (i >> 2) + 4 * (l >> 5) <= (l & 31)) mk |= 1ull << l;
  return mk;
}
DI float max16(const f32x16& s, float m) {
#pragma unroll
  for (int i = 0; i < 16; i += 2) m = __builtin_fmaxf(__builtin_fmaxf(s[i], s[i + 1]), m);
  return m;
}

#define NEXT_SO() so = so == (NS - 1) * SLOT ? 0 : so + SLOT

template <bool ISV, class Hook, class Hook14, class Mid>
DI void qkv_consume(lptr L, int& so, int w, int lane, f32x16 (&acc)[2], Hook hook, Hook14 hook14, Mid mid) {
  const int r = lane & 31, h = lane >> 5, wm = w >> 2, wn = w & 3;
  int xa[4];
#pragma unroll
  for (int s = 0; s < 4; ++s) xa[s] = (wm * 2 + (r >> 4)) * 4096 + (r & 15) * 128 + (((2 * s + h) ^ ((r >> 1) & 7)) << 4);
  const int q = (lane & 15) >> 2, p = lane & 3, blk = (lane >> 4) & 1;
  const int wa = h * 4096 + 2048 + q * 256 + (((wn * 4 + blk * 2 + (p >> 1)) ^ (q << 2)) << 4) + (p & 1) * 8;
  bf16x8 xA[2][4], xB[2][4], wA[4], wB[4];
#define QM(X, W, mi, s) do { if (ISV) acc[mi] = MFMA32(X[mi][s], W[s], acc[mi]); else acc[mi] = MFMA32(W[s], X[mi][s], acc[mi]); SB(); } while (0)
#define QRX(X, mi, s) X[mi][s] = ldsr128(sl + xa[s])
#define QRW(W, mi, e) W[2 * mi + e] = cat8(ldstr(sl + wa + e * 8192), ldstr(sl + wa + e * 8192 + 1024))
#define QSTEP(mi, XN, WN, XP, WP, MM) do { lptr sl = L + so; NEXT_SO(); \
    if (MM) QM(XP, WP, 0, 2 * mi); \
    QRX(XN, mi, 0); QRX(XN, mi, 1); SB(); if (MM) QM(XP, WP, 1, 2 * mi); \
    QRX(XN, mi, 2); QRX(XN, mi, 3); QRW(WN, mi, 0); SB(); if (MM) QM(XP, WP, 0, 2 * mi + 1); \
    QRW(WN, mi, 1); SB(); if (MM) QM(XP, WP, 1, 2 * mi + 1); } while (0)
  LBAR(); QSTEP(0, xA, wA, xB, wB, false); QSTEP(1, xA, wA, xB, wB, false);
#pragma unroll 1
  for (int c = 1; c < 7; c += 2) {
    hook(c);     QSTEP(0, xB, wB, xA, wA, true); QSTEP(1, xB, wB, xA, wA, true);
    hook(c + 1); QSTEP(0, xA, wA, xB, wB, true); QSTEP(1, xA, wA, xB, wB, true);
  }
  hook14(); QSTEP(0, xB, wB, xA, wA, true); QSTEP(1, xB, wB, xA, wA, true);
#pragma unroll
  for (int s = 0; s < 4; ++s) QM(xB, wB, 0, s);
  mid();
#pragma unroll
  for (int s = 0; s < 4; ++s) QM(xB, wB, 1, s);
}

__global__ __launch_bounds__(768, 3) void fused_kernel(const char* __restrict__ x, const char* __restrict__ wqkv, const char* __restrict__ wo,
                                                       const char* __restrict__ w1, const char* __restrict__ b1, const char* __restrict__ w2,
                                                       const char* __restrict__ b2, const char* __restrict__ gw, char* __restrict__ ws,
                                                       char* __restrict__ out) {
  extern __shared__ __attribute__((aligned(16))) char smem[];
  asm volatile("" :: "s"(x), "s"(wqkv), "s"(wo), "s"(w1), "s"(b1), "s"(w2), "s"(b2), "s"(gw), "s"(ws), "s"(out));
  STAMP(0);
  const int b = blockIdx.x, xcd = b & 7, idx = ((b >> 3) + 24) & 31;
  const bool lead = b == 65;
  const int lane = threadIdx.x & 63, w = rfl(threadIdx.x >> 6);
  const unsigned lds0 = (unsigned)(uintptr_t)smem;
  lptr L = (lptr)smem;
  const int tm = (xcd >> 1) * 4 + idx / 6, tn = (xcd & 1) * 6 + idx % 6;
  const int lnb = xcd * 8 + (idx - 24);
  const unsigned x0 = (lane >> 3) * 1024 + (((lane & 7) ^ (lane >> 4)) << 4), x1 = (8 + (lane >> 3)) * 1024 + (((lane & 7) ^ (4 + (lane >> 4))) << 4) - 1024;
  const unsigned q0 = (lane >> 4) * 3072 + (((lane & 15) ^ ((lane >> 4) << 2)) << 4), q1 = q0 + 4 * 3072 - 1024;
  const char* xp = x + (size_t)(tm * 128 + ((w >> 1) & 1) * 64 + (w & 1) * 16) * 1024;
  const char* qp = wqkv + (size_t)((w & 3) * 8) * 3072 + tn * 256 - 2048;
  int kc = QROT ? ((idx / 6 + idx % 6) % 3) * 3 : 0;
  if (w >= 8) {
    if (idx < 24) {
#pragma unroll 1
      for (int f = 0; f < 7; ++f) {
        dma_2x2(lds0 + (w & 3) * 4096 + f * SLOT, xp + kc * 128 + (f & 1) * 32 * 1024, x0, x1, qp + (size_t)(kc * 2 + (f & 1)) * (32 * 3072), q0, q1);
        kc = (kc + (f & 1)) & 7;
      }
    }
  }
  asm volatile("" ::: "memory");
  SB();
  const int gi = 31 - idx;
  const bool hasQ = idx < 24, hasG1 = xcd == 0 || gi < 30;
  const bool lowd = xcd == 0 ? gi < 12 : xcd < 5;
  const int gj = xcd == 0 && !lowd ? gi - 12 : gi;
  const int g1ft = lowd ? (xcd == 0 ? 20 : (xcd - 1) * 5) + gj / 6 : (xcd == 0 ? 18 : (xcd - 5) * 6) + gj / 5;
  const int g1dt = lowd ? gj % 6 : 6 + gj % 5;
  const int head = xcd * 4 + (idx & 3), qb = idx >> 2;
  const int st = (xcd >> 1) * 8 + (idx >> 2), dt = (xcd & 1) * 4 + (idx & 3);
  unsigned fq_off;
  { const int l = lane < 12 ? lane : 3, wh = l >> 2; int tmi = l & 3; if (wh == 0 || qb == 0) tmi |= 2;
    fq_off = WS_FL + (FL_Q + ((wh * 4 + (head >> 3)) * 16 + 2 * qb - 2 + tmi) * 32) * 4; }
  const unsigned fu_off = WS_FL + (FL_U + (((lane & 32) ? (4 * dt + 3) / 3 : (4 * dt) / 3) * 32 + min(lane & 31, 21)) * 32) * 4;
  const unsigned fo_off = WS_FL + (FL_O + ((st >> 2) * 32 + (lane & 31)) * 32) * 4;
  const unsigned fh_off = WS_FL + FL_H * 4 + lane * 128;
  const rsrc_t rws = __builtin_amdgcn_make_buffer_rsrc(ws, 0, WS_END, 0x00020000);
  volatile LAS unsigned* relay = (volatile LAS unsigned*)(L + L_C + 64);
  unsigned eoff = WS_FL + FL_EPOCH * 4;
  asm volatile("" : "+v"(eoff));
  const unsigned epoch = ld32(rws, eoff);
#define TOKEN_OF(e) ((e) * 1664525u + 1013904223u)

  if (w >= 8) {
    const int j = w - 8;
#define token TOKEN_OF(epoch)
    const unsigned m0b = lds0 + j * 4096;
    const unsigned vl = lane * 16;
    unsigned so = 0;
    const char* hp = ws + WS_H + (size_t)g1dt * 64 * 3072 + j * 1536 - 2048;
    const char* w1p = w1 + (size_t)(j * 16) * 2752 + g1ft * 128;
    const unsigned u0 = (lane >> 3) * 2752 + ((((lane & 7) ^ (((lane >> 4) & 3) << 1)) & (g1ft == 21 ? 3 : 7)) << 4), u1 = u0 + 8 * 2752 - 1024;
    int cw = G1ROT ? (lowd ? g1dt * 6 : (g1dt - 6) * 6 + 3) : 0, ch = cw;
    auto fill_g1 = [&]() { dma_g1(m0b + so, w1p + (size_t)cw * (64 * 2752), u0, u1, hp + cw * 6144, vl); cw = (cw + 1) & 31; NEXT_SO(); };
    auto fill_g1w = [&]() { dma_wh(m0b + so, w1p + (size_t)cw * (64 * 2752), u0, u1); cw = (cw + 1) & 31; NEXT_SO(); };
    auto fill_g1h3 = [&](unsigned so0) {
      const unsigned keep = so; so = so0;
#pragma unroll 1
      for (int i = 0; i < 3; ++i) { dma_hh(m0b + so, hp + ch * 6144, vl); ch = (ch + 1) & 31; NEXT_SO(); }
      so = keep;
    };
    const unsigned swz = ((lane & 7) ^ (((lane >> 4) & 1) << 2)) << 4;
    unsigned vw0 = (lane >> 3) * 4096 + swz, vw1 = vw0 + 8 * 4096 - 1024;
    const char* w2p = w2 + (size_t)(j * 16) * 4096 + st * 128;
    const char* up = ws + WS_U + (size_t)dt * 86 * 2048 + j * 2048 - 2048;
    const size_t w2half = (size_t)(j < 2 ? 21 * 64 : 20 * 64) * 4096;
    const int uhalf = j < 2 ? 21 * 8192 : 20 * 8192;
    auto fill_kv = [&]() {
      int t0 = qb * 8 - 8 + 4 * j; t0 = t0 < 0 ? t0 + 8 : t0;
      dma_lin4_sc1(lds0 + L_K + j * 4096, ws + WS_K + (size_t)(head * 64 + t0) * 1024, vl);
      dma_lin4_sc1(lds0 + L_V + j * 4096, ws + WS_V + (size_t)(head * 64 + t0) * 1024, vl);
    };
    int nwh = 0;
    const int r2 = G2ROT ? (((dt & 3) * 2 + (dt >> 2)) * 21) >> 3 : 0;
    int c2 = r2;
    auto fill_wh = [&]() {
      dma_wh(m0b + so, nwh == 0 ? w2p + w2half : w2p + (size_t)c2 * (64 * 4096), vw0, vw1);
      if (nwh) c2 = c2 == 20 ? 0 : c2 + 1;
      ++nwh; NEXT_SO();
    };
    unsigned so_g2 = 0;
    auto warm_g2 = [&]() {
      if (PREFETCH & 48) {
        const int t = idx * 4 + j;
        if ((PREFETCH & 16) && t < 120) dma_touch(lds0 + L_P + 1024, w2 + (size_t)(384 + t * 8) * 4096 + (xcd >> 1) * 1024, (lane >> 3) * 4096 + (lane & 7) * 128);
        if (j == 1) dma_touch(lds0 + L_P + 1024, wo + (size_t)(idx * 16) * 1024 + (xcd & 1) * 512, (lane >> 2) * 1024 + (lane & 3) * 128);
      }
    };

    if (hasQ) {
      auto fill_a = [&]() { dma_2x2(m0b + so, xp + kc * 128, x0, x1, qp + (size_t)kc * 64 * 3072, q0, q1); NEXT_SO(); };
      auto fill_b = [&]() { dma_2x2(m0b + so, xp + kc * 128 + 32 * 1024, x0, x1, qp + (size_t)kc * 64 * 3072 + 32 * 3072, q0, q1); kc = (kc + 1) & 7; NEXT_SO(); };
      VBAR(20);
#pragma unroll 1
      for (int c = 1; c < 5; ++c) { VBAR(12); fill_b(); fill_a(); }
      VBAR(12); fill_b();
      if (hasG1) {
        const unsigned so0 = so;
        fill_g1w();
        VBAR(10); fill_g1w(); fill_g1w();
        VBAR(6); fill_g1h3(so0); fill_g1(); fill_g1();
      } else {
        VBAR(8); VBAR(0);
      }
    } else {
      BAR();
      VBAR(0);
#pragma unroll 1
      for (int s = 0; s < 3; ++s) fill_g1w();
      if (j == 0) spin1(rws, fh_off, token);
      else if (PREFETCH & 2) {
        const int pw = lnb * 3 + (j - 1);
#pragma unroll 1
        for (int i = pw; i < 752; i += 192) dma_touch(lds0 + L_P + 1024, i < 688 ? w2 + (size_t)i * 8192 : wo + (size_t)(i - 688) * 8192, lane * 128);
      } else if (PREFETCH & 4) {
        const int pw = lnb * 3 + (j - 1);
#pragma unroll 1
        for (int i = pw; i < 688; i += 192) dma_touch(lds0 + L_P + 1024, w1 + (size_t)i * 8192, lane * 128);
      }
      BAR();
      fill_g1h3(0); fill_g1(); fill_g1();
    }
    STAMP(1);
    if (hasG1) {
      VBAR(12); fill_g1(); VBAR(14); fill_g1();
#pragma unroll 1
      for (int s = 2; s <= KVS; ++s) { VBAR(16); fill_g1(); }
      fill_kv();
#pragma unroll 1
      for (int s = KVS + 1; s < 27; ++s) { VBAR(16); fill_g1(); }
      so_g2 = so;
      VBAR(16); fill_wh(); VBAR(14); fill_wh(); VBAR(12); fill_wh(); VBAR(10); fill_wh(); VBAR(8); fill_wh();
      BAR(); fill_wh(); fill_wh();
      warm_g2();
      BAR();
      BAR();
    } else {
      BAR();
      so_g2 = so;
#pragma unroll 1
      for (int i = 0; i < 7; ++i) fill_wh();
      warm_g2();
      if (j == 0) spin1(rws, fq_off, token);
      BAR();
      fill_kv();
      VBAR(0);
    }
    STAMP(2);
    {
      if (j == 0) { spin1(rws, fu_off, token); *relay = 1u; }
      else { for (unsigned spins = 0; spins < 64 * SPIN_CAP && *relay == 0u; ++spins) __builtin_amdgcn_s_sleep(1); }
      STAMP(3);
      so = so_g2;
      dma_fh(m0b + so, up + uhalf, vl); NEXT_SO();
#pragma unroll 1
      for (int i = 0, c = r2; i < 6; ++i) { dma_fh(m0b + so, up + c * 8192, vl); c = c == 20 ? 0 : c + 1; NEXT_SO(); }
    }
    unsigned wsh = 18;
    int ci = 7;
    auto fill_g2 = [&]() {
      if (ci == 22) {
        w2p = wo + (size_t)(j * 16) * 1024 + dt * 128; up = ws + WS_O + (size_t)st * 32 * 2048 + j * 2048 - 2048; wsh = 16; c2 = 0;
        vw0 = (lane >> 3) * 1024 + swz; vw1 = vw0 + 8 * 1024 - 1024;
      }
      dma_wh(m0b + so, w2p + ((size_t)c2 << wsh), vw0, vw1); dma_fh(m0b + so, up + c2 * 8192, vl);
      c2 = c2 == 20 ? 0 : c2 + 1; NEXT_SO(); ++ci;
    };
    VBAR(12); VBAR(10);
    VBAR(8); fill_g2(); VBAR(10); fill_g2(); VBAR(12); fill_g2(); VBAR(14); fill_g2();
#pragma unroll 1
    for (int s = 6; s < 25; ++s) { VBAR(16); fill_g2(); }
    const unsigned fa_off = WS_FL + FL_O * 4 + (j * 64 + lane) * 128;
    if (lead) peek_issue(lds0 + L_X + j * 1024, ws, fa_off);
    VBAR(16); VBAR(12); VBAR(8); VBAR(4); VBAR(0);
    if (lead) peek_check(L + L_X + j * 1024 + lane * 16, rws, fa_off, token);
    BAR();
    if (lead && j == 0 && lane == 0) st32(rws, WS_FL + FL_EPOCH * 4, token);
    BAR();
    STAMP(4);
    if (j == 0) STAMP_OUT(ws, 1);
    return;
  }

#undef token
  unsigned token;
  u32x4 xv[4], gv[4];
  if (!hasQ) {
#pragma unroll
    for (int i = 0; i < 4; ++i) xv[i] = *(const u32x4*)(x + (size_t)(lnb * 32 + w * 4 + (lane >> 4)) * 1024 + (lane & 15) * 16 + i * 256);
#pragma unroll
    for (int i = 0; i < 4; ++i) gv[i] = *(const u32x4*)(gw + (lane & 15) * 16 + i * 256);
  }
  asm volatile("" ::: "memory");
  SB();
  const int r = lane & 31, h = lane >> 5;
  const int g4 = lane >> 4, q4 = (lane & 15) >> 2, p4 = lane & 3, blk = (lane >> 4) & 1;
  int so = 0;
  if (threadIdx.x == 4) *relay = 0u;
  if (threadIdx.x < 4) {
    const unsigned cv = (threadIdx.x & 1) ? 0u : 0x3f803f80u;
    *(LAS u32x4*)(L + L_C + (threadIdx.x >> 1) * 512 + (threadIdx.x & 1) * 16) = u32x4{cv, cv, cv, cv};
  }
  lptr pk = L + L_P + lane * 16;

  const int kh = w >> 2, fb = w & 3;
  int ha[3];
#pragma unroll
  for (int db = 0; db < 3; ++db) { const int o = (kh * 3 + db) * 1024 + lane * 16, rg = o / 1536; ha[db] = rg * 4096 + 2048 + (o - rg * 1536); }
  const int wa = kh * 8192 + (4 * g4 + q4) * 128 + ((fb ^ ((2 * g4 + (q4 >> 1)) & 3)) << 5) + p4 * 8;
  f32x4 acc[3] = {};
  bf16x8 wA = {}, hA[3] = {}, wB = {}, hB[3] = {};
#define G1RD(W, H) do { lptr sl = L + so; NEXT_SO(); W = cat8(ldstr(sl + wa), ldstr(sl + wa + 4096)); \
  _Pragma("unroll") for (int db = 0; db < 3; ++db) H[db] = ldsr128(sl + ha[db]); SB(); } while (0)
#define G1MM(W, H) do { _Pragma("unroll") for (int db = 0; db < 3; ++db) acc[db] = MFMA16(W, H[db], acc[db]); SB(); } while (0)

  token = TOKEN_OF(epoch);
  asm volatile("" : "+v"(token), "+v"(gv[3]));
  if (hasQ) {
    if (w == 7) *(LAS unsigned*)pk = ~token;
    STAMP(1);
    f32x16 qacc[2] = {};
    const bool poller = w == 7 && hasG1;
    const int pi = (xcd * 24 + idx) * 4 + w;
    const unsigned post = (poller ? (1u << 4) | (1u << 5) | (1u << 6) : 0u) | (((PREFETCH & 1) && w < 4 && pi < 688) ? 1u << PFSTEP : 0u);
    if (post & 1) dma_touch(lds0 + L_P + 1024, w1 + (size_t)pi * 8192, lane * 128);
    auto hook = [&](int s) {
      LBAR();
      if (RARE((post >> s) & 1)) { if (w == 7) peek_issue(lds0 + L_P, ws, fh_off); else dma_touch(lds0 + L_P + 1024, w1 + (size_t)pi * 8192, lane * 128); }
      SB();
    };
    auto hook14 = [&]() { if (poller) peek_check(pk, rws, fh_off, token); LBAR(); };
    auto mid = [&]() { if (hasG1) { LBAR(); G1RD(wA, hA); } };
    if (tn >= 8) qkv_consume<true>(L, so, w, lane, qacc, hook, hook14, mid);
    else qkv_consume<false>(L, so, w, lane, qacc, hook, hook14, mid);
    STAMP(2);
    const int wm = w >> 2, wn = w & 3;
    const int hd0 = (tn & 3) * 8 + wn * 2;
    const float sc = tn < 4 ? QSCALE : 1.0f;
    const unsigned dst = tn >= 8 ? WS_V + (((hd0 + (r >> 4)) * 64 + tm * 4 + wm * 2) * 64 + h * 16 + (r & 15)) * 16
                                 : (tn < 4 ? WS_Q : WS_K) + ((hd0 * 64 + tm * 4 + wm * 2) * 64 + lane) * 16;
    const unsigned dstep = tn >= 8 ? 512 : 65536;
#pragma unroll
    for (int mi = 0; mi < 2; ++mi) {
#pragma unroll
      for (int s = 0; s < 2; ++s) {
        u32x4 v;
#pragma unroll
        for (int jj = 0; jj < 4; ++jj) v[jj] = pk2(qacc[mi][8 * s + 2 * jj] * sc, qacc[mi][8 * s + 2 * jj + 1] * sc);
        st128(rws, dst + mi * 1024 + s * dstep, v);
      }
      if (mi == 0 && hasG1) { SB(); LBAR(); G1RD(wB, hB); G1MM(wA, hA); }
    }
  } else {
    constexpr int RS = 1056;
    f32x2 v[16], s2 = {0.f, 0.f}, g2 = {0.f, 0.f};
#pragma unroll
    for (int i = 0; i < 16; ++i) {
      v[i] = f32x2{bflo(xv[i >> 2][i & 3]), bfhi(xv[i >> 2][i & 3])};
      s2 += v[i];
      g2 += v[i] * f32x2{bflo(gv[i >> 2][i & 3]), bfhi(gv[i >> 2][i & 3])};
    }
    const float mean = row16_sum(s2[0] + s2[1]) * (1.0f / 512), gs = row16_sum(g2[0] + g2[1]);
    f32x2 q2 = {0.f, 0.f};
#pragma unroll
    for (int i = 0; i < 16; ++i) { v[i] -= f32x2{mean, mean}; q2 += v[i] * v[i]; }
    const float rs = __builtin_amdgcn_rsqf(row16_sum(q2[0] + q2[1]) * (1.0f / 512) + 1e-5f);
#pragma unroll
    for (int i = 0; i < 4; ++i) {
      u32x4 ov;
#pragma unroll
      for (int jj = 0; jj < 4; ++jj) { const f32x2 t = v[4 * i + jj] * f32x2{rs, rs}; ov[jj] = pk2(t[0], t[1]); }
      *(LAS u32x4*)(L + (w * 4 + g4) * RS + (lane & 15) * 16 + i * 256) = ov;
    }
    if ((lane & 15) == 0) st32(rws, WS_G + (lnb * 32 + w * 4 + g4) * 4, __float_as_uint(1.0f / (1.0f + __expf(-gs))));
    STAMP(1);
    LBAR();
#pragma unroll
    for (int i = 0; i < 4; ++i) {
      const int D = w * 4 + i, d48 = D / 3;
      lptr a = L + (4 * g4 + q4) * RS + D * 32 + p4 * 8;
      const bf16x8 f = cat8(ldstr(a), ldstr(a + 16 * RS));
      st128(rws, WS_H + ((d48 * 64 + lnb) * 3 + (D - d48 * 3)) * 1024 + lane * 16, __builtin_bit_cast(u32x4, f));
      if (i == 3 && w == 7) st128(rws, WS_H + ((10 * 64 + lnb) * 3 + 2) * 1024 + lane * 16, __builtin_bit_cast(u32x4, f));
    }
    DBAR();
    if (threadIdx.x == 0) flag_st(ws, FL_H + lnb * 32, token);
    STAMP(2);
    LBAR();
    LBAR(); G1RD(wA, hA);
    LBAR(); G1RD(wB, hB); G1MM(wA, hA);
  }

  bf16x8 qf;
  if (hasG1) {
    const unsigned pre = (1u << QDS) | (w == 7 ? 1u << KVS : 0u);
    const unsigned post = (w == 0 && hasQ ? 1u << QDS : 0u) | (w == 7 ? (1u << QDS) | (1u << (KVS - 6)) | (1u << (KVS - 4)) | (1u << (KVS - 2)) : 0u);
    auto hook = [&](int s) {
      if (RARE((pre >> s) & 1)) { if (s == QDS) asm volatile("s_waitcnt vmcnt(0)" ::: "memory"); else peek_check(pk, rws, fq_off, token); }
      CBAR(5);
      if (RARE((post >> s) & 1)) {
        if (s == QDS) { if (w == 7) *(LAS unsigned*)pk = ~token; else if (lane == 0) flag_st(ws, FL_Q + (tn * 16 + tm) * 32, token); }
        else peek_issue(lds0 + L_P, ws, fq_off);
      }
      SB();
    };
#pragma unroll 1
    for (int s = 2; s < KVS + 2; s += 2) {
      hook(s); G1RD(wA, hA); G1MM(wB, hB);
      CBAR(5); G1RD(wB, hB); G1MM(wA, hA);
    }
    STAMP(10);
    qf = __builtin_bit_cast(bf16x8, ld128(rws, WS_Q + (head * 64 + qb * 8 + w) * 1024 + lane * 16));
    const int fblk = g1ft * 4 + fb;
    u32x2 bb = *(const u32x2*)(b1 + min(fblk * 16 + 4 * g4, 1372) * 2);
    unsigned ua[2];
#pragma unroll
    for (int e = 0; e < 2; ++e) {
      const int D = g1dt * 3 + (kh ? 2 : e);
      ua[e] = WS_U + ((((D >> 2) * 86 + fblk) * 2 + ((D >> 1) & 1)) * 64 + (g4 >> 1) * 32 + (D & 1) * 16 + (lane & 15)) * 16 + (g4 & 1) * 8;
      if (fblk >= 86 || D >= 32) ua[e] = WS_END;
    }
    lptr xw = L + L_X + (fb * 3 * 64 + lane) * 16;
#pragma unroll 1
    for (int s = KVS + 2; s < 32; s += 2) {
      CBAR(5); G1RD(wA, hA); G1MM(wB, hB);
      CBAR(5); G1RD(wB, hB); G1MM(wA, hA);
    }
    asm volatile("" : "+v"(qf), "+v"(bb), "+v"(ua[0]), "+v"(ua[1]));
    const f32x4 bv = {bflo(bb[0]), bfhi(bb[0]), bflo(bb[1]), bfhi(bb[1])};
    LBAR();
    G1MM(wB, hB);
    STAMP(3);
    if (kh == 1) { *(LAS f32x4*)(xw) = acc[0]; *(LAS f32x4*)(xw + 1024) = acc[1]; }
    else *(LAS f32x4*)(xw + 2048) = acc[2];
    LBAR();
    auto fin = [&](const f32x4& a, int off, unsigned addr) {
      const f32x4 t = a + *(const LAS f32x4*)(xw + off) + bv;
      st64(rws, addr, u32x2{pk2(gelu_tanh(t[0]), gelu_tanh(t[1])), pk2(gelu_tanh(t[2]), gelu_tanh(t[3]))});
    };
    if (kh == 1) fin(acc[2], 2048, ua[0]);
    else { fin(acc[0], 0, ua[0]); fin(acc[1], 1024, ua[1]); }
  } else {
    DBAR();
    if (threadIdx.x == 0) flag_st(ws, FL_Q + (tn * 16 + tm) * 32, token);
    LBAR();
    qf = __builtin_bit_cast(bf16x8, ld128(rws, WS_Q + (head * 64 + qb * 8 + w) * 1024 + lane * 16));
    asm volatile("" : "+v"(qf));
    LBAR();
    STAMP(3);
  }

  {
    lptr kp = L + L_K + w * 1024 + lane * 16;
    const int i_lo = qb > 0 ? 1 : 8 - w;
    const f32x16 zero = {};
    bf16x8 kf[9];
#pragma unroll
    for (int i = 0; i < 9; ++i) kf[i] = ldsr128(kp + i * 1024);
    f32x16 sb, sa, sc;
    float mx[4];
#define TMAX(S, i) (((i) >= i_lo) ? max16(S, -INFINITY) : -INFINITY)
    sa = MFMA32(kf[8], qf, zero); sc = MFMA32(kf[0], qf, zero);
    f32x16 s1 = MFMA32(kf[1], qf, zero), s2 = MFMA32(kf[2], qf, zero); SB();
#pragma unroll
    for (int i = 0; i < 16; ++i) sb[i] = __builtin_amdgcn_inverse_ballot_w64(diag_mask(i)) ? sa[i] : (qb > 0 ? sc[i] : -INFINITY);
    SB();
    sa = MFMA32(kf[3], qf, zero); sc = MFMA32(kf[4], qf, zero); SB();
    mx[0] = max16(sb, -INFINITY); mx[1] = TMAX(s1, 1); mx[2] = TMAX(s2, 2); SB();
    s1 = MFMA32(kf[5], qf, zero); s2 = MFMA32(kf[6], qf, zero); SB();
    mx[3] = TMAX(sa, 3); mx[0] = __builtin_fmaxf(mx[0], TMAX(sc, 4)); SB();
    sa = MFMA32(kf[7], qf, zero); SB();
    mx[1] = __builtin_fmaxf(mx[1], TMAX(s1, 5)); mx[2] = __builtin_fmaxf(mx[2], TMAX(s2, 6)); mx[3] = __builtin_fmaxf(mx[3], TMAX(sa, 7));
    float m = __builtin_fmaxf(__builtin_fmaxf(mx[0], mx[1]), __builtin_fmaxf(mx[2], mx[3]));
    {
      auto rr = __builtin_amdgcn_permlane32_swap(__float_as_uint(m), __float_as_uint(m), false, false);
      m = __builtin_fmaxf(__uint_as_float(rr[0]), __uint_as_float(rr[1]));
    }
    STAMP(11);
    if (hasG1) {
      DBAR();
      if (threadIdx.x == 0) flag_st(ws, FL_U + (g1dt * 32 + g1ft) * 32, token);
    }
    STAMP(4);
    const int vstr = r < 16 ? 1024 : 0;
    lptr vp0 = L + (r < 16 ? L_V + w * 1024 + h * 256 + r * 16 : (L_C + ((r == 16 || r == 20) ? 0 : 16)));
    f32x16 cin;
#pragma unroll
    for (int i = 0; i < 16; ++i) cin[i] = -m;
    f32x16 s = MFMA32(ldsr128(kp + i_lo * 1024), qf, cin);
    f32x16 o = {};
    {
      u32x4 pd[2], pf[2];
#pragma unroll
      for (int i = 0; i < 16; i += 2) {
        const unsigned pp = pk2(__builtin_amdgcn_exp2f(sb[i] - m), __builtin_amdgcn_exp2f(sb[i + 1] - m));
        const int dlt = r - crow(i, h);
        const unsigned mk = dlt >= 1 ? 0xffffffffu : (dlt == 0 ? 0x0000ffffu : 0u);
        pd[i >> 3][(i >> 1) & 3] = pp & mk;
        pf[i >> 3][(i >> 1) & 3] = pp & ~mk;
      }
      lptr vp = vp0 + 8 * vstr;
      o = MFMA32(ldsr128(vp), __builtin_bit_cast(bf16x8, pd[0]), o);
      o = MFMA32(ldsr128(vp + 512), __builtin_bit_cast(bf16x8, pd[1]), o);
      o = MFMA32(ldsr128(vp0), __builtin_bit_cast(bf16x8, pf[0]), o);
      o = MFMA32(ldsr128(vp0 + 512), __builtin_bit_cast(bf16x8, pf[1]), o);
    }
#pragma unroll 1
    for (int i = i_lo; i < 8; ++i) {
      lptr vp = vp0 + i * vstr;
      const bf16x8 kn = ldsr128(kp + (i + 1) * 1024), v0 = ldsr128(vp), v1 = ldsr128(vp + 512);
      u32x4 p0, p1;
#pragma unroll
      for (int e = 0; e < 8; e += 2) p0[e >> 1] = pk2(__builtin_amdgcn_exp2f(s[e]), __builtin_amdgcn_exp2f(s[e + 1]));
      o = MFMA32(v0, __builtin_bit_cast(bf16x8, p0), o);
#pragma unroll
      for (int e = 8; e < 16; e += 2) p1[(e >> 1) & 3] = pk2(__builtin_amdgcn_exp2f(s[e]), __builtin_amdgcn_exp2f(s[e + 1]));
      s = MFMA32(kn, qf, cin);
      o = MFMA32(v1, __builtin_bit_cast(bf16x8, p1), o);
    }
    const float il = __builtin_amdgcn_rcpf(o[8]);
    u32x4 ov;
#pragma unroll
    for (int jj = 0; jj < 4; ++jj) ov[jj] = pk2(o[2 * jj] * il, o[2 * jj + 1] * il);
    const int B = qb * 8 + w;
    st128(rws, WS_O + (((B >> 1) * 32 + head) * 2 + (B & 1)) * 1024 + lane * 16, ov);
  }
  STAMP(5);

  {
    const int mt = (w >> 1) & 1, nt = w & 1;
    const int tok = st * 64 + nt * 32 + r;
    const int ta2 = kh * 8192 + (8 * h + q4) * 128 + (((nt * 4 + blk * 2 + (p4 >> 1)) ^ ((q4 >> 1) << 2)) << 4) + (p4 & 1) * 8;
    const int tao = kh * 8192 + (4 * h + q4) * 128 + (((mt * 4 + blk * 2 + (p4 >> 1)) ^ ((q4 >> 1) << 2)) << 4) + (p4 & 1) * 8;
    const int fa2 = kh * 8192 + 2048 + mt * 1024 + lane * 16;
    const int fao = kh * 8192 + 2048 + nt * 1024 + lane * 16;
    f32x16 ay = {}, aa = {};
    bf16x8 pa0, pb0, pa1, pb1, qa0, qb0, qa1, qb1;
    float g = 0.f; unsigned b2v = 0;
#define RD2(A0, B0, A1, B1) do { lptr sl = L + so; NEXT_SO(); B0 = cat8(ldstr(sl + ta2), ldstr(sl + ta2 + 512)); A0 = ldsr128(sl + fa2); \
    B1 = cat8(ldstr(sl + ta2 + 4096), ldstr(sl + ta2 + 4096 + 512)); A1 = ldsr128(sl + fa2 + 4096); SB(); } while (0)
#define RDO(A0, B0, A1, B1) do { lptr sl = L + so; NEXT_SO(); A0 = cat8(ldstr(sl + tao), ldstr(sl + tao + 1024)); B0 = ldsr128(sl + fao); \
    A1 = cat8(ldstr(sl + tao + 4096), ldstr(sl + tao + 4096 + 1024)); B1 = ldsr128(sl + fao + 4096); SB(); } while (0)
#define MM2(ACC, A0, B0, A1, B1) do { ACC = MFMA32(A0, B0, ACC); ACC = MFMA32(A1, B1, ACC); SB(); } while (0)
    const unsigned pre = (1u << ODS) | (w == 7 ? 1u << 16 : 0u);
    const unsigned post = (w == 0 ? 1u << ODS : 0u) | (w == 7 ? (1u << ODS) | (1u << 10) | (1u << 12) | (1u << 14) : 0u);
    auto hook = [&](int s) {
      if (RARE((pre >> s) & 1)) { if (s == ODS) asm volatile("s_waitcnt vmcnt(0)" ::: "memory"); else peek_check(pk, rws, fo_off, token); }
      CBAR(6);
      if (RARE((post >> s) & 1)) {
        if (s == ODS) { if (w == 7) *(LAS unsigned*)pk = ~token; else if (lane == 0) flag_st(ws, FL_O + (qb * 32 + head) * 32, token); }
        else peek_issue(lds0 + L_P, ws, fo_off);
      }
      SB();
    };
    LBAR(); STAMP(6);
    RD2(pa0, pb0, pa1, pb1);
    if (kh) { pa0 = bf16x8{}; pa1 = bf16x8{}; pb0 = bf16x8{}; pb1 = bf16x8{}; }
#pragma unroll 1
    for (int s = 2; s < 22; s += 2) {
      CBAR(6); RD2(qa0, qb0, qa1, qb1); MM2(ay, pa0, pb0, pa1, pb1);
      hook(s); RD2(pa0, pb0, pa1, pb1); MM2(ay, qa0, qb0, qa1, qb1);
    }
    g = __uint_as_float(ld32(rws, WS_G + tok * 4));
    b2v = ((const unsigned short*)b2)[tok];
    CBAR(6); RD2(qa0, qb0, qa1, qb1); MM2(ay, pa0, pb0, pa1, pb1);
    STAMP(7);
    CBAR(6); RDO(pa0, pb0, pa1, pb1); MM2(ay, qa0, qb0, qa1, qb1);
#pragma unroll 1
    for (int s = 23; s < 29; s += 2) {
      CBAR(6); RDO(qa0, qb0, qa1, qb1); MM2(aa, pa0, pb0, pa1, pb1);
      CBAR(6); RDO(pa0, pb0, pa1, pb1); MM2(aa, qa0, qb0, qa1, qb1);
    }
    CBAR(6); RDO(qa0, qb0, qa1, qb1); MM2(aa, pa0, pb0, pa1, pb1);
    MM2(aa, qa0, qb0, qa1, qb1);
    STAMP(8);
    const float og = 1.0f - g;
    float v[16];
#pragma unroll
    for (int i = 0; i < 16; ++i) v[i] = ay[i] * og + aa[i] * g;
    LBAR();
    {
      lptr ex = L + w * 2048 + lane * 16;
      f32x4 s0, s1;
#pragma unroll
      for (int jj = 0; jj < 4; ++jj) { s0[jj] = kh ? v[jj] : v[8 + jj]; s1[jj] = kh ? v[4 + jj] : v[12 + jj]; }
      *(LAS f32x4*)(ex) = s0; *(LAS f32x4*)(ex + 1024) = s1;
    }
    LBAR();
    {
      lptr ex = L + (w ^ 4) * 2048 + lane * 16;
      const f32x4 r0 = *(const LAS f32x4*)(ex), r1 = *(const LAS f32x4*)(ex + 1024);
      const float bo = bflo(b2v) * og;
      float f[8];
#pragma unroll
      for (int jj = 0; jj < 4; ++jj) { f[jj] = (kh ? v[8 + jj] : v[jj]) + r0[jj] + bo; f[4 + jj] = (kh ? v[12 + jj] : v[4 + jj]) + r1[jj] + bo; }
      const unsigned a0 = pk2(f[0], f[1]), a1 = pk2(f[2], f[3]), c0 = pk2(f[4], f[5]), c1 = pk2(f[6], f[7]);
      auto x0 = __builtin_amdgcn_permlane32_swap(a0, c0, false, false);
      auto x1 = __builtin_amdgcn_permlane32_swap(a1, c1, false, false);
      const u32x4 ov = {x0[0], x1[0], x0[1], x1[1]};
      __builtin_amdgcn_raw_buffer_store_b128(ov, __builtin_amdgcn_make_buffer_rsrc(out, 0, 2048 * 1024, 0x00020000), tok * 1024 + (dt * 64 + mt * 32 + kh * 16 + h * 8) * 2, 0, 2);
    }
  }
  STAMP(9);
  if (w == 0) STAMP_OUT(ws, 0);
}

extern "C" void kernel_launch(void* const* d_in, const int* in_sizes, int n_in, void* d_out, int out_size, void* d_ws, size_t ws_size,
                              hipStream_t stream) {
  (void)hipFuncSetAttribute((const void*)fused_kernel, hipFuncAttributeMaxDynamicSharedMemorySize, LDS_BYTES);
  fused_kernel<<<256, 768, LDS_BYTES, stream>>>((const char*)d_in[0], (const char*)d_in[1], (const char*)d_in[2], (const char*)d_in[4],
                                                (const char*)d_in[5], (const char*)d_in[6], (const char*)d_in[7], (const char*)d_in[8],
                                                (char*)d_ws, (char*)d_out);
}
```

```cpp
#include <hip/hip_runtime.h>
#include <hip/hip_bf16.h>
#include <stdint.h>

#define LAS __attribute__((address_space(3)))
#define GAS __attribute__((address_space(1)))
#define DI __device__ __forceinline__
typedef __attribute__((ext_vector_type(8))) __bf16 bf16x8;
typedef __attribute__((ext_vector_type(2))) __bf16 bf16x2;
typedef __attribute__((ext_vector_type(4))) short s16x4;
typedef __attribute__((ext_vector_type(2))) float f32x2;
typedef __attribute__((ext_vector_type(4))) float f32x4;
typedef __attribute__((ext_vector_type(16))) float f32x16;
typedef __attribute__((ext_vector_type(2))) unsigned u32x2;
typedef __attribute__((ext_vector_type(4))) unsigned u32x4;
typedef LAS char* lptr;
typedef __amdgpu_buffer_rsrc_t rsrc_t;

constexpr unsigned WS_Q = 0, WS_K = 2u << 20, WS_V = 4u << 20, WS_O = 6u << 20, WS_U = 8u << 20, WS_H = 10u << 20,
                   WS_G = 13u << 20, WS_FL = 14u << 20, WS_END = 15u << 20;
constexpr int FL_H = 0;
constexpr int FL_Q = 64 * 32;
constexpr int FL_U = FL_Q + 192 * 32;
constexpr int FL_O = FL_U + 352 * 32;
constexpr int FL_EPOCH = FL_O + 256 * 32;
constexpr float QSCALE = 0.25f * 1.4426950408889634f;
constexpr int NS = 7, SLOT = 16384, RING = NS * SLOT;
constexpr int L_K = RING, L_V = RING + 16384, L_X = RING + 32768  , L_C = L_X + 12288  , L_P = L_C + 1024  ;
constexpr int LDS_BYTES = 163840;
constexpr unsigned SPIN_CAP = 200000;
#define CODEPF 0
#define PREFETCH 32
#define PFSTEP 1
#define G1ROT 1
#define G2ROT 1
#define KVS 22
#define QDS 10
#define ODS 4
#define OTOUCH 1
#define QROT 1

#define MFMA32(a, b, c) __builtin_amdgcn_mfma_f32_32x32x16_bf16(a, b, c, 0, 0, 0)
#define MFMA16(a, b, c) __builtin_amdgcn_mfma_f32_16x16x32_bf16(a, b, c, 0, 0, 0)
#define SB() __builtin_amdgcn_sched_barrier(0)
#define LBAR() do { asm volatile("s_waitcnt lgkmcnt(0)\n\ts_barrier" ::: "memory"); SB(); } while (0)
#define CBAR(N) do { asm volatile("s_waitcnt lgkmcnt(" #N ")\n\ts_barrier" ::: "memory"); SB(); } while (0)
#define DBAR() do { asm volatile("s_waitcnt vmcnt(0) lgkmcnt(0)\n\ts_barrier" ::: "memory"); SB(); } while (0)
#define VBAR(N) asm volatile("s_waitcnt vmcnt(" #N ")\n\ts_barrier" ::: "memory")
#define BAR() asm volatile("s_barrier" ::: "memory")

#define STAMP(i) do {} while (0)
#define STAMP_OUT(ws, slot) do {} while (0)

DI int rfl(int x) { return __builtin_amdgcn_readfirstlane(x); }
DI bf16x8 ldsr128(lptr p) { return *(const LAS bf16x8*)p; }
DI s16x4 ldstr(lptr p) { return __builtin_amdgcn_ds_read_tr16_b64_v4i16((LAS s16x4*)p); }
DI bf16x8 cat8(s16x4 a, s16x4 b) { return __builtin_bit_cast(bf16x8, __builtin_shufflevector(a, b, 0, 1, 2, 3, 4, 5, 6, 7)); }
DI unsigned pk2(float a, float b) { f32x2 f = {a, b}; return __builtin_bit_cast(unsigned, __builtin_convertvector(f, bf16x2)); }
DI float bflo(unsigned u) { return __uint_as_float(u << 16); }
DI float bfhi(unsigned u) { return __uint_as_float(u & 0xffff0000u); }
DI int crow(int i, int h) { return (i & 3) + 8 * (i >> 2) + 4 * h; }
DI void st128(rsrc_t r, unsigned off, u32x4 v) { __builtin_amdgcn_raw_buffer_store_b128(v, r, off, 0, 16); }
DI void st64(rsrc_t r, unsigned off, u32x2 v) { __builtin_amdgcn_raw_buffer_store_b64(v, r, off, 0, 16); }
DI void st32(rsrc_t r, unsigned off, unsigned v) { __builtin_amdgcn_raw_buffer_store_b32(v, r, off, 0, 16); }
DI u32x4 ld128(rsrc_t r, unsigned off) { return __builtin_amdgcn_raw_buffer_load_b128(r, off, 0, 16); }
DI unsigned ld32(rsrc_t r, unsigned off) { return __builtin_amdgcn_raw_buffer_load_b32(r, off, 0, 16); }
DI unsigned flag_ld(const char* ws, int idx) { return __hip_atomic_load((const GAS unsigned*)(ws + WS_FL) + idx, __ATOMIC_RELAXED, __HIP_MEMORY_SCOPE_AGENT); }
DI void flag_st(char* ws, int idx, unsigned v) { __hip_atomic_store((GAS unsigned*)(ws + WS_FL) + idx, v, __ATOMIC_RELAXED, __HIP_MEMORY_SCOPE_AGENT); }

#define M0_IN "s_mov_b32 %0, m0\n\ts_mov_b32 m0, %1\n\ts_nop 0\n\t"
#define M0_OUT "s_mov_b32 m0, %0"
DI void dma_2x2(unsigned m0v, const void* b0, unsigned v0, unsigned v1, const void* b1, unsigned v2, unsigned v3) {
  unsigned sv;
  asm volatile(M0_IN "global_load_lds_dwordx4 %4, %2\n\tglobal_load_lds_dwordx4 %5, %2 offset:1024\n\t"
               "global_load_lds_dwordx4 %6, %3 offset:2048\n\tglobal_load_lds_dwordx4 %7, %3 offset:3072\n\t" M0_OUT
               : "=&s"(sv) : "s"(m0v), "s"(b0), "s"(b1), "v"(v0), "v"(v1), "v"(v2), "v"(v3) : "memory");
}
DI void dma_lin4_sc1(unsigned m0v, const void* b, unsigned vl) {
  unsigned sv;
  asm volatile(M0_IN "global_load_lds_dwordx4 %3, %2 sc1\n\tglobal_load_lds_dwordx4 %3, %2 offset:1024 sc1\n\t"
               "global_load_lds_dwordx4 %3, %2 offset:2048 sc1\n\tglobal_load_lds_dwordx4 %3, %2 offset:3072 sc1\n\t" M0_OUT
               : "=&s"(sv) : "s"(m0v), "s"(b), "v"(vl) : "memory");
}
DI void dma_g1(unsigned m0v, const void* wb, unsigned v0, unsigned v1, const void* hb_m2048, unsigned vl) {
  unsigned sv;
  asm volatile(M0_IN "global_load_lds_dwordx4 %4, %2\n\tglobal_load_lds_dwordx4 %5, %2 offset:1024\n\t"
               "global_load_lds_dwordx4 %6, %3 offset:2048 sc1\n\t"
               "s_mov_b32 exec_hi, 0\n\tglobal_load_lds_dwordx4 %6, %3 offset:3072 sc1\n\ts_mov_b32 exec_hi, -1\n\t" M0_OUT
               : "=&s"(sv) : "s"(m0v), "s"(wb), "s"(hb_m2048), "v"(v0), "v"(v1), "v"(vl) : "memory");
}
DI void dma_hh(unsigned m0v, const void* hb_m2048, unsigned vl) {
  unsigned sv;
  asm volatile(M0_IN "global_load_lds_dwordx4 %3, %2 offset:2048 sc1\n\t"
               "s_mov_b32 exec_hi, 0\n\tglobal_load_lds_dwordx4 %3, %2 offset:3072 sc1\n\ts_mov_b32 exec_hi, -1\n\t" M0_OUT
               : "=&s"(sv) : "s"(m0v), "s"(hb_m2048), "v"(vl) : "memory");
}
DI void dma_lin4(unsigned m0v, const void* b, unsigned vl) {
  unsigned sv;
  asm volatile(M0_IN "global_load_lds_dwordx4 %3, %2\n\tglobal_load_lds_dwordx4 %3, %2 offset:1024\n\t"
               "global_load_lds_dwordx4 %3, %2 offset:2048\n\tglobal_load_lds_dwordx4 %3, %2 offset:3072\n\t" M0_OUT
               : "=&s"(sv) : "s"(m0v), "s"(b), "v"(vl) : "memory");
}
DI void dma_touch(unsigned m0v, const void* b, unsigned voff) {
  unsigned sv;
  asm volatile(M0_IN "global_load_lds_dword %3, %2\n\t" M0_OUT : "=&s"(sv) : "s"(m0v), "s"(b), "v"(voff) : "memory");
}
DI void dma_wh(unsigned m0v, const void* wb, unsigned v0, unsigned v1) {
  unsigned sv;
  asm volatile(M0_IN "global_load_lds_dwordx4 %3, %2\n\tglobal_load_lds_dwordx4 %4, %2 offset:1024\n\t" M0_OUT
               : "=&s"(sv) : "s"(m0v), "s"(wb), "v"(v0), "v"(v1) : "memory");
}
DI void dma_fh(unsigned m0v, const void* fb_m2048, unsigned vl) {
  unsigned sv;
  asm volatile(M0_IN "global_load_lds_dwordx4 %3, %2 offset:2048 sc1\n\tglobal_load_lds_dwordx4 %3, %2 offset:3072 sc1\n\t" M0_OUT
               : "=&s"(sv) : "s"(m0v), "s"(fb_m2048), "v"(vl) : "memory");
}

DI void spin1(rsrc_t r, unsigned off, unsigned tok) {
  unsigned n = 0;
#pragma nounroll
  do { const unsigned v = ld32(r, off); if (__all(v == tok)) break; __builtin_amdgcn_s_sleep(4); asm volatile("" ::: "memory"); } while (++n < SPIN_CAP);
  __builtin_amdgcn_fence(__ATOMIC_ACQUIRE, "wavefront");
}
DI void peek_issue(unsigned m0v, const void* ws, unsigned off) {
  unsigned sv;
  asm volatile(M0_IN "global_load_lds_dwordx4 %3, %2 sc1\n\t" M0_OUT : "=&s"(sv) : "s"(m0v), "s"(ws), "v"(off) : "memory");
}
DI void peek_check(lptr p, rsrc_t r, unsigned off, unsigned tok) {
  if (__all(*(const volatile LAS unsigned*)p == tok)) return;
  asm volatile("s_waitcnt vmcnt(0)" ::: "memory");
  if (__all(*(const volatile LAS unsigned*)p == tok)) return;
  spin1(r, off, tok);
}
#define RARE(c) __builtin_expect((c) != 0, 0)

DI float wave_sum(float v) {
#define DPPADD(ctrl, rm) v += __int_as_float(__builtin_amdgcn_update_dpp(0, __float_as_int(v), ctrl, rm, 0xf, false))
  DPPADD(0x111, 0xf); DPPADD(0x112, 0xf); DPPADD(0x114, 0xf); DPPADD(0x118, 0xf); DPPADD(0x142, 0xa); DPPADD(0x143, 0xc);
#undef DPPADD
  return __int_as_float(__builtin_amdgcn_readlane(__float_as_int(v), 63));
}
DI float row16_sum(float v) {
#define DPPX(ctrl) v += __int_as_float(__builtin_amdgcn_update_dpp(0, __float_as_int(v), ctrl, 0xf, 0xf, true))
  DPPX(0xB1); DPPX(0x4E); DPPX(0x141); DPPX(0x140);
#undef DPPX
  return v;
}
DI float gelu_tanh(float u) {
  const float z = u * (0.7978845608028654f + 0.7978845608028654f * 0.044715f * u * u);
  const float e = __builtin_amdgcn_exp2f(z * (-2.0f * 1.4426950408889634f));
  return u * __builtin_amdgcn_rcpf(1.0f + e);
}
constexpr unsigned long long diag_mask(int i) {
  unsigned long long mk = 0;
  for (int l = 0; l < 64; ++l) if ((i & 3) + 8 * (i >> 2) + 4 * (l >> 5) <= (l & 31)) mk |= 1ull << l;
  return mk;
}
DI float max16(const f32x16& s, float m) {
#pragma unroll
  for (int i = 0; i < 16; i += 2) m = __builtin_fmaxf(__builtin_fmaxf(s[i], s[i + 1]), m);
  return m;
}

#define NEXT_SO() so = so == (NS - 1) * SLOT ? 0 : so + SLOT

template <bool ISV, class Hook, class Hook14, class Mid>
DI void qkv_consume(lptr L, int& so, int w, int lane, f32x16 (&acc)[2], Hook hook, Hook14 hook14, Mid mid) {
  const int r = lane & 31, h = lane >> 5, wm = w >> 2, wn = w & 3;
  int xa[4];
#pragma unroll
  for (int s = 0; s < 4; ++s) xa[s] = (wm * 2 + (r >> 4)) * 4096 + (r & 15) * 128 + (((2 * s + h) ^ ((r >> 1) & 7)) << 4);
  const int q = (lane & 15) >> 2, p = lane & 3, blk = (lane >> 4) & 1;
  const int wa = h * 4096 + 2048 + q * 256 + (((wn * 4 + blk * 2 + (p >> 1)) ^ (q << 2)) << 4) + (p & 1) * 8;
  bf16x8 xA[2][4], xB[2][4], wA[4], wB[4];
#define QM(X, W, mi, s) do { if (ISV) acc[mi] = MFMA32(X[mi][s], W[s], acc[mi]); else acc[mi] = MFMA32(W[s], X[mi][s], acc[mi]); SB(); } while (0)
#define QRX(X, mi, s) X[mi][s] = ldsr128(sl + xa[s])
#define QRW(W, mi, e) W[2 * mi + e] = cat8(ldstr(sl + wa + e * 8192), ldstr(sl + wa + e * 8192 + 1024))
#define QSTEP(mi, XN, WN, XP, WP, MM) do { lptr sl = L + so; NEXT_SO(); \
    if (MM) QM(XP, WP, 0, 2 * mi); \
    QRX(XN, mi, 0); QRX(XN, mi, 1); SB(); if (MM) QM(XP, WP, 1, 2 * mi); \
    QRX(XN, mi, 2); QRX(XN, mi, 3); QRW(WN, mi, 0); SB(); if (MM) QM(XP, WP, 0, 2 * mi + 1); \
    QRW(WN, mi, 1); SB(); if (MM) QM(XP, WP, 1, 2 * mi + 1); } while (0)
  LBAR(); QSTEP(0, xA, wA, xB, wB, false); QSTEP(1, xA, wA, xB, wB, false);
#pragma unroll 1
  for (int c = 1; c < 7; c += 2) {
    hook(c);     QSTEP(0, xB, wB, xA, wA, true); QSTEP(1, xB, wB, xA, wA, true);
    hook(c + 1); QSTEP(0, xA, wA, xB, wB, true); QSTEP(1, xA, wA, xB, wB, true);
  }
  hook14(); QSTEP(0, xB, wB, xA, wA, true); QSTEP(1, xB, wB, xA, wA, true);
#pragma unroll
  for (int s = 0; s < 4; ++s) QM(xB, wB, 0, s);
  mid();
#pragma unroll
  for (int s = 0; s < 4; ++s) QM(xB, wB, 1, s);
}

__global__ __launch_bounds__(768, 3) void fused_kernel(const char* __restrict__ x, const char* __restrict__ wqkv, const char* __restrict__ wo,
                                                       const char* __restrict__ w1, const char* __restrict__ b1, const char* __restrict__ w2,
                                                       const char* __restrict__ b2, const char* __restrict__ gw, char* __restrict__ ws,
                                                       char* __restrict__ out) {
  extern __shared__ __attribute__((aligned(16))) char smem[];
  asm volatile("" :: "s"(x), "s"(wqkv), "s"(wo), "s"(w1), "s"(b1), "s"(w2), "s"(b2), "s"(gw), "s"(ws), "s"(out));
  STAMP(0);
  const int b = blockIdx.x, xcd = b & 7, idx = ((b >> 3) + 24) & 31;
  const bool lead = b == 65;
  const int lane = threadIdx.x & 63, w = rfl(threadIdx.x >> 6);
  const unsigned lds0 = (unsigned)(uintptr_t)smem;
  lptr L = (lptr)smem;
  const int tm = (xcd >> 1) * 4 + idx / 6, tn = (xcd & 1) * 6 + idx % 6;
  const int lnb = xcd * 8 + (idx - 24);
  const unsigned x0 = (lane >> 3) * 1024 + (((lane & 7) ^ (lane >> 4)) << 4), x1 = (8 + (lane >> 3)) * 1024 + (((lane & 7) ^ (4 + (lane >> 4))) << 4) - 1024;
  const unsigned q0 = (lane >> 4) * 3072 + (((lane & 15) ^ ((lane >> 4) << 2)) << 4), q1 = q0 + 4 * 3072 - 1024;
  const char* xp = x + (size_t)(tm * 128 + ((w >> 1) & 1) * 64 + (w & 1) * 16) * 1024;
  const char* qp = wqkv + (size_t)((w & 3) * 8) * 3072 + tn * 256 - 2048;
  int kc = QROT ? ((idx / 6 + idx % 6) % 3) * 3 : 0;
  if (w >= 8) {
    if (idx < 24) {
#pragma unroll 1
      for (int f = 0; f < 7; ++f) {
        dma_2x2(lds0 + (w & 3) * 4096 + f * SLOT, xp + kc * 128 + (f & 1) * 32 * 1024, x0, x1, qp + (size_t)(kc * 2 + (f & 1)) * (32 * 3072), q0, q1);
        kc = (kc + (f & 1)) & 7;
      }
    }
  }
  asm volatile("" ::: "memory");
  SB();
  const int gi = 31 - idx;
  const bool hasQ = idx < 24, hasG1 = xcd == 0 || gi < 30;
  const bool lowd = xcd == 0 ? gi < 12 : xcd < 5;
  const int gj = xcd == 0 && !lowd ? gi - 12 : gi;
  const int g1ft = lowd ? (xcd == 0 ? 20 : (xcd - 1) * 5) + gj / 6 : (xcd == 0 ? 18 : (xcd - 5) * 6) + gj / 5;
  const int g1dt = lowd ? gj % 6 : 6 + gj % 5;
  const int head = xcd * 4 + (idx & 3), qb = idx >> 2;
  const int st = (xcd >> 1) * 8 + (idx >> 2), dt = (xcd & 1) * 4 + (idx & 3);
  unsigned fq_off;
  { const int l = lane < 12 ? lane : 3, wh = l >> 2; int tmi = l & 3; if (wh == 0 || qb == 0) tmi |= 2;
    fq_off = WS_FL + (FL_Q + ((wh * 4 + (head >> 3)) * 16 + 2 * qb - 2 + tmi) * 32) * 4; }
  const unsigned fu_off = WS_FL + (FL_U + (((lane & 32) ? (4 * dt + 3) / 3 : (4 * dt) / 3) * 32 + min(lane & 31, 21)) * 32) * 4;
  const unsigned fo_off = WS_FL + (FL_O + ((st >> 2) * 32 + (lane & 31)) * 32) * 4;
  const unsigned fh_off = WS_FL + FL_H * 4 + lane * 128;
  const rsrc_t rws = __builtin_amdgcn_make_buffer_rsrc(ws, 0, WS_END, 0x00020000);
  volatile LAS unsigned* relay = (volatile LAS unsigned*)(L + L_C + 64);
  unsigned eoff = WS_FL + FL_EPOCH * 4;
  asm volatile("" : "+v"(eoff));
  const unsigned epoch = ld32(rws, eoff);
#define TOKEN_OF(e) ((e) * 1664525u + 1013904223u)

  if (w >= 8) {
    const int j = w - 8;
#define token TOKEN_OF(epoch)
    const unsigned m0b = lds0 + j * 4096;
    const unsigned vl = lane * 16;
    unsigned so = 0;
    const char* hp = ws + WS_H + (size_t)g1dt * 64 * 3072 + j * 1536 - 2048;
    const char* w1p = w1 + (size_t)(j * 16) * 2752 + g1ft * 128;
    const unsigned u0 = (lane >> 3) * 2752 + ((((lane & 7) ^ (((lane >> 4) & 3) << 1)) & (g1ft == 21 ? 3 : 7)) << 4), u1 = u0 + 8 * 2752 - 1024;
    int cw = G1ROT ? (lowd ? g1dt * 6 : (g1dt - 6) * 6 + 3) : 0, ch = cw;
    auto fill_g1 = [&]() { dma_g1(m0b + so, w1p + (size_t)cw * (64 * 2752), u0, u1, hp + cw * 6144, vl); cw = (cw + 1) & 31; NEXT_SO(); };
    auto fill_g1w = [&]() { dma_wh(m0b + so, w1p + (size_t)cw * (64 * 2752), u0, u1); cw = (cw + 1) & 31; NEXT_SO(); };
    auto fill_g1h3 = [&](unsigned so0) {
      const unsigned keep = so; so = so0;
#pragma unroll 1
      for (int i = 0; i < 3; ++i) { dma_hh(m0b + so, hp + ch * 6144, vl); ch = (ch + 1) & 31; NEXT_SO(); }
      so = keep;
    };
    const unsigned swz = ((lane & 7) ^ (((lane >> 4) & 1) << 2)) << 4;
    unsigned vw0 = (lane >> 3) * 4096 + swz, vw1 = vw0 + 8 * 4096 - 1024;
    const char* w2p = w2 + (size_t)(j * 16) * 4096 + st * 128;
    const char* up = ws + WS_U + (size_t)dt * 86 * 2048 + j * 2048 - 2048;
    const size_t w2half = (size_t)(j < 2 ? 21 * 64 : 20 * 64) * 4096;
    const int uhalf = j < 2 ? 21 * 8192 : 20 * 8192;
    auto fill_kv = [&]() {
      int t0 = qb * 8 - 8 + 4 * j; t0 = t0 < 0 ? t0 + 8 : t0;
      dma_lin4_sc1(lds0 + L_K + j * 4096, ws + WS_K + (size_t)(head * 64 + t0) * 1024, vl);
      dma_lin4_sc1(lds0 + L_V + j * 4096, ws + WS_V + (size_t)(head * 64 + t0) * 1024, vl);
    };
    int nwh = 0;
    const int r2 = G2ROT ? (((dt & 3) * 2 + (dt >> 2)) * 21) >> 3 : 0;
    int c2 = r2;
    auto fill_wh = [&]() {
      dma_wh(m0b + so, nwh == 0 ? w2p + w2half : w2p + (size_t)c2 * (64 * 4096), vw0, vw1);
      if (nwh) c2 = c2 == 20 ? 0 : c2 + 1;
      ++nwh; NEXT_SO();
    };
    unsigned so_g2 = 0;
    auto warm_g2 = [&]() {
      if (PREFETCH & 48) {
        const int t = idx * 4 + j;
        if ((PREFETCH & 16) && t < 120) dma_touch(lds0 + L_P + 1024, w2 + (size_t)(384 + t * 8) * 4096 + (xcd >> 1) * 1024, (lane >> 3) * 4096 + (lane & 7) * 128);
        if (j == 1) dma_touch(lds0 + L_P + 1024, wo + (size_t)(idx * 16) * 1024 + (xcd & 1) * 512, (lane >> 2) * 1024 + (lane & 3) * 128);
        if (OTOUCH == 1 && j == 2) dma_touch(lds0 + L_P + 1024, out + (size_t)st * 65536 + dt * 128, lane * 1024);
        if (OTOUCH == 2 && j == 2) dma_touch(lds0 + L_P + 1024, out + (size_t)st * 65536 + dt * 128, 0);
        if (OTOUCH && j == 3) dma_touch(lds0 + L_P + 1024, b2 + st * 128, 0);
      }
    };

    if (hasQ) {
      auto fill_a = [&]() { dma_2x2(m0b + so, xp + kc * 128, x0, x1, qp + (size_t)kc * 64 * 3072, q0, q1); NEXT_SO(); };
      auto fill_b = [&]() { dma_2x2(m0b + so, xp + kc * 128 + 32 * 1024, x0, x1, qp + (size_t)kc * 64 * 3072 + 32 * 3072, q0, q1); kc = (kc + 1) & 7; NEXT_SO(); };
      VBAR(20);
#pragma unroll 1
      for (int c = 1; c < 5; ++c) { VBAR(12); fill_b(); fill_a(); }
      VBAR(12); fill_b();
      if (hasG1) {
        const unsigned so0 = so;
        fill_g1w();
        VBAR(10); fill_g1w(); fill_g1w();
        VBAR(6); fill_g1h3(so0); fill_g1(); fill_g1();
      } else {
        VBAR(8); VBAR(0);
      }
    } else {
      BAR();
      VBAR(0);
#pragma unroll 1
      for (int s = 0; s < 3; ++s) fill_g1w();
      if (j == 0) spin1(rws, fh_off, token);
      else if (PREFETCH & 2) {
        const int pw = lnb * 3 + (j - 1);
#pragma unroll 1
        for (int i = pw; i < 752; i += 192) dma_touch(lds0 + L_P + 1024, i < 688 ? w2 + (size_t)i * 8192 : wo + (size_t)(i - 688) * 8192, lane * 128);
      } else if (PREFETCH & 4) {
        const int pw = lnb * 3 + (j - 1);
#pragma unroll 1
        for (int i = pw; i < 688; i += 192) dma_touch(lds0 + L_P + 1024, w1 + (size_t)i * 8192, lane * 128);
      }
      BAR();
      fill_g1h3(0); fill_g1(); fill_g1();
    }
    STAMP(1);
    if (hasG1) {
      VBAR(12); fill_g1(); VBAR(14); fill_g1();
#pragma unroll 1
      for (int s = 2; s <= KVS; ++s) { VBAR(16); fill_g1(); }
      fill_kv();
#pragma unroll 1
      for (int s = KVS + 1; s < 27; ++s) { VBAR(16); fill_g1(); }
      so_g2 = so;
      VBAR(16); fill_wh(); VBAR(14); fill_wh(); VBAR(12); fill_wh(); VBAR(10); fill_wh(); VBAR(8); fill_wh();
      BAR(); fill_wh(); fill_wh();
      warm_g2();
      BAR();
      BAR();
    } else {
      BAR();
      so_g2 = so;
#pragma unroll 1
      for (int i = 0; i < 7; ++i) fill_wh();
      warm_g2();
      if (j == 0) spin1(rws, fq_off, token);
      BAR();
      fill_kv();
      VBAR(0);
    }
    STAMP(2);
    {
      if (j == 0) { spin1(rws, fu_off, token); *relay = 1u; }
      else { for (unsigned spins = 0; spins < 64 * SPIN_CAP && *relay == 0u; ++spins) __builtin_amdgcn_s_sleep(1); }
      STAMP(3);
      so = so_g2;
      dma_fh(m0b + so, up + uhalf, vl); NEXT_SO();
#pragma unroll 1
      for (int i = 0, c = r2; i < 6; ++i) { dma_fh(m0b + so, up + c * 8192, vl); c = c == 20 ? 0 : c + 1; NEXT_SO(); }
    }
    unsigned wsh = 18;
    int ci = 7;
    auto fill_g2 = [&]() {
      if (ci == 22) {
        w2p = wo + (size_t)(j * 16) * 1024 + dt * 128; up = ws + WS_O + (size_t)st * 32 * 2048 + j * 2048 - 2048; wsh = 16; c2 = 0;
        vw0 = (lane >> 3) * 1024 + swz; vw1 = vw0 + 8 * 1024 - 1024;
      }
      dma_wh(m0b + so, w2p + ((size_t)c2 << wsh), vw0, vw1); dma_fh(m0b + so, up + c2 * 8192, vl);
      c2 = c2 == 20 ? 0 : c2 + 1; NEXT_SO(); ++ci;
    };
    VBAR(12); VBAR(10);
    VBAR(8); fill_g2(); VBAR(10); fill_g2(); VBAR(12); fill_g2(); VBAR(14); fill_g2();
#pragma unroll 1
    for (int s = 6; s < 25; ++s) { VBAR(16); fill_g2(); }
    const unsigned fa_off = WS_FL + FL_O * 4 + (j * 64 + lane) * 128;
    if (lead) peek_issue(lds0 + L_X + j * 1024, ws, fa_off);
    VBAR(16); VBAR(12); VBAR(8); VBAR(4); VBAR(0);
    if (lead) peek_check(L + L_X + j * 1024 + lane * 16, rws, fa_off, token);
    BAR();
    if (lead && j == 0 && lane == 0) st32(rws, WS_FL + FL_EPOCH * 4, token);
    BAR();
    STAMP(4);
    if (j == 0) STAMP_OUT(ws, 1);
    return;
  }

#undef token
  unsigned token;
  u32x4 xv[4], gv[4];
  if (!hasQ) {
#pragma unroll
    for (int i = 0; i < 4; ++i) xv[i] = *(const u32x4*)(x + (size_t)(lnb * 32 + w * 4 + (lane >> 4)) * 1024 + (lane & 15) * 16 + i * 256);
#pragma unroll
    for (int i = 0; i < 4; ++i) gv[i] = *(const u32x4*)(gw + (lane & 15) * 16 + i * 256);
  }
  asm volatile("" ::: "memory");
  SB();
  const int r = lane & 31, h = lane >> 5;
  const int g4 = lane >> 4, q4 = (lane & 15) >> 2, p4 = lane & 3, blk = (lane >> 4) & 1;
  int so = 0;
  if (threadIdx.x == 4) *relay = 0u;
  if (threadIdx.x < 4) {
    const unsigned cv = (threadIdx.x & 1) ? 0u : 0x3f803f80u;
    *(LAS u32x4*)(L + L_C + (threadIdx.x >> 1) * 512 + (threadIdx.x & 1) * 16) = u32x4{cv, cv, cv, cv};
  }
  lptr pk = L + L_P + lane * 16;

  const int kh = w >> 2, fb = w & 3;
  int ha[3];
#pragma unroll
  for (int db = 0; db < 3; ++db) { const int o = (kh * 3 + db) * 1024 + lane * 16, rg = o / 1536; ha[db] = rg * 4096 + 2048 + (o - rg * 1536); }
  const int wa = kh * 8192 + (4 * g4 + q4) * 128 + ((fb ^ ((2 * g4 + (q4 >> 1)) & 3)) << 5) + p4 * 8;
  f32x4 acc[3] = {};
  bf16x8 wA = {}, hA[3] = {}, wB = {}, hB[3] = {};
#define G1RD(W, H) do { lptr sl = L + so; NEXT_SO(); W = cat8(ldstr(sl + wa), ldstr(sl + wa + 4096)); \
  _Pragma("unroll") for (int db = 0; db < 3; ++db) H[db] = ldsr128(sl + ha[db]); SB(); } while (0)
#define G1MM(W, H) do { _Pragma("unroll") for (int db = 0; db < 3; ++db) acc[db] = MFMA16(W, H[db], acc[db]); SB(); } while (0)

  token = TOKEN_OF(epoch);
  asm volatile("" : "+v"(token), "+v"(gv[3]));
  if (hasQ) {
    if (w == 7) *(LAS unsigned*)pk = ~token;
    STAMP(1);
    f32x16 qacc[2] = {};
    const bool poller = w == 7 && hasG1;
    const int pi = (xcd * 24 + idx) * 4 + w;
    const unsigned post = (poller ? (1u << 4) | (1u << 5) | (1u << 6) : 0u) | (((PREFETCH & 1) && w < 4 && pi < 688) ? 1u << PFSTEP : 0u);
    if (post & 1) dma_touch(lds0 + L_P + 1024, w1 + (size_t)pi * 8192, lane * 128);
    auto hook = [&](int s) {
      LBAR();
      if (RARE((post >> s) & 1)) { if (w == 7) peek_issue(lds0 + L_P, ws, fh_off); else dma_touch(lds0 + L_P + 1024, w1 + (size_t)pi * 8192, lane * 128); }
      SB();
    };
    auto hook14 = [&]() { if (poller) peek_check(pk, rws, fh_off, token); LBAR(); };
    auto mid = [&]() { if (hasG1) { LBAR(); G1RD(wA, hA); } };
    if (tn >= 8) qkv_consume<true>(L, so, w, lane, qacc, hook, hook14, mid);
    else qkv_consume<false>(L, so, w, lane, qacc, hook, hook14, mid);
    STAMP(2);
    const int wm = w >> 2, wn = w & 3;
    const int hd0 = (tn & 3) * 8 + wn * 2;
    const float sc = tn < 4 ? QSCALE : 1.0f;
    const unsigned dst = tn >= 8 ? WS_V + (((hd0 + (r >> 4)) * 64 + tm * 4 + wm * 2) * 64 + h * 16 + (r & 15)) * 16
                                 : (tn < 4 ? WS_Q : WS_K) + ((hd0 * 64 + tm * 4 + wm * 2) * 64 + lane) * 16;
    const unsigned dstep = tn >= 8 ? 512 : 65536;
#pragma unroll
    for (int mi = 0; mi < 2; ++mi) {
#pragma unroll
      for (int s = 0; s < 2; ++s) {
        u32x4 v;
#pragma unroll
        for (int jj = 0; jj < 4; ++jj) v[jj] = pk2(qacc[mi][8 * s + 2 * jj] * sc, qacc[mi][8 * s + 2 * jj + 1] * sc);
        st128(rws, dst + mi * 1024 + s * dstep, v);
      }
      if (mi == 0 && hasG1) { SB(); LBAR(); G1RD(wB, hB); G1MM(wA, hA); }
    }
  } else {
    constexpr int RS = 1056;
    f32x2 v[16], s2 = {0.f, 0.f}, g2 = {0.f, 0.f};
#pragma unroll
    for (int i = 0; i < 16; ++i) {
      v[i] = f32x2{bflo(xv[i >> 2][i & 3]), bfhi(xv[i >> 2][i & 3])};
      s2 += v[i];
      g2 += v[i] * f32x2{bflo(gv[i >> 2][i & 3]), bfhi(gv[i >> 2][i & 3])};
    }
    const float mean = row16_sum(s2[0] + s2[1]) * (1.0f / 512), gs = row16_sum(g2[0] + g2[1]);
    f32x2 q2 = {0.f, 0.f};
#pragma unroll
    for (int i = 0; i < 16; ++i) { v[i] -= f32x2{mean, mean}; q2 += v[i] * v[i]; }
    const float rs = __builtin_amdgcn_rsqf(row16_sum(q2[0] + q2[1]) * (1.0f / 512) + 1e-5f);
#pragma unroll
    for (int i = 0; i < 4; ++i) {
      u32x4 ov;
#pragma unroll
      for (int jj = 0; jj < 4; ++jj) { const f32x2 t = v[4 * i + jj] * f32x2{rs, rs}; ov[jj] = pk2(t[0], t[1]); }
      *(LAS u32x4*)(L + (w * 4 + g4) * RS + (lane & 15) * 16 + i * 256) = ov;
    }
    if ((lane & 15) == 0) st32(rws, WS_G + (lnb * 32 + w * 4 + g4) * 4, __float_as_uint(1.0f / (1.0f + __expf(-gs))));
    STAMP(1);
    LBAR();
#pragma unroll
    for (int i = 0; i < 4; ++i) {
      const int D = w * 4 + i, d48 = D / 3;
      lptr a = L + (4 * g4 + q4) * RS + D * 32 + p4 * 8;
      const bf16x8 f = cat8(ldstr(a), ldstr(a + 16 * RS));
      st128(rws, WS_H + ((d48 * 64 + lnb) * 3 + (D - d48 * 3)) * 1024 + lane * 16, __builtin_bit_cast(u32x4, f));
      if (i == 3 && w == 7) st128(rws, WS_H + ((10 * 64 + lnb) * 3 + 2) * 1024 + lane * 16, __builtin_bit_cast(u32x4, f));
    }
    DBAR();
    if (threadIdx.x == 0) flag_st(ws, FL_H + lnb * 32, token);
    STAMP(2);
    LBAR();
    LBAR(); G1RD(wA, hA);
    LBAR(); G1RD(wB, hB); G1MM(wA, hA);
  }

  bf16x8 qf;
  if (hasG1) {
    const unsigned pre = (1u << QDS) | (w == 7 ? 1u << KVS : 0u);
    const unsigned post = (w == 0 && hasQ ? 1u << QDS : 0u) | (w == 7 ? (1u << QDS) | (1u << (KVS - 6)) | (1u << (KVS - 4)) | (1u << (KVS - 2)) : 0u);
    auto hook = [&](int s) {
      if (RARE((pre >> s) & 1)) { if (s == QDS) asm volatile("s_waitcnt vmcnt(0)" ::: "memory"); else peek_check(pk, rws, fq_off, token); }
      CBAR(5);
      if (RARE((post >> s) & 1)) {
        if (s == QDS) { if (w == 7) *(LAS unsigned*)pk = ~token; else if (lane == 0) flag_st(ws, FL_Q + (tn * 16 + tm) * 32, token); }
        else peek_issue(lds0 + L_P, ws, fq_off);
      }
      SB();
    };
#pragma unroll 1
    for (int s = 2; s < KVS + 2; s += 2) {
      hook(s); G1RD(wA, hA); G1MM(wB, hB);
      CBAR(5); G1RD(wB, hB); G1MM(wA, hA);
    }
    STAMP(10);
    qf = __builtin_bit_cast(bf16x8, ld128(rws, WS_Q + (head * 64 + qb * 8 + w) * 1024 + lane * 16));
    const int fblk = g1ft * 4 + fb;
    u32x2 bb = *(const u32x2*)(b1 + min(fblk * 16 + 4 * g4, 1372) * 2);
    unsigned ua[2];
#pragma unroll
    for (int e = 0; e < 2; ++e) {
      const int D = g1dt * 3 + (kh ? 2 : e);
      ua[e] = WS_U + ((((D >> 2) * 86 + fblk) * 2 + ((D >> 1) & 1)) * 64 + (g4 >> 1) * 32 + (D & 1) * 16 + (lane & 15)) * 16 + (g4 & 1) * 8;
      if (fblk >= 86 || D >= 32) ua[e] = WS_END;
    }
    lptr xw = L + L_X + (fb * 3 * 64 + lane) * 16;
#pragma unroll 1
    for (int s = KVS + 2; s < 32; s += 2) {
      CBAR(5); G1RD(wA, hA); G1MM(wB, hB);
      CBAR(5); G1RD(wB, hB); G1MM(wA, hA);
    }
    asm volatile("" : "+v"(qf), "+v"(bb), "+v"(ua[0]), "+v"(ua[1]));
    const f32x4 bv = {bflo(bb[0]), bfhi(bb[0]), bflo(bb[1]), bfhi(bb[1])};
    LBAR();
    G1MM(wB, hB);
    STAMP(3);
    if (kh == 1) { *(LAS f32x4*)(xw) = acc[0]; *(LAS f32x4*)(xw + 1024) = acc[1]; }
    else *(LAS f32x4*)(xw + 2048) = acc[2];
    LBAR();
    auto fin = [&](const f32x4& a, int off, unsigned addr) {
      const f32x4 t = a + *(const LAS f32x4*)(xw + off) + bv;
      st64(rws, addr, u32x2{pk2(gelu_tanh(t[0]), gelu_tanh(t[1])), pk2(gelu_tanh(t[2]), gelu_tanh(t[3]))});
    };
    if (kh == 1) fin(acc[2], 2048, ua[0]);
    else { fin(acc[0], 0, ua[0]); fin(acc[1], 1024, ua[1]); }
  } else {
    DBAR();
    if (threadIdx.x == 0) flag_st(ws, FL_Q + (tn * 16 + tm) * 32, token);
    LBAR();
    qf = __builtin_bit_cast(bf16x8, ld128(rws, WS_Q + (head * 64 + qb * 8 + w) * 1024 + lane * 16));
    asm volatile("" : "+v"(qf));
    LBAR();
    STAMP(3);
  }

  {
    lptr kp = L + L_K + w * 1024 + lane * 16;
    const int i_lo = qb > 0 ? 1 : 8 - w;
    const f32x16 zero = {};
    bf16x8 kf[9];
#pragma unroll
    for (int i = 0; i < 9; ++i) kf[i] = ldsr128(kp + i * 1024);
    f32x16 sb, sa, sc;
    float mx[4];
#define TMAX(S, i) (((i) >= i_lo) ? max16(S, -INFINITY) : -INFINITY)
    sa = MFMA32(kf[8], qf, zero); sc = MFMA32(kf[0], qf, zero);
    f32x16 s1 = MFMA32(kf[1], qf, zero), s2 = MFMA32(kf[2], qf, zero); SB();
#pragma unroll
    for (int i = 0; i < 16; ++i) sb[i] = __builtin_amdgcn_inverse_ballot_w64(diag_mask(i)) ? sa[i] : (qb > 0 ? sc[i] : -INFINITY);
    SB();
    sa = MFMA32(kf[3], qf, zero); sc = MFMA32(kf[4], qf, zero); SB();
    mx[0] = max16(sb, -INFINITY); mx[1] = TMAX(s1, 1); mx[2] = TMAX(s2, 2); SB();
    s1 = MFMA32(kf[5], qf, zero); s2 = MFMA32(kf[6], qf, zero); SB();
    mx[3] = TMAX(sa, 3); mx[0] = __builtin_fmaxf(mx[0], TMAX(sc, 4)); SB();
    sa = MFMA32(kf[7], qf, zero); SB();
    mx[1] = __builtin_fmaxf(mx[1], TMAX(s1, 5)); mx[2] = __builtin_fmaxf(mx[2], TMAX(s2, 6)); mx[3] = __builtin_fmaxf(mx[3], TMAX(sa, 7));
    float m = __builtin_fmaxf(__builtin_fmaxf(mx[0], mx[1]), __builtin_fmaxf(mx[2], mx[3]));
    {
      auto rr = __builtin_amdgcn_permlane32_swap(__float_as_uint(m), __float_as_uint(m), false, false);
      m = __builtin_fmaxf(__uint_as_float(rr[0]), __uint_as_float(rr[1]));
    }
    STAMP(11);
    if (hasG1) {
      DBAR();
      if (threadIdx.x == 0) flag_st(ws, FL_U + (g1dt * 32 + g1ft) * 32, token);
    }
    STAMP(4);
    const int vstr = r < 16 ? 1024 : 0;
    lptr vp0 = L + (r < 16 ? L_V + w * 1024 + h * 256 + r * 16 : (L_C + ((r == 16 || r == 20) ? 0 : 16)));
    f32x16 cin;
#pragma unroll
    for (int i = 0; i < 16; ++i) cin[i] = -m;
    f32x16 s = MFMA32(ldsr128(kp + i_lo * 1024), qf, cin);
    f32x16 o = {};
    {
      u32x4 pd[2], pf[2];
#pragma unroll
      for (int i = 0; i < 16; i += 2) {
        const unsigned pp = pk2(__builtin_amdgcn_exp2f(sb[i] - m), __builtin_amdgcn_exp2f(sb[i + 1] - m));
        const int dlt = r - crow(i, h);
        const unsigned mk = dlt >= 1 ? 0xffffffffu : (dlt == 0 ? 0x0000ffffu : 0u);
        pd[i >> 3][(i >> 1) & 3] = pp & mk;
        pf[i >> 3][(i >> 1) & 3] = pp & ~mk;
      }
      lptr vp = vp0 + 8 * vstr;
      o = MFMA32(ldsr128(vp), __builtin_bit_cast(bf16x8, pd[0]), o);
      o = MFMA32(ldsr128(vp + 512), __builtin_bit_cast(bf16x8, pd[1]), o);
      o = MFMA32(ldsr128(vp0), __builtin_bit_cast(bf16x8, pf[0]), o);
      o = MFMA32(ldsr128(vp0 + 512), __builtin_bit_cast(bf16x8, pf[1]), o);
    }
#pragma unroll 1
    for (int i = i_lo; i < 8; ++i) {
      lptr vp = vp0 + i * vstr;
      const bf16x8 kn = ldsr128(kp + (i + 1) * 1024), v0 = ldsr128(vp), v1 = ldsr128(vp + 512);
      u32x4 p0, p1;
#pragma unroll
      for (int e = 0; e < 8; e += 2) p0[e >> 1] = pk2(__builtin_amdgcn_exp2f(s[e]), __builtin_amdgcn_exp2f(s[e + 1]));
      o = MFMA32(v0, __builtin_bit_cast(bf16x8, p0), o);
#pragma unroll
      for (int e = 8; e < 16; e += 2) p1[(e >> 1) & 3] = pk2(__builtin_amdgcn_exp2f(s[e]), __builtin_amdgcn_exp2f(s[e + 1]));
      s = MFMA32(kn, qf, cin);
      o = MFMA32(v1, __builtin_bit_cast(bf16x8, p1), o);
    }
    const float il = __builtin_amdgcn_rcpf(o[8]);
    u32x4 ov;
#pragma unroll
    for (int jj = 0; jj < 4; ++jj) ov[jj] = pk2(o[2 * jj] * il, o[2 * jj + 1] * il);
    const int B = qb * 8 + w;
    st128(rws, WS_O + (((B >> 1) * 32 + head) * 2 + (B & 1)) * 1024 + lane * 16, ov);
  }
  STAMP(5);

  {
    const int mt = (w >> 1) & 1, nt = w & 1;
    const int tok = st * 64 + nt * 32 + r;
    const int ta2 = kh * 8192 + (8 * h + q4) * 128 + (((nt * 4 + blk * 2 + (p4 >> 1)) ^ ((q4 >> 1) << 2)) << 4) + (p4 & 1) * 8;
    const int tao = kh * 8192 + (4 * h + q4) * 128 + (((mt * 4 + blk * 2 + (p4 >> 1)) ^ ((q4 >> 1) << 2)) << 4) + (p4 & 1) * 8;
    const int fa2 = kh * 8192 + 2048 + mt * 1024 + lane * 16;
    const int fao = kh * 8192 + 2048 + nt * 1024 + lane * 16;
    f32x16 ay = {}, aa = {};
    bf16x8 pa0, pb0, pa1, pb1, qa0, qb0, qa1, qb1;
    float g = 0.f; unsigned b2v = 0;
#define RD2(A0, B0, A1, B1) do { lptr sl = L + so; NEXT_SO(); B0 = cat8(ldstr(sl + ta2), ldstr(sl + ta2 + 512)); A0 = ldsr128(sl + fa2); \
    B1 = cat8(ldstr(sl + ta2 + 4096), ldstr(sl + ta2 + 4096 + 512)); A1 = ldsr128(sl + fa2 + 4096); SB(); } while (0)
#define RDO(A0, B0, A1, B1) do { lptr sl = L + so; NEXT_SO(); A0 = cat8(ldstr(sl + tao), ldstr(sl + tao + 1024)); B0 = ldsr128(sl + fao); \
    A1 = cat8(ldstr(sl + tao + 4096), ldstr(sl + tao + 4096 + 1024)); B1 = ldsr128(sl + fao + 4096); SB(); } while (0)
#define MM2(ACC, A0, B0, A1, B1) do { ACC = MFMA32(A0, B0, ACC); ACC = MFMA32(A1, B1, ACC); SB(); } while (0)
    const unsigned pre = (1u << ODS) | (w == 7 ? 1u << 16 : 0u);
    const unsigned post = (w == 0 ? 1u << ODS : 0u) | (w == 7 ? (1u << ODS) | (1u << 10) | (1u << 12) | (1u << 14) : 0u);
    auto hook = [&](int s) {
      if (RARE((pre >> s) & 1)) { if (s == ODS) asm volatile("s_waitcnt vmcnt(0)" ::: "memory"); else peek_check(pk, rws, fo_off, token); }
      CBAR(6);
      if (RARE((post >> s) & 1)) {
        if (s == ODS) { if (w == 7) *(LAS unsigned*)pk = ~token; else if (lane == 0) flag_st(ws, FL_O + (qb * 32 + head) * 32, token); }
        else peek_issue(lds0 + L_P, ws, fo_off);
      }
      SB();
    };
    LBAR(); STAMP(6);
    RD2(pa0, pb0, pa1, pb1);
    if (kh) { pa0 = bf16x8{}; pa1 = bf16x8{}; pb0 = bf16x8{}; pb1 = bf16x8{}; }
#pragma unroll 1
    for (int s = 2; s < 22; s += 2) {
      CBAR(6); RD2(qa0, qb0, qa1, qb1); MM2(ay, pa0, pb0, pa1, pb1);
      hook(s); RD2(pa0, pb0, pa1, pb1); MM2(ay, qa0, qb0, qa1, qb1);
    }
    g = __uint_as_float(ld32(rws, WS_G + tok * 4));
    b2v = ((const unsigned short*)b2)[tok];
    CBAR(6); RD2(qa0, qb0, qa1, qb1); MM2(ay, pa0, pb0, pa1, pb1);
    STAMP(7);
    CBAR(6); RDO(pa0, pb0, pa1, pb1); MM2(ay, qa0, qb0, qa1, qb1);
#pragma unroll 1
    for (int s = 23; s < 29; s += 2) {
      CBAR(6); RDO(qa0, qb0, qa1, qb1); MM2(aa, pa0, pb0, pa1, pb1);
      CBAR(6); RDO(pa0, pb0, pa1, pb1); MM2(aa, qa0, qb0, qa1, qb1);
    }
    CBAR(6); RDO(qa0, qb0, qa1, qb1); MM2(aa, pa0, pb0, pa1, pb1);
    MM2(aa, qa0, qb0, qa1, qb1);
    STAMP(8);
    const float og = 1.0f - g;
    float v[16];
#pragma unroll
    for (int i = 0; i < 16; ++i) v[i] = ay[i] * og + aa[i] * g;
    LBAR();
    {
      lptr ex = L + w * 2048 + lane * 16;
      f32x4 s0, s1;
#pragma unroll
      for (int jj = 0; jj < 4; ++jj) { s0[jj] = kh ? v[jj] : v[8 + jj]; s1[jj] = kh ? v[4 + jj] : v[12 + jj]; }
      *(LAS f32x4*)(ex) = s0; *(LAS f32x4*)(ex + 1024) = s1;
    }
    LBAR();
    {
      lptr ex = L + (w ^ 4) * 2048 + lane * 16;
      const f32x4 r0 = *(const LAS f32x4*)(ex), r1 = *(const LAS f32x4*)(ex + 1024);
      const float bo = bflo(b2v) * og;
      float f[8];
#pragma unroll
      for (int jj = 0; jj < 4; ++jj) { f[jj] = (kh ? v[8 + jj] : v[jj]) + r0[jj] + bo; f[4 + jj] = (kh ? v[12 + jj] : v[4 + jj]) + r1[jj] + bo; }
      const unsigned a0 = pk2(f[0], f[1]), a1 = pk2(f[2], f[3]), c0 = pk2(f[4], f[5]), c1 = pk2(f[6], f[7]);
      auto x0 = __builtin_amdgcn_permlane32_swap(a0, c0, false, false);
      auto x1 = __builtin_amdgcn_permlane32_swap(a1, c1, false, false);
      const u32x4 ov = {x0[0], x1[0], x0[1], x1[1]};
      __builtin_amdgcn_raw_buffer_store_b128(ov, __builtin_amdgcn_make_buffer_rsrc(out, 0, 2048 * 1024, 0x00020000), tok * 1024 + (dt * 64 + mt * 32 + kh * 16 + h * 8) * 2, 0, 0);
    }
  }
  STAMP(9);
  if (w == 0) STAMP_OUT(ws, 0);
}

extern "C" void kernel_launch(void* const* d_in, const int* in_sizes, int n_in, void* d_out, int out_size, void* d_ws, size_t ws_size,
                              hipStream_t stream) {
  (void)hipFuncSetAttribute((const void*)fused_kernel, hipFuncAttributeMaxDynamicSharedMemorySize, LDS_BYTES);
  fused_kernel<<<256, 768, LDS_BYTES, stream>>>((const char*)d_in[0], (const char*)d_in[1], (const char*)d_in[2], (const char*)d_in[4],
                                                (const char*)d_in[5], (const char*)d_in[6], (const char*)d_in[7], (const char*)d_in[8],
                                                (char*)d_ws, (char*)d_out);
}
```

```cpp
#include <hip/hip_runtime.h>
#include <hip/hip_bf16.h>
#include <stdint.h>

#define LAS __attribute__((address_space(3)))
#define GAS __attribute__((address_space(1)))
#define DI __device__ __forceinline__
typedef __attribute__((ext_vector_type(8))) __bf16 bf16x8;
typedef __attribute__((ext_vector_type(2))) __bf16 bf16x2;
typedef __attribute__((ext_vector_type(4))) short s16x4;
typedef __attribute__((ext_vector_type(2))) float f32x2;
typedef __attribute__((ext_vector_type(4))) float f32x4;
typedef __attribute__((ext_vector_type(16))) float f32x16;
typedef __attribute__((ext_vector_type(2))) unsigned u32x2;
typedef __attribute__((ext_vector_type(4))) unsigned u32x4;
typedef LAS char* lptr;
typedef __amdgpu_buffer_rsrc_t rsrc_t;

constexpr unsigned WS_Q = 0, WS_K = 2u << 20, WS_V = 4u << 20, WS_O = 6u << 20, WS_U = 8u << 20, WS_H = 10u << 20,
                   WS_G = 13u << 20, WS_FL = 14u << 20, WS_END = 15u << 20;
constexpr int FL_H = 0;
constexpr int FL_Q = 64 * 32;
constexpr int FL_U = FL_Q + 192 * 32;
constexpr int FL_O = FL_U + 352 * 32;
constexpr int FL_EPOCH = FL_O + 256 * 32;
constexpr float QSCALE = 0.25f * 1.4426950408889634f;
constexpr int NS = 7, SLOT = 16384, RING = NS * SLOT;
constexpr int L_K = RING, L_V = RING + 16384, L_X = RING + 32768  , L_C = L_X + 12288  , L_P = L_C + 1024  ;
constexpr int LDS_BYTES = 163840;
constexpr unsigned SPIN_CAP = 200000;
constexpr int KVS = 22;
constexpr int QDS = 10;
constexpr int ODS = 4;

#define MFMA32(a, b, c) __builtin_amdgcn_mfma_f32_32x32x16_bf16(a, b, c, 0, 0, 0)
#define MFMA16(a, b, c) __builtin_amdgcn_mfma_f32_16x16x32_bf16(a, b, c, 0, 0, 0)
#define SB() __builtin_amdgcn_sched_barrier(0)
#define LBAR() do { asm volatile("s_waitcnt lgkmcnt(0)\n\ts_barrier" ::: "memory"); SB(); } while (0)
#define CBAR(N) do { asm volatile("s_waitcnt lgkmcnt(" #N ")\n\ts_barrier" ::: "memory"); SB(); } while (0)
#define DBAR() do { asm volatile("s_waitcnt vmcnt(0) lgkmcnt(0)\n\ts_barrier" ::: "memory"); SB(); } while (0)
#define VBAR(N) asm volatile("s_waitcnt vmcnt(" #N ")\n\ts_barrier" ::: "memory")
#define BAR() asm volatile("s_barrier" ::: "memory")

#define STAMP(i) do {} while (0)
#define STAMP_OUT(ws, slot) do {} while (0)

DI int rfl(int x) { return __builtin_amdgcn_readfirstlane(x); }
DI bf16x8 ldsr128(lptr p) { return *(const LAS bf16x8*)p; }
DI s16x4 ldstr(lptr p) { return __builtin_amdgcn_ds_read_tr16_b64_v4i16((LAS s16x4*)p); }
DI bf16x8 cat8(s16x4 a, s16x4 b) { return __builtin_bit_cast(bf16x8, __builtin_shufflevector(a, b, 0, 1, 2, 3, 4, 5, 6, 7)); }
DI unsigned pk2(float a, float b) { f32x2 f = {a, b}; return __builtin_bit_cast(unsigned, __builtin_convertvector(f, bf16x2)); }
DI float bflo(unsigned u) { return __uint_as_float(u << 16); }
DI float bfhi(unsigned u) { return __uint_as_float(u & 0xffff0000u); }
DI int crow(int i, int h) { return (i & 3) + 8 * (i >> 2) + 4 * h; }
DI void st128(rsrc_t r, unsigned off, u32x4 v) { __builtin_amdgcn_raw_buffer_store_b128(v, r, off, 0, 16); }
DI void st64(rsrc_t r, unsigned off, u32x2 v) { __builtin_amdgcn_raw_buffer_store_b64(v, r, off, 0, 16); }
DI void st32(rsrc_t r, unsigned off, unsigned v) { __builtin_amdgcn_raw_buffer_store_b32(v, r, off, 0, 16); }
DI u32x4 ld128(rsrc_t r, unsigned off) { return __builtin_amdgcn_raw_buffer_load_b128(r, off, 0, 16); }
DI unsigned ld32(rsrc_t r, unsigned off) { return __builtin_amdgcn_raw_buffer_load_b32(r, off, 0, 16); }
DI void flag_st(char* ws, int idx, unsigned v) { __hip_atomic_store((GAS unsigned*)(ws + WS_FL) + idx, v, __ATOMIC_RELAXED, __HIP_MEMORY_SCOPE_AGENT); }

#define M0_IN "s_mov_b32 %0, m0\n\ts_mov_b32 m0, %1\n\ts_nop 2\n\t"
#define M0_OUT "s_mov_b32 m0, %0"
DI void dma_2x2(unsigned m0v, const void* b0, unsigned v0, unsigned v1, const void* b1, unsigned v2, unsigned v3) {
  unsigned sv;
  asm volatile(M0_IN "global_load_lds_dwordx4 %4, %2\n\tglobal_load_lds_dwordx4 %5, %2 offset:1024\n\t"
               "global_load_lds_dwordx4 %6, %3 offset:2048\n\tglobal_load_lds_dwordx4 %7, %3 offset:3072\n\t" M0_OUT
               : "=&s"(sv) : "s"(m0v), "s"(b0), "s"(b1), "v"(v0), "v"(v1), "v"(v2), "v"(v3) : "memory");
}
DI void dma_lin4_sc1(unsigned m0v, const void* b, unsigned vl) {
  unsigned sv;
  asm volatile(M0_IN "global_load_lds_dwordx4 %3, %2 sc1\n\tglobal_load_lds_dwordx4 %3, %2 offset:1024 sc1\n\t"
               "global_load_lds_dwordx4 %3, %2 offset:2048 sc1\n\tglobal_load_lds_dwordx4 %3, %2 offset:3072 sc1\n\t" M0_OUT
               : "=&s"(sv) : "s"(m0v), "s"(b), "v"(vl) : "memory");
}
DI void dma_g1(unsigned m0v, const void* wb, unsigned v0, unsigned v1, const void* hb_m2048, unsigned vl) {
  unsigned sv;
  asm volatile(M0_IN "global_load_lds_dwordx4 %4, %2\n\tglobal_load_lds_dwordx4 %5, %2 offset:1024\n\t"
               "global_load_lds_dwordx4 %6, %3 offset:2048 sc1\n\t"
               "s_mov_b32 exec_hi, 0\n\tglobal_load_lds_dwordx4 %6, %3 offset:3072 sc1\n\ts_mov_b32 exec_hi, -1\n\t" M0_OUT
               : "=&s"(sv) : "s"(m0v), "s"(wb), "s"(hb_m2048), "v"(v0), "v"(v1), "v"(vl) : "memory");
}
DI void dma_hh(unsigned m0v, const void* hb_m2048, unsigned vl) {
  unsigned sv;
  asm volatile(M0_IN "global_load_lds_dwordx4 %3, %2 offset:2048 sc1\n\t"
               "s_mov_b32 exec_hi, 0\n\tglobal_load_lds_dwordx4 %3, %2 offset:3072 sc1\n\ts_mov_b32 exec_hi, -1\n\t" M0_OUT
               : "=&s"(sv) : "s"(m0v), "s"(hb_m2048), "v"(vl) : "memory");
}
DI void dma_touch(unsigned m0v, const void* b, unsigned voff) {
  unsigned sv;
  asm volatile(M0_IN "global_load_lds_dword %3, %2\n\t" M0_OUT : "=&s"(sv) : "s"(m0v), "s"(b), "v"(voff) : "memory");
}
DI void dma_wh(unsigned m0v, const void* wb, unsigned v0, unsigned v1) {
  unsigned sv;
  asm volatile(M0_IN "global_load_lds_dwordx4 %3, %2\n\tglobal_load_lds_dwordx4 %4, %2 offset:1024\n\t" M0_OUT
               : "=&s"(sv) : "s"(m0v), "s"(wb), "v"(v0), "v"(v1) : "memory");
}
DI void dma_fh(unsigned m0v, const void* fb_m2048, unsigned vl) {
  unsigned sv;
  asm volatile(M0_IN "global_load_lds_dwordx4 %3, %2 offset:2048 sc1\n\tglobal_load_lds_dwordx4 %3, %2 offset:3072 sc1\n\t" M0_OUT
               : "=&s"(sv) : "s"(m0v), "s"(fb_m2048), "v"(vl) : "memory");
}

DI void spin1(rsrc_t r, unsigned off, unsigned tok) {
  unsigned n = 0;
#pragma nounroll
  do { const unsigned v = ld32(r, off); if (__all(v == tok)) break; __builtin_amdgcn_s_sleep(4); asm volatile("" ::: "memory"); } while (++n < SPIN_CAP);
  __builtin_amdgcn_fence(__ATOMIC_ACQUIRE, "wavefront");
}
DI void peek_issue(unsigned m0v, const void* ws, unsigned off) {
  unsigned sv;
  asm volatile(M0_IN "global_load_lds_dwordx4 %3, %2 sc1\n\t" M0_OUT : "=&s"(sv) : "s"(m0v), "s"(ws), "v"(off) : "memory");
}
DI void peek_check(lptr p, rsrc_t r, unsigned off, unsigned tok) {
  if (__all(*(const volatile LAS unsigned*)p == tok)) return;
  asm volatile("s_waitcnt vmcnt(0)" ::: "memory");
  if (__all(*(const volatile LAS unsigned*)p == tok)) return;
  spin1(r, off, tok);
}
#define RARE(c) __builtin_expect((c) != 0, 0)

DI float row16_sum(float v) {
#define DPPX(ctrl) v += __int_as_float(__builtin_amdgcn_update_dpp(0, __float_as_int(v), ctrl, 0xf, 0xf, true))
  DPPX(0xB1); DPPX(0x4E); DPPX(0x141); DPPX(0x140);
#undef DPPX
  return v;
}
DI float gelu_tanh(float u) {
  const float z = u * (0.7978845608028654f + 0.7978845608028654f * 0.044715f * u * u);
  const float e = __builtin_amdgcn_exp2f(z * (-2.0f * 1.4426950408889634f));
  return u * __builtin_amdgcn_rcpf(1.0f + e);
}
constexpr unsigned long long diag_mask(int i) {
  unsigned long long mk = 0;
  for (int l = 0; l < 64; ++l) if ((i & 3) + 8 * (i >> 2) + 4 * (l >> 5) <= (l & 31)) mk |= 1ull << l;
  return mk;
}
DI float max16(const f32x16& s, float m) {
#pragma unroll
  for (int i = 0; i < 16; i += 2) m = __builtin_fmaxf(__builtin_fmaxf(s[i], s[i + 1]), m);
  return m;
}

#define NEXT_SO() so = so == (NS - 1) * SLOT ? 0 : so + SLOT

template <bool ISV, class Hook, class Hook14, class Mid>
DI void qkv_consume(lptr L, int& so, int w, int lane, f32x16 (&acc)[2], Hook hook, Hook14 hook14, Mid mid) {
  const int r = lane & 31, h = lane >> 5, wm = w >> 2, wn = w & 3;
  int xa[4];
#pragma unroll
  for (int s = 0; s < 4; ++s) xa[s] = (wm * 2 + (r >> 4)) * 4096 + (r & 15) * 128 + (((2 * s + h) ^ ((r >> 1) & 7)) << 4);
  const int q = (lane & 15) >> 2, p = lane & 3, blk = (lane >> 4) & 1;
  const int wa = h * 4096 + 2048 + q * 256 + (((wn * 4 + blk * 2 + (p >> 1)) ^ (q << 2)) << 4) + (p & 1) * 8;
  bf16x8 xA[2][4], xB[2][4], wA[4], wB[4];
#define QM(X, W, mi, s) do { if (ISV) acc[mi] = MFMA32(X[mi][s], W[s], acc[mi]); else acc[mi] = MFMA32(W[s], X[mi][s], acc[mi]); SB(); } while (0)
#define QRX(X, mi, s) X[mi][s] = ldsr128(sl + xa[s])
#define QRW(W, mi, e) W[2 * mi + e] = cat8(ldstr(sl + wa + e * 8192), ldstr(sl + wa + e * 8192 + 1024))
#define QSTEP(mi, XN, WN, XP, WP, MM) do { lptr sl = L + so; NEXT_SO(); \
    if (MM) QM(XP, WP, 0, 2 * mi); \
    QRX(XN, mi, 0); QRX(XN, mi, 1); SB(); if (MM) QM(XP, WP, 1, 2 * mi); \
    QRX(XN, mi, 2); QRX(XN, mi, 3); QRW(WN, mi, 0); SB(); if (MM) QM(XP, WP, 0, 2 * mi + 1); \
    QRW(WN, mi, 1); SB(); if (MM) QM(XP, WP, 1, 2 * mi + 1); } while (0)
  LBAR(); QSTEP(0, xA, wA, xB, wB, false); QSTEP(1, xA, wA, xB, wB, false);
#pragma unroll 1
  for (int c = 1; c < 7; c += 2) {
    hook(c);     QSTEP(0, xB, wB, xA, wA, true); QSTEP(1, xB, wB, xA, wA, true);
    hook(c + 1); QSTEP(0, xA, wA, xB, wB, true); QSTEP(1, xA, wA, xB, wB, true);
  }
  hook14(); QSTEP(0, xB, wB, xA, wA, true); QSTEP(1, xB, wB, xA, wA, true);
#pragma unroll
  for (int s = 0; s < 4; ++s) QM(xB, wB, 0, s);
  mid();
#pragma unroll
  for (int s = 0; s < 4; ++s) QM(xB, wB, 1, s);
}

__global__ __launch_bounds__(768, 3) void fused_kernel(const char* __restrict__ x, const char* __restrict__ wqkv, const char* __restrict__ wo,
                                                       const char* __restrict__ w1, const char* __restrict__ b1, const char* __restrict__ w2,
                                                       const char* __restrict__ b2, const char* __restrict__ gw, char* __restrict__ ws,
                                                       char* __restrict__ out) {
  extern __shared__ __attribute__((aligned(16))) char smem[];
  asm volatile("" :: "s"(x), "s"(wqkv), "s"(wo), "s"(w1), "s"(b1), "s"(w2), "s"(b2), "s"(gw), "s"(ws), "s"(out));
  STAMP(0);
  const int b = blockIdx.x, xcd = b & 7, idx = ((b >> 3) + 24) & 31;
  const bool lead = b == 65;
  const int lane = threadIdx.x & 63, w = rfl(threadIdx.x >> 6);
  const unsigned lds0 = (unsigned)(uintptr_t)smem;
  lptr L = (lptr)smem;
  const int tm = (xcd >> 1) * 4 + idx / 6, tn = (xcd & 1) * 6 + idx % 6;
  const int lnb = xcd * 8 + (idx - 24);
  const unsigned x0 = (lane >> 3) * 1024 + (((lane & 7) ^ (lane >> 4)) << 4), x1 = (8 + (lane >> 3)) * 1024 + (((lane & 7) ^ (4 + (lane >> 4))) << 4) - 1024;
  const unsigned q0 = (lane >> 4) * 3072 + (((lane & 15) ^ ((lane >> 4) << 2)) << 4), q1 = q0 + 4 * 3072 - 1024;
  const char* xp = x + (size_t)(tm * 128 + ((w >> 1) & 1) * 64 + (w & 1) * 16) * 1024;
  const char* qp = wqkv + (size_t)((w & 3) * 8) * 3072 + tn * 256 - 2048;
  int kc = ((idx / 6 + idx % 6) % 3) * 3;
  if (w >= 8) {
    if (idx < 24) {
#pragma unroll 1
      for (int f = 0; f < 7; ++f) {
        dma_2x2(lds0 + (w & 3) * 4096 + f * SLOT, xp + kc * 128 + (f & 1) * 32 * 1024, x0, x1, qp + (size_t)(kc * 2 + (f & 1)) * (32 * 3072), q0, q1);
        kc = (kc + (f & 1)) & 7;
      }
    }
  }
  asm volatile("" ::: "memory");
  SB();
  const int gi = 31 - idx;
  const bool hasQ = idx < 24, hasG1 = xcd == 0 || gi < 30;
  const bool lowd = xcd == 0 ? gi < 12 : xcd < 5;
  const int gj = xcd == 0 && !lowd ? gi - 12 : gi;
  const int g1ft = lowd ? (xcd == 0 ? 20 : (xcd - 1) * 5) + gj / 6 : (xcd == 0 ? 18 : (xcd - 5) * 6) + gj / 5;
  const int g1dt = lowd ? gj % 6 : 6 + gj % 5;
  const int head = xcd * 4 + (idx & 3), qb = idx >> 2;
  const int st = (xcd >> 1) * 8 + (idx >> 2), dt = (xcd & 1) * 4 + (idx & 3);
  unsigned fq_off;
  { const int l = lane < 12 ? lane : 3, wh = l >> 2; int tmi = l & 3; if (wh == 0 || qb == 0) tmi |= 2;
    fq_off = WS_FL + (FL_Q + ((wh * 4 + (head >> 3)) * 16 + 2 * qb - 2 + tmi) * 32) * 4; }
  const unsigned fu_off = WS_FL + (FL_U + (((lane & 32) ? (4 * dt + 3) / 3 : (4 * dt) / 3) * 32 + min(lane & 31, 21)) * 32) * 4;
  const unsigned fo_off = WS_FL + (FL_O + ((st >> 2) * 32 + (lane & 31)) * 32) * 4;
  const unsigned fh_off = WS_FL + FL_H * 4 + lane * 128;
  const rsrc_t rws = __builtin_amdgcn_make_buffer_rsrc(ws, 0, WS_END, 0x00020000);
  volatile LAS unsigned* relay = (volatile LAS unsigned*)(L + L_C + 64);
  unsigned eoff = WS_FL + FL_EPOCH * 4;
  asm volatile("" : "+v"(eoff));
  const unsigned epoch = ld32(rws, eoff);
#define TOKEN_OF(e) ((e) * 1664525u + 1013904223u)

  if (w >= 8) {
    const int j = w - 8;
#define token TOKEN_OF(epoch)
    const unsigned m0b = lds0 + j * 4096;
    const unsigned vl = lane * 16;
    unsigned so = 0;
    const char* hp = ws + WS_H + (size_t)g1dt * 64 * 3072 + j * 1536 - 2048;
    const char* w1p = w1 + (size_t)(j * 16) * 2752 + g1ft * 128;
    const unsigned u0 = (lane >> 3) * 2752 + ((((lane & 7) ^ (((lane >> 4) & 3) << 1)) & (g1ft == 21 ? 3 : 7)) << 4), u1 = u0 + 8 * 2752 - 1024;
    int cw = lowd ? g1dt * 6 : (g1dt - 6) * 6 + 3, ch = cw;
    auto fill_g1 = [&]() { dma_g1(m0b + so, w1p + (size_t)cw * (64 * 2752), u0, u1, hp + cw * 6144, vl); cw = (cw + 1) & 31; NEXT_SO(); };
    auto fill_g1w = [&]() { dma_wh(m0b + so, w1p + (size_t)cw * (64 * 2752), u0, u1); cw = (cw + 1) & 31; NEXT_SO(); };
    auto fill_g1h3 = [&](unsigned so0) {
      const unsigned keep = so; so = so0;
#pragma unroll 1
      for (int i = 0; i < 3; ++i) { dma_hh(m0b + so, hp + ch * 6144, vl); ch = (ch + 1) & 31; NEXT_SO(); }
      so = keep;
    };
    const unsigned swz = ((lane & 7) ^ (((lane >> 4) & 1) << 2)) << 4;
    unsigned vw0 = (lane >> 3) * 4096 + swz, vw1 = vw0 + 8 * 4096 - 1024;
    const char* w2p = w2 + (size_t)(j * 16) * 4096 + st * 128;
    const char* up = ws + WS_U + (size_t)dt * 86 * 2048 + j * 2048 - 2048;
    const size_t w2half = (size_t)(j < 2 ? 21 * 64 : 20 * 64) * 4096;
    const int uhalf = j < 2 ? 21 * 8192 : 20 * 8192;
    auto fill_kv = [&]() {
      int t0 = qb * 8 - 8 + 4 * j; t0 = t0 < 0 ? t0 + 8 : t0;
      dma_lin4_sc1(lds0 + L_K + j * 4096, ws + WS_K + (size_t)(head * 64 + t0) * 1024, vl);
      dma_lin4_sc1(lds0 + L_V + j * 4096, ws + WS_V + (size_t)(head * 64 + t0) * 1024, vl);
    };
    int nwh = 0;
    const int r2 = (((dt & 3) * 2 + (dt >> 2)) * 21) >> 3;
    int c2 = r2;
    auto fill_wh = [&]() {
      dma_wh(m0b + so, nwh == 0 ? w2p + w2half : w2p + (size_t)c2 * (64 * 4096), vw0, vw1);
      if (nwh) c2 = c2 == 20 ? 0 : c2 + 1;
      ++nwh; NEXT_SO();
    };
    unsigned so_g2 = 0;
    auto warm_g2 = [&]() {
      if (j == 1) dma_touch(lds0 + L_P + 1024, wo + (size_t)(idx * 16) * 1024 + (xcd & 1) * 512, (lane >> 2) * 1024 + (lane & 3) * 128);
    };

    if (hasQ) {
      auto fill_a = [&]() { dma_2x2(m0b + so, xp + kc * 128, x0, x1, qp + (size_t)kc * 64 * 3072, q0, q1); NEXT_SO(); };
      auto fill_b = [&]() { dma_2x2(m0b + so, xp + kc * 128 + 32 * 1024, x0, x1, qp + (size_t)kc * 64 * 3072 + 32 * 3072, q0, q1); kc = (kc + 1) & 7; NEXT_SO(); };
      VBAR(20);
#pragma unroll 1
      for (int c = 1; c < 5; ++c) { VBAR(12); fill_b(); fill_a(); }
      VBAR(12); fill_b();
      if (hasG1) {
        const unsigned so0 = so;
        fill_g1w();
        VBAR(10); fill_g1w(); fill_g1w();
        VBAR(6); fill_g1h3(so0); fill_g1(); fill_g1();
      } else {
        VBAR(8); VBAR(0);
      }
    } else {
      BAR();
      VBAR(0);
#pragma unroll 1
      for (int s = 0; s < 3; ++s) fill_g1w();
      if (j == 0) spin1(rws, fh_off, token);
      BAR();
      fill_g1h3(0); fill_g1(); fill_g1();
    }
    STAMP(1);
    if (hasG1) {
      VBAR(12); fill_g1(); VBAR(14); fill_g1();
#pragma unroll 1
      for (int s = 2; s <= KVS; ++s) { VBAR(16); fill_g1(); }
      fill_kv();
#pragma unroll 1
      for (int s = KVS + 1; s < 27; ++s) { VBAR(16); fill_g1(); }
      so_g2 = so;
      VBAR(16); fill_wh(); VBAR(14); fill_wh(); VBAR(12); fill_wh(); VBAR(10); fill_wh(); VBAR(8); fill_wh();
      BAR(); fill_wh(); fill_wh();
      warm_g2();
      BAR();
      BAR();
    } else {
      BAR();
      so_g2 = so;
#pragma unroll 1
      for (int i = 0; i < 7; ++i) fill_wh();
      warm_g2();
      if (j == 0) spin1(rws, fq_off, token);
      BAR();
      fill_kv();
      VBAR(0);
    }
    STAMP(2);
    {
      if (j == 0) { spin1(rws, fu_off, token); *relay = 1u; }
      else { for (unsigned spins = 0; spins < 64 * SPIN_CAP && *relay == 0u; ++spins) __builtin_amdgcn_s_sleep(1); }
      STAMP(3);
      so = so_g2;
      dma_fh(m0b + so, up + uhalf, vl); NEXT_SO();
#pragma unroll 1
      for (int i = 0, c = r2; i < 6; ++i) { dma_fh(m0b + so, up + c * 8192, vl); c = c == 20 ? 0 : c + 1; NEXT_SO(); }
    }
    unsigned wsh = 18;
    int ci = 7;
    auto fill_g2 = [&]() {
      if (ci == 22) {
        w2p = wo + (size_t)(j * 16) * 1024 + dt * 128; up = ws + WS_O + (size_t)st * 32 * 2048 + j * 2048 - 2048; wsh = 16; c2 = 0;
        vw0 = (lane >> 3) * 1024 + swz; vw1 = vw0 + 8 * 1024 - 1024;
      }
      dma_wh(m0b + so, w2p + ((size_t)c2 << wsh), vw0, vw1); dma_fh(m0b + so, up + c2 * 8192, vl);
      c2 = c2 == 20 ? 0 : c2 + 1; NEXT_SO(); ++ci;
    };
    VBAR(12); VBAR(10);
    VBAR(8); fill_g2(); VBAR(10); fill_g2(); VBAR(12); fill_g2(); VBAR(14); fill_g2();
#pragma unroll 1
    for (int s = 6; s < 25; ++s) { VBAR(16); fill_g2(); }
    const unsigned fa_off = WS_FL + FL_O * 4 + (j * 64 + lane) * 128;
    if (lead) peek_issue(lds0 + L_X + j * 1024, ws, fa_off);
    VBAR(16); VBAR(12); VBAR(8); VBAR(4); VBAR(0);
    if (lead) peek_check(L + L_X + j * 1024 + lane * 16, rws, fa_off, token);
    BAR();
    if (lead && j == 0 && lane == 0) st32(rws, WS_FL + FL_EPOCH * 4, token);
    BAR();
    STAMP(4);
    if (j == 0) STAMP_OUT(ws, 1);
    return;
  }

#undef token
  unsigned token;
  u32x4 xv[4], gv[4];
  if (!hasQ) {
#pragma unroll
    for (int i = 0; i < 4; ++i) xv[i] = *(const u32x4*)(x + (size_t)(lnb * 32 + w * 4 + (lane >> 4)) * 1024 + (lane & 15) * 16 + i * 256);
#pragma unroll
    for (int i = 0; i < 4; ++i) gv[i] = *(const u32x4*)(gw + (lane & 15) * 16 + i * 256);
  }
  asm volatile("" ::: "memory");
  SB();
  const int r = lane & 31, h = lane >> 5;
  const int g4 = lane >> 4, q4 = (lane & 15) >> 2, p4 = lane & 3, blk = (lane >> 4) & 1;
  int so = 0;
  if (threadIdx.x == 4) *relay = 0u;
  if (threadIdx.x < 4) {
    const unsigned cv = (threadIdx.x & 1) ? 0u : 0x3f803f80u;
    *(LAS u32x4*)(L + L_C + (threadIdx.x >> 1) * 512 + (threadIdx.x & 1) * 16) = u32x4{cv, cv, cv, cv};
  }
  lptr pk = L + L_P + lane * 16;

  const int kh = w >> 2, fb = w & 3;
  int ha[3];
#pragma unroll
  for (int db = 0; db < 3; ++db) { const int o = (kh * 3 + db) * 1024 + lane * 16, rg = o / 1536; ha[db] = rg * 4096 + 2048 + (o - rg * 1536); }
  const int wa = kh * 8192 + (4 * g4 + q4) * 128 + ((fb ^ ((2 * g4 + (q4 >> 1)) & 3)) << 5) + p4 * 8;
  f32x4 acc[3] = {};
  bf16x8 wA = {}, hA[3] = {}, wB = {}, hB[3] = {};
#define G1RD(W, H) do { lptr sl = L + so; NEXT_SO(); W = cat8(ldstr(sl + wa), ldstr(sl + wa + 4096)); \
  _Pragma("unroll") for (int db = 0; db < 3; ++db) H[db] = ldsr128(sl + ha[db]); SB(); } while (0)
#define G1MM(W, H) do { _Pragma("unroll") for (int db = 0; db < 3; ++db) acc[db] = MFMA16(W, H[db], acc[db]); SB(); } while (0)

  token = TOKEN_OF(epoch);
  asm volatile("" : "+v"(token), "+v"(gv[3]));
  if (hasQ) {
    if (w == 7) *(LAS unsigned*)pk = ~token;
    STAMP(1);
    f32x16 qacc[2] = {};
    const bool poller = w == 7 && hasG1;
    const unsigned post = poller ? (1u << 4) | (1u << 5) | (1u << 6) : 0u;
    auto hook = [&](int s) {
      LBAR();
      if (RARE((post >> s) & 1)) peek_issue(lds0 + L_P, ws, fh_off);
      SB();
    };
    auto hook14 = [&]() { if (poller) peek_check(pk, rws, fh_off, token); LBAR(); };
    auto mid = [&]() { if (hasG1) { LBAR(); G1RD(wA, hA); } };
    if (tn >= 8) qkv_consume<true>(L, so, w, lane, qacc, hook, hook14, mid);
    else qkv_consume<false>(L, so, w, lane, qacc, hook, hook14, mid);
    STAMP(2);
    const int wm = w >> 2, wn = w & 3;
    const int hd0 = (tn & 3) * 8 + wn * 2;
    const float sc = tn < 4 ? QSCALE : 1.0f;
    const unsigned dst = tn >= 8 ? WS_V + (((hd0 + (r >> 4)) * 64 + tm * 4 + wm * 2) * 64 + h * 16 + (r & 15)) * 16
                                 : (tn < 4 ? WS_Q : WS_K) + ((hd0 * 64 + tm * 4 + wm * 2) * 64 + lane) * 16;
    const unsigned dstep = tn >= 8 ? 512 : 65536;
#pragma unroll
    for (int mi = 0; mi < 2; ++mi) {
#pragma unroll
      for (int s = 0; s < 2; ++s) {
        u32x4 v;
#pragma unroll
        for (int jj = 0; jj < 4; ++jj) v[jj] = pk2(qacc[mi][8 * s + 2 * jj] * sc, qacc[mi][8 * s + 2 * jj + 1] * sc);
        st128(rws, dst + mi * 1024 + s * dstep, v);
      }
      if (mi == 0 && hasG1) { SB(); LBAR(); G1RD(wB, hB); G1MM(wA, hA); }
    }
  } else {
    constexpr int RS = 1056;
    f32x2 v[16], s2 = {0.f, 0.f}, g2 = {0.f, 0.f};
#pragma unroll
    for (int i = 0; i < 16; ++i) {
      v[i] = f32x2{bflo(xv[i >> 2][i & 3]), bfhi(xv[i >> 2][i & 3])};
      s2 += v[i];
      g2 += v[i] * f32x2{bflo(gv[i >> 2][i & 3]), bfhi(gv[i >> 2][i & 3])};
    }
    const float mean = row16_sum(s2[0] + s2[1]) * (1.0f / 512), gs = row16_sum(g2[0] + g2[1]);
    f32x2 q2 = {0.f, 0.f};
#pragma unroll
    for (int i = 0; i < 16; ++i) { v[i] -= f32x2{mean, mean}; q2 += v[i] * v[i]; }
    const float rs = __builtin_amdgcn_rsqf(row16_sum(q2[0] + q2[1]) * (1.0f / 512) + 1e-5f);
#pragma unroll
    for (int i = 0; i < 4; ++i) {
      u32x4 ov;
#pragma unroll
      for (int jj = 0; jj < 4; ++jj) { const f32x2 t = v[4 * i + jj] * f32x2{rs, rs}; ov[jj] = pk2(t[0], t[1]); }
      *(LAS u32x4*)(L + (w * 4 + g4) * RS + (lane & 15) * 16 + i * 256) = ov;
    }
    if ((lane & 15) == 0) st32(rws, WS_G + (lnb * 32 + w * 4 + g4) * 4, __float_as_uint(1.0f / (1.0f + __expf(-gs))));
    STAMP(1);
    LBAR();
#pragma unroll
    for (int i = 0; i < 4; ++i) {
      const int D = w * 4 + i, d48 = D / 3;
      lptr a = L + (4 * g4 + q4) * RS + D * 32 + p4 * 8;
      const bf16x8 f = cat8(ldstr(a), ldstr(a + 16 * RS));
      st128(rws, WS_H + ((d48 * 64 + lnb) * 3 + (D - d48 * 3)) * 1024 + lane * 16, __builtin_bit_cast(u32x4, f));
      if (i == 3 && w == 7) st128(rws, WS_H + ((10 * 64 + lnb) * 3 + 2) * 1024 + lane * 16, __builtin_bit_cast(u32x4, f));
    }
    DBAR();
    if (threadIdx.x == 0) flag_st(ws, FL_H + lnb * 32, token);
    STAMP(2);
    LBAR();
    LBAR(); G1RD(wA, hA);
    LBAR(); G1RD(wB, hB); G1MM(wA, hA);
  }

  bf16x8 qf;
  unsigned pmk[8];
#pragma unroll
  for (int k = 0; k < 8; ++k) { const int dlt = r - crow(2 * k, h); pmk[k] = dlt >= 1 ? 0xffffffffu : (dlt == 0 ? 0x0000ffffu : 0u); }
  int vstr = r < 16 ? 1024 : 0;
  int vp0o = r < 16 ? L_V + w * 1024 + h * 256 + r * 16 : (L_C + ((r == 16 || r == 20) ? 0 : 16));
#define PIN_ATT() asm volatile("" : "+v"(pmk[0]), "+v"(pmk[1]), "+v"(pmk[2]), "+v"(pmk[3]), "+v"(pmk[4]), "+v"(pmk[5]), "+v"(pmk[6]), "+v"(pmk[7]), "+v"(vstr), "+v"(vp0o))
  if (hasG1) {
    const unsigned pre = (1u << QDS) | (w == 7 ? 1u << KVS : 0u);
    const unsigned post = (w == 0 && hasQ ? 1u << QDS : 0u) | (w == 7 ? (1u << QDS) | (1u << (KVS - 6)) | (1u << (KVS - 4)) | (1u << (KVS - 2)) : 0u);
    auto hook = [&](int s) {
      if (RARE((pre >> s) & 1)) { if (s == QDS) asm volatile("s_waitcnt vmcnt(0)" ::: "memory"); else peek_check(pk, rws, fq_off, token); }
      CBAR(5);
      if (RARE((post >> s) & 1)) {
        if (s == QDS) { if (w == 7) *(LAS unsigned*)pk = ~token; else if (lane == 0) flag_st(ws, FL_Q + (tn * 16 + tm) * 32, token); }
        else peek_issue(lds0 + L_P, ws, fq_off);
      }
      SB();
    };
#pragma unroll 1
    for (int s = 2; s < KVS + 2; s += 2) {
      hook(s); G1RD(wA, hA); G1MM(wB, hB);
      CBAR(5); G1RD(wB, hB); G1MM(wA, hA);
    }
    STAMP(10);
    qf = __builtin_bit_cast(bf16x8, ld128(rws, WS_Q + (head * 64 + qb * 8 + w) * 1024 + lane * 16));
    const int fblk = g1ft * 4 + fb;
    u32x2 bb = *(const u32x2*)(b1 + min(fblk * 16 + 4 * g4, 1372) * 2);
    unsigned ua[2];
#pragma unroll
    for (int e = 0; e < 2; ++e) {
      const int D = g1dt * 3 + (kh ? 2 : e);
      ua[e] = WS_U + ((((D >> 2) * 86 + fblk) * 2 + ((D >> 1) & 1)) * 64 + (g4 >> 1) * 32 + (D & 1) * 16 + (lane & 15)) * 16 + (g4 & 1) * 8;
      if (fblk >= 86 || D >= 32) ua[e] = WS_END;
    }
    lptr xw = L + L_X + (fb * 3 * 64 + lane) * 16;
    PIN_ATT();
#pragma unroll 1
    for (int s = KVS + 2; s < 32; s += 2) {
      CBAR(5); G1RD(wA, hA); G1MM(wB, hB);
      CBAR(5); G1RD(wB, hB); G1MM(wA, hA);
    }
    asm volatile("" : "+v"(qf), "+v"(bb), "+v"(ua[0]), "+v"(ua[1]));
    const f32x4 bv = {bflo(bb[0]), bfhi(bb[0]), bflo(bb[1]), bfhi(bb[1])};
    LBAR();
    G1MM(wB, hB);
    STAMP(3);
    if (kh == 1) { *(LAS f32x4*)(xw) = acc[0]; *(LAS f32x4*)(xw + 1024) = acc[1]; }
    else *(LAS f32x4*)(xw + 2048) = acc[2];
    LBAR();
    auto fin = [&](const f32x4& a, int off, unsigned addr) {
      const f32x4 t = a + *(const LAS f32x4*)(xw + off) + bv;
      st64(rws, addr, u32x2{pk2(gelu_tanh(t[0]), gelu_tanh(t[1])), pk2(gelu_tanh(t[2]), gelu_tanh(t[3]))});
    };
    if (kh == 1) fin(acc[2], 2048, ua[0]);
    else { fin(acc[0], 0, ua[0]); fin(acc[1], 1024, ua[1]); }
  } else {
    DBAR();
    if (threadIdx.x == 0) flag_st(ws, FL_Q + (tn * 16 + tm) * 32, token);
    LBAR();
    qf = __builtin_bit_cast(bf16x8, ld128(rws, WS_Q + (head * 64 + qb * 8 + w) * 1024 + lane * 16));
    asm volatile("" : "+v"(qf));
    PIN_ATT();
    LBAR();
    STAMP(3);
  }

  {
    lptr kp = L + L_K + w * 1024 + lane * 16;
    const int i_lo = qb > 0 ? 1 : 8 - w;
    const f32x16 zero = {};
    bf16x8 kf[9];
#pragma unroll
    for (int i = 0; i < 9; ++i) kf[i] = ldsr128(kp + i * 1024);
    f32x16 sb, sa, sc;
    float mx[4];
#define TMAX(S, i) (((i) >= i_lo) ? max16(S, -INFINITY) : -INFINITY)
    sa = MFMA32(kf[8], qf, zero); sc = MFMA32(kf[0], qf, zero);
    f32x16 s1 = MFMA32(kf[1], qf, zero), s2 = MFMA32(kf[2], qf, zero); SB();
#pragma unroll
    for (int i = 0; i < 16; ++i) sb[i] = __builtin_amdgcn_inverse_ballot_w64(diag_mask(i)) ? sa[i] : (qb > 0 ? sc[i] : -INFINITY);
    SB();
    sa = MFMA32(kf[3], qf, zero); sc = MFMA32(kf[4], qf, zero); SB();
    mx[0] = max16(sb, -INFINITY); mx[1] = TMAX(s1, 1); mx[2] = TMAX(s2, 2); SB();
    s1 = MFMA32(kf[5], qf, zero); s2 = MFMA32(kf[6], qf, zero); SB();
    mx[3] = TMAX(sa, 3); mx[0] = __builtin_fmaxf(mx[0], TMAX(sc, 4)); SB();
    sa = MFMA32(kf[7], qf, zero); SB();
    mx[1] = __builtin_fmaxf(mx[1], TMAX(s1, 5)); mx[2] = __builtin_fmaxf(mx[2], TMAX(s2, 6)); mx[3] = __builtin_fmaxf(mx[3], TMAX(sa, 7));
    float m = __builtin_fmaxf(__builtin_fmaxf(mx[0], mx[1]), __builtin_fmaxf(mx[2], mx[3]));
    {
      auto rr = __builtin_amdgcn_permlane32_swap(__float_as_uint(m), __float_as_uint(m), false, false);
      m = __builtin_fmaxf(__uint_as_float(rr[0]), __uint_as_float(rr[1]));
    }
    STAMP(11);
    if (hasG1) {
      DBAR();
      if (threadIdx.x == 0) flag_st(ws, FL_U + (g1dt * 32 + g1ft) * 32, token);
    }
    STAMP(4);
    lptr vp0 = L + vp0o;
    f32x16 cin;
#pragma unroll
    for (int i = 0; i < 16; ++i) cin[i] = -m;
    f32x16 o = {};
    sa = MFMA32(kf[1], qf, cin);
    {
      lptr vp = vp0 + 8 * vstr;
      const bf16x8 vd0 = ldsr128(vp), vd1 = ldsr128(vp + 512), vf0 = ldsr128(vp0), vf1 = ldsr128(vp0 + 512);
      SB();
      u32x4 pd[2], pf[2];
#pragma unroll
      for (int i = 0; i < 16; i += 2) {
        const unsigned pp = pk2(__builtin_amdgcn_exp2f(sb[i] - m), __builtin_amdgcn_exp2f(sb[i + 1] - m));
        pd[i >> 3][(i >> 1) & 3] = pp & pmk[i >> 1];
        pf[i >> 3][(i >> 1) & 3] = pp & ~pmk[i >> 1];
      }
      o = MFMA32(vd0, __builtin_bit_cast(bf16x8, pd[0]), o);
      o = MFMA32(vd1, __builtin_bit_cast(bf16x8, pd[1]), o);
      o = MFMA32(vf0, __builtin_bit_cast(bf16x8, pf[0]), o);
      o = MFMA32(vf1, __builtin_bit_cast(bf16x8, pf[1]), o);
    }
#define PTILE(i, SC, SN) do { SB(); \
      if (i < 7) SN = MFMA32(kf[i + 1], qf, cin); \
      if (i >= i_lo) { \
        lptr vp = vp0 + i * vstr; \
        const bf16x8 v0 = ldsr128(vp), v1 = ldsr128(vp + 512); SB(); \
        u32x4 p0, p1; \
        _Pragma("unroll") for (int e = 0; e < 8; e += 2) p0[e >> 1] = pk2(__builtin_amdgcn_exp2f(SC[e]), __builtin_amdgcn_exp2f(SC[e + 1])); \
        o = MFMA32(v0, __builtin_bit_cast(bf16x8, p0), o); \
        _Pragma("unroll") for (int e = 8; e < 16; e += 2) p1[(e >> 1) & 3] = pk2(__builtin_amdgcn_exp2f(SC[e]), __builtin_amdgcn_exp2f(SC[e + 1])); \
        o = MFMA32(v1, __builtin_bit_cast(bf16x8, p1), o); \
      } } while (0)
    PTILE(1, sa, sc); PTILE(2, sc, sa); PTILE(3, sa, sc); PTILE(4, sc, sa); PTILE(5, sa, sc); PTILE(6, sc, sa); PTILE(7, sa, sc);
    const float il = __builtin_amdgcn_rcpf(o[8]);
    u32x4 ov;
#pragma unroll
    for (int jj = 0; jj < 4; ++jj) ov[jj] = pk2(o[2 * jj] * il, o[2 * jj + 1] * il);
    const int B = qb * 8 + w;
    st128(rws, WS_O + (((B >> 1) * 32 + head) * 2 + (B & 1)) * 1024 + lane * 16, ov);
  }
  STAMP(5);

  {
    const int mt = (w >> 1) & 1, nt = w & 1;
    const int tok = st * 64 + nt * 32 + r;
    const int ta2 = kh * 8192 + (8 * h + q4) * 128 + (((nt * 4 + blk * 2 + (p4 >> 1)) ^ ((q4 >> 1) << 2)) << 4) + (p4 & 1) * 8;
    const int tao = kh * 8192 + (4 * h + q4) * 128 + (((mt * 4 + blk * 2 + (p4 >> 1)) ^ ((q4 >> 1) << 2)) << 4) + (p4 & 1) * 8;
    const int fa2 = kh * 8192 + 2048 + mt * 1024 + lane * 16;
    const int fao = kh * 8192 + 2048 + nt * 1024 + lane * 16;
    f32x16 ay = {}, aa = {};
    bf16x8 pa0, pb0, pa1, pb1, qa0, qb0, qa1, qb1;
    float g = 0.f; unsigned b2v = 0;
#define RD2(A0, B0, A1, B1) do { lptr sl = L + so; NEXT_SO(); B0 = cat8(ldstr(sl + ta2), ldstr(sl + ta2 + 512)); A0 = ldsr128(sl + fa2); \
    B1 = cat8(ldstr(sl + ta2 + 4096), ldstr(sl + ta2 + 4096 + 512)); A1 = ldsr128(sl + fa2 + 4096); SB(); } while (0)
#define RDO(A0, B0, A1, B1) do { lptr sl = L + so; NEXT_SO(); A0 = cat8(ldstr(sl + tao), ldstr(sl + tao + 1024)); B0 = ldsr128(sl + fao); \
    A1 = cat8(ldstr(sl + tao + 4096), ldstr(sl + tao + 4096 + 1024)); B1 = ldsr128(sl + fao + 4096); SB(); } while (0)
#define MM2(ACC, A0, B0, A1, B1) do { ACC = MFMA32(A0, B0, ACC); ACC = MFMA32(A1, B1, ACC); SB(); } while (0)
    const unsigned pre = (1u << ODS) | (w == 7 ? 1u << 16 : 0u);
    const unsigned post = (w == 0 ? 1u << ODS : 0u) | (w == 7 ? (1u << ODS) | (1u << 10) | (1u << 12) | (1u << 14) : 0u);
    auto hook = [&](int s) {
      if (RARE((pre >> s) & 1)) { if (s == ODS) asm volatile("s_waitcnt vmcnt(0)" ::: "memory"); else peek_check(pk, rws, fo_off, token); }
      CBAR(6);
      if (RARE((post >> s) & 1)) {
        if (s == ODS) { if (w == 7) *(LAS unsigned*)pk = ~token; else if (lane == 0) flag_st(ws, FL_O + (qb * 32 + head) * 32, token); }
        else peek_issue(lds0 + L_P, ws, fo_off);
      }
      SB();
    };
    LBAR(); STAMP(6);
    RD2(pa0, pb0, pa1, pb1);
    if (kh) { pa0 = bf16x8{}; pa1 = bf16x8{}; pb0 = bf16x8{}; pb1 = bf16x8{}; }
#pragma unroll 1
    for (int s = 2; s < 22; s += 2) {
      CBAR(6); RD2(qa0, qb0, qa1, qb1); MM2(ay, pa0, pb0, pa1, pb1);
      hook(s); RD2(pa0, pb0, pa1, pb1); MM2(ay, qa0, qb0, qa1, qb1);
    }
    g = __uint_as_float(ld32(rws, WS_G + tok * 4));
    b2v = ((const unsigned short*)b2)[tok];
    CBAR(6); RD2(qa0, qb0, qa1, qb1); MM2(ay, pa0, pb0, pa1, pb1);
    STAMP(7);
    CBAR(6); RDO(pa0, pb0, pa1, pb1); MM2(ay, qa0, qb0, qa1, qb1);
#pragma unroll 1
    for (int s = 23; s < 29; s += 2) {
      CBAR(6); RDO(qa0, qb0, qa1, qb1); MM2(aa, pa0, pb0, pa1, pb1);
      CBAR(6); RDO(pa0, pb0, pa1, pb1); MM2(aa, qa0, qb0, qa1, qb1);
    }
    CBAR(6); RDO(qa0, qb0, qa1, qb1); MM2(aa, pa0, pb0, pa1, pb1);
    MM2(aa, qa0, qb0, qa1, qb1);
    STAMP(8);
    const float og = 1.0f - g;
    float v[16];
#pragma unroll
    for (int i = 0; i < 16; ++i) v[i] = ay[i] * og + aa[i] * g;
    LBAR();
    {
      lptr ex = L + w * 2048 + lane * 16;
      f32x4 s0, s1;
#pragma unroll
      for (int jj = 0; jj < 4; ++jj) { s0[jj] = kh ? v[jj] : v[8 + jj]; s1[jj] = kh ? v[4 + jj] : v[12 + jj]; }
      *(LAS f32x4*)(ex) = s0; *(LAS f32x4*)(ex + 1024) = s1;
    }
    LBAR();
    {
      lptr ex = L + (w ^ 4) * 2048 + lane * 16;
      const f32x4 r0 = *(const LAS f32x4*)(ex), r1 = *(const LAS f32x4*)(ex + 1024);
      const float bo = bflo(b2v) * og;
      float f[8];
#pragma unroll
      for (int jj = 0; jj < 4; ++jj) { f[jj] = (kh ? v[8 + jj] : v[jj]) + r0[jj] + bo; f[4 + jj] = (kh ? v[12 + jj] : v[4 + jj]) + r1[jj] + bo; }
      const unsigned a0 = pk2(f[0], f[1]), a1 = pk2(f[2], f[3]), c0 = pk2(f[4], f[5]), c1 = pk2(f[6], f[7]);
      auto x0 = __builtin_amdgcn_permlane32_swap(a0, c0, false, false);
      auto x1 = __builtin_amdgcn_permlane32_swap(a1, c1, false, false);
      const u32x4 ov = {x0[0], x1[0], x0[1], x1[1]};
      __builtin_amdgcn_raw_buffer_store_b128(ov, __builtin_amdgcn_make_buffer_rsrc(out, 0, 2048 * 1024, 0x00020000), tok * 1024 + (dt * 64 + mt * 32 + kh * 16 + h * 8) * 2, 0, 2);
    }
  }
  STAMP(9);
  if (w == 0) STAMP_OUT(ws, 0);
}

extern "C" void kernel_launch(void* const* d_in, const int* in_sizes, int n_in, void* d_out, int out_size, void* d_ws, size_t ws_size,
                              hipStream_t stream) {
  (void)hipFuncSetAttribute((const void*)fused_kernel, hipFuncAttributeMaxDynamicSharedMemorySize, LDS_BYTES);
  fused_kernel<<<256, 768, LDS_BYTES, stream>>>((const char*)d_in[0], (const char*)d_in[1], (const char*)d_in[2], (const char*)d_in[4],
                                                (const char*)d_in[5], (const char*)d_in[6], (const char*)d_in[7], (const char*)d_in[8],
                                                (char*)d_ws, (char*)d_out);
}
```

```cpp
#include <hip/hip_runtime.h>
#include <hip/hip_bf16.h>
#include <stdint.h>

#define LAS __attribute__((address_space(3)))
#define GAS __attribute__((address_space(1)))
#define DI __device__ __forceinline__
typedef __attribute__((ext_vector_type(8))) __bf16 bf16x8;
typedef __attribute__((ext_vector_type(2))) __bf16 bf16x2;
typedef __attribute__((ext_vector_type(4))) short s16x4;
typedef __attribute__((ext_vector_type(2))) float f32x2;
typedef __attribute__((ext_vector_type(4))) float f32x4;
typedef __attribute__((ext_vector_type(16))) float f32x16;
typedef __attribute__((ext_vector_type(2))) unsigned u32x2;
typedef __attribute__((ext_vector_type(4))) unsigned u32x4;
typedef LAS char* lptr;
typedef __amdgpu_buffer_rsrc_t rsrc_t;

constexpr unsigned WS_Q = 0, WS_K = 2u << 20, WS_V = 4u << 20, WS_O = 6u << 20, WS_U = 8u << 20, WS_H = 10u << 20,
                   WS_G = 13u << 20, WS_FL = 14u << 20, WS_END = 15u << 20;
constexpr int FL_H = 0;
constexpr int FL_Q = 64 * 32;
constexpr int FL_U = FL_Q + 192 * 32;
constexpr int FL_O = FL_U + 352 * 32;
constexpr int FL_EPOCH = FL_O + 256 * 32;
constexpr float QSCALE = 0.25f * 1.4426950408889634f;
constexpr int NS = 7, SLOT = 16384, RING = NS * SLOT;
constexpr int L_K = RING, L_V = RING + 16384, L_X = RING + 32768  , L_C = L_X + 12288  , L_P = L_C + 1024  ;
constexpr int LDS_BYTES = 163840;
constexpr unsigned SPIN_CAP = 200000;
constexpr int KVS = 22;
constexpr int QDS = 10;
constexpr int ODS = 4;

#define MFMA32(a, b, c) __builtin_amdgcn_mfma_f32_32x32x16_bf16(a, b, c, 0, 0, 0)
#define MFMA16(a, b, c) __builtin_amdgcn_mfma_f32_16x16x32_bf16(a, b, c, 0, 0, 0)
#define SB() __builtin_amdgcn_sched_barrier(0)
#define LBAR() do { asm volatile("s_waitcnt lgkmcnt(0)\n\ts_barrier" ::: "memory"); SB(); } while (0)
#define CBAR(N) do { asm volatile("s_waitcnt lgkmcnt(" #N ")\n\ts_barrier" ::: "memory"); SB(); } while (0)
#define DBAR() do { asm volatile("s_waitcnt vmcnt(0) lgkmcnt(0)\n\ts_barrier" ::: "memory"); SB(); } while (0)
#define VBAR(N) asm volatile("s_waitcnt vmcnt(" #N ")\n\ts_barrier" ::: "memory")
#define BAR() asm volatile("s_barrier" ::: "memory")

DI int rfl(int x) { return __builtin_amdgcn_readfirstlane(x); }
DI bf16x8 ldsr128(lptr p) { return *(const LAS bf16x8*)p; }
DI s16x4 ldstr(lptr p) { return __builtin_amdgcn_ds_read_tr16_b64_v4i16((LAS s16x4*)p); }
DI bf16x8 cat8(s16x4 a, s16x4 b) { return __builtin_bit_cast(bf16x8, __builtin_shufflevector(a, b, 0, 1, 2, 3, 4, 5, 6, 7)); }
DI unsigned pk2(float a, float b) { f32x2 f = {a, b}; return __builtin_bit_cast(unsigned, __builtin_convertvector(f, bf16x2)); }
DI float bflo(unsigned u) { return __uint_as_float(u << 16); }
DI float bfhi(unsigned u) { return __uint_as_float(u & 0xffff0000u); }
DI int crow(int i, int h) { return (i & 3) + 8 * (i >> 2) + 4 * h; }
DI void st128(rsrc_t r, unsigned off, u32x4 v) { __builtin_amdgcn_raw_buffer_store_b128(v, r, off, 0, 16); }
DI void st64(rsrc_t r, unsigned off, u32x2 v) { __builtin_amdgcn_raw_buffer_store_b64(v, r, off, 0, 16); }
DI void st32(rsrc_t r, unsigned off, unsigned v) { __builtin_amdgcn_raw_buffer_store_b32(v, r, off, 0, 16); }
DI u32x4 ld128(rsrc_t r, unsigned off) { return __builtin_amdgcn_raw_buffer_load_b128(r, off, 0, 16); }
DI unsigned ld32(rsrc_t r, unsigned off) { return __builtin_amdgcn_raw_buffer_load_b32(r, off, 0, 16); }
DI void flag_st(char* ws, int idx, unsigned v) { __hip_atomic_store((GAS unsigned*)(ws + WS_FL) + idx, v, __ATOMIC_RELAXED, __HIP_MEMORY_SCOPE_AGENT); }

#define M0_IN "s_mov_b32 %0, m0\n\ts_mov_b32 m0, %1\n\ts_nop 2\n\t"
#define M0_OUT "s_mov_b32 m0, %0"
DI void dma_2x2(unsigned m0v, const void* b0, unsigned v0, unsigned v1, const void* b1, unsigned v2, unsigned v3) {
  unsigned sv;
  asm volatile(M0_IN "global_load_lds_dwordx4 %4, %2\n\tglobal_load_lds_dwordx4 %5, %2 offset:1024\n\t"
               "global_load_lds_dwordx4 %6, %3 offset:2048\n\tglobal_load_lds_dwordx4 %7, %3 offset:3072\n\t" M0_OUT
               : "=&s"(sv) : "s"(m0v), "s"(b0), "s"(b1), "v"(v0), "v"(v1), "v"(v2), "v"(v3) : "memory");
}
DI void dma_lin4_sc1(unsigned m0v, const void* b, unsigned vl) {
  unsigned sv;
  asm volatile(M0_IN "global_load_lds_dwordx4 %3, %2 sc1\n\tglobal_load_lds_dwordx4 %3, %2 offset:1024 sc1\n\t"
               "global_load_lds_dwordx4 %3, %2 offset:2048 sc1\n\tglobal_load_lds_dwordx4 %3, %2 offset:3072 sc1\n\t" M0_OUT
               : "=&s"(sv) : "s"(m0v), "s"(b), "v"(vl) : "memory");
}
DI void dma_g1(unsigned m0v, const void* wb, unsigned v0, unsigned v1, const void* hb_m2048, unsigned vl) {
  unsigned sv;
  asm volatile(M0_IN "global_load_lds_dwordx4 %4, %2\n\tglobal_load_lds_dwordx4 %5, %2 offset:1024\n\t"
               "global_load_lds_dwordx4 %6, %3 offset:2048 sc1\n\t"
               "s_mov_b32 exec_hi, 0\n\tglobal_load_lds_dwordx4 %6, %3 offset:3072 sc1\n\ts_mov_b32 exec_hi, -1\n\t" M0_OUT
               : "=&s"(sv) : "s"(m0v), "s"(wb), "s"(hb_m2048), "v"(v0), "v"(v1), "v"(vl) : "memory");
}
DI void dma_hh(unsigned m0v, const void* hb_m2048, unsigned vl) {
  unsigned sv;
  asm volatile(M0_IN "global_load_lds_dwordx4 %3, %2 offset:2048 sc1\n\t"
               "s_mov_b32 exec_hi, 0\n\tglobal_load_lds_dwordx4 %3, %2 offset:3072 sc1\n\ts_mov_b32 exec_hi, -1\n\t" M0_OUT
               : "=&s"(sv) : "s"(m0v), "s"(hb_m2048), "v"(vl) : "memory");
}
DI void dma_touch(unsigned m0v, const void* b, unsigned voff) {
  unsigned sv;
  asm volatile(M0_IN "global_load_lds_dword %3, %2\n\t" M0_OUT : "=&s"(sv) : "s"(m0v), "s"(b), "v"(voff) : "memory");
}
DI void dma_wh(unsigned m0v, const void* wb, unsigned v0, unsigned v1) {
  unsigned sv;
  asm volatile(M0_IN "global_load_lds_dwordx4 %3, %2\n\tglobal_load_lds_dwordx4 %4, %2 offset:1024\n\t" M0_OUT
               : "=&s"(sv) : "s"(m0v), "s"(wb), "v"(v0), "v"(v1) : "memory");
}
DI void dma_fh(unsigned m0v, const void* fb_m2048, unsigned vl) {
  unsigned sv;
  asm volatile(M0_IN "global_load_lds_dwordx4 %3, %2 offset:2048 sc1\n\tglobal_load_lds_dwordx4 %3, %2 offset:3072 sc1\n\t" M0_OUT
               : "=&s"(sv) : "s"(m0v), "s"(fb_m2048), "v"(vl) : "memory");
}

DI void spin1(rsrc_t r, unsigned off, unsigned tok) {
  unsigned n = 0;
#pragma nounroll
  do { const unsigned v = ld32(r, off); if (__all(v == tok)) break; __builtin_amdgcn_s_sleep(4); asm volatile("" ::: "memory"); } while (++n < SPIN_CAP);
  __builtin_amdgcn_fence(__ATOMIC_ACQUIRE, "wavefront");
}
DI void peek_issue(unsigned m0v, const void* ws, unsigned off) {
  unsigned sv;
  asm volatile(M0_IN "global_load_lds_dwordx4 %3, %2 sc1\n\t" M0_OUT : "=&s"(sv) : "s"(m0v), "s"(ws), "v"(off) : "memory");
}
DI void peek_check(lptr p, rsrc_t r, unsigned off, unsigned tok) {
  if (__all(*(const volatile LAS unsigned*)p == tok)) return;
  asm volatile("s_waitcnt vmcnt(0)" ::: "memory");
  if (__all(*(const volatile LAS unsigned*)p == tok)) return;
  spin1(r, off, tok);
}
#define RARE(c) __builtin_expect((c) != 0, 0)

DI float row16_sum(float v) {
#define DPPX(ctrl) v += __int_as_float(__builtin_amdgcn_update_dpp(0, __float_as_int(v), ctrl, 0xf, 0xf, true))
  DPPX(0xB1); DPPX(0x4E); DPPX(0x141); DPPX(0x140);
#undef DPPX
  return v;
}
DI float gelu_tanh(float u) {
  const float z = u * (0.7978845608028654f + 0.7978845608028654f * 0.044715f * u * u);
  const float e = __builtin_amdgcn_exp2f(z * (-2.0f * 1.4426950408889634f));
  return u * __builtin_amdgcn_rcpf(1.0f + e);
}
constexpr unsigned long long diag_mask(int i) {
  unsigned long long mk = 0;
  for (int l = 0; l < 64; ++l) if ((i & 3) + 8 * (i >> 2) + 4 * (l >> 5) <= (l & 31)) mk |= 1ull << l;
  return mk;
}
DI float max16(const f32x16& s, float m) {
#pragma unroll
  for (int i = 0; i < 16; i += 2) m = __builtin_fmaxf(__builtin_fmaxf(s[i], s[i + 1]), m);
  return m;
}

#define NEXT_SO() so = so == (NS - 1) * SLOT ? 0 : so + SLOT

template <bool ISV, class Hook, class Hook14, class Mid>
DI void qkv_consume(lptr L, int& so, int w, int lane, f32x16 (&acc)[2], Hook hook, Hook14 hook14, Mid mid) {
  const int r = lane & 31, h = lane >> 5, wm = w >> 2, wn = w & 3;
  int xa[4];
#pragma unroll
  for (int s = 0; s < 4; ++s) xa[s] = (wm * 2 + (r >> 4)) * 4096 + (r & 15) * 128 + (((2 * s + h) ^ ((r >> 1) & 7)) << 4);
  const int q = (lane & 15) >> 2, p = lane & 3, blk = (lane >> 4) & 1;
  const int wa = h * 4096 + 2048 + q * 256 + (((wn * 4 + blk * 2 + (p >> 1)) ^ (q << 2)) << 4) + (p & 1) * 8;
  bf16x8 xA[2][4], xB[2][4], wA[4], wB[4];
#define QM(X, W, mi, s) do { if (ISV) acc[mi] = MFMA32(X[mi][s], W[s], acc[mi]); else acc[mi] = MFMA32(W[s], X[mi][s], acc[mi]); SB(); } while (0)
#define QRX(X, mi, s) X[mi][s] = ldsr128(sl + xa[s])
#define QRW(W, mi, e) W[2 * mi + e] = cat8(ldstr(sl + wa + e * 8192), ldstr(sl + wa + e * 8192 + 1024))
#define QSTEP(mi, XN, WN, XP, WP, MM) do { lptr sl = L + so; NEXT_SO(); \
    if (MM) QM(XP, WP, 0, 2 * mi); \
    QRX(XN, mi, 0); QRX(XN, mi, 1); SB(); if (MM) QM(XP, WP, 1, 2 * mi); \
    QRX(XN, mi, 2); QRX(XN, mi, 3); QRW(WN, mi, 0); SB(); if (MM) QM(XP, WP, 0, 2 * mi + 1); \
    QRW(WN, mi, 1); SB(); if (MM) QM(XP, WP, 1, 2 * mi + 1); } while (0)
  LBAR(); QSTEP(0, xA, wA, xB, wB, false); QSTEP(1, xA, wA, xB, wB, false);
#pragma unroll 1
  for (int c = 1; c < 7; c += 2) {
    hook(c);     QSTEP(0, xB, wB, xA, wA, true); QSTEP(1, xB, wB, xA, wA, true);
    hook(c + 1); QSTEP(0, xA, wA, xB, wB, true); QSTEP(1, xA, wA, xB, wB, true);
  }
  hook14(); QSTEP(0, xB, wB, xA, wA, true); QSTEP(1, xB, wB, xA, wA, true);
#pragma unroll
  for (int s = 0; s < 4; ++s) QM(xB, wB, 0, s);
  mid();
#pragma unroll
  for (int s = 0; s < 4; ++s) QM(xB, wB, 1, s);
}

__global__ __launch_bounds__(768, 3) void fused_kernel(const char* __restrict__ x, const char* __restrict__ wqkv, const char* __restrict__ wo,
                                                       const char* __restrict__ w1, const char* __restrict__ b1, const char* __restrict__ w2,
                                                       const char* __restrict__ b2, const char* __restrict__ gw, char* __restrict__ ws,
                                                       char* __restrict__ out) {
  extern __shared__ __attribute__((aligned(16))) char smem[];
  asm volatile("" :: "s"(x), "s"(wqkv), "s"(wo), "s"(w1), "s"(b1), "s"(w2), "s"(b2), "s"(gw), "s"(ws), "s"(out));
  const int b = blockIdx.x, xcd = b & 7, idx = ((b >> 3) + 24) & 31;
  const bool lead = b == 65;
  const int lane = threadIdx.x & 63, w = rfl(threadIdx.x >> 6);
  const unsigned lds0 = (unsigned)(uintptr_t)smem;
  lptr L = (lptr)smem;
  const int tm = (xcd >> 1) * 4 + idx / 6, tn = (xcd & 1) * 6 + idx % 6;
  const int lnb = xcd * 8 + (idx - 24);
  const unsigned x0 = (lane >> 3) * 1024 + (((lane & 7) ^ (lane >> 4)) << 4), x1 = (8 + (lane >> 3)) * 1024 + (((lane & 7) ^ (4 + (lane >> 4))) << 4) - 1024;
  const unsigned q0 = (lane >> 4) * 3072 + (((lane & 15) ^ ((lane >> 4) << 2)) << 4), q1 = q0 + 4 * 3072 - 1024;
  const char* xp = x + (size_t)(tm * 128 + ((w >> 1) & 1) * 64 + (w & 1) * 16) * 1024;
  const char* qp = wqkv + (size_t)((w & 3) * 8) * 3072 + tn * 256 - 2048;
  int kc = ((idx / 6 + idx % 6) % 3) * 3;
  if (w >= 8) {
    if (idx < 24) {
#pragma unroll 1
      for (int f = 0; f < 7; ++f) {
        dma_2x2(lds0 + (w & 3) * 4096 + f * SLOT, xp + kc * 128 + (f & 1) * 32 * 1024, x0, x1, qp + (size_t)(kc * 2 + (f & 1)) * (32 * 3072), q0, q1);
        kc = (kc + (f & 1)) & 7;
      }
    }
  }
  asm volatile("" ::: "memory");
  SB();
  const int gi = 31 - idx;
  const bool hasQ = idx < 24, hasG1 = xcd == 0 || gi < 30;
  const bool lowd = xcd == 0 ? gi < 12 : xcd < 5;
  const int gj = xcd == 0 && !lowd ? gi - 12 : gi;
  const int g1ft = lowd ? (xcd == 0 ? 20 : (xcd - 1) * 5) + gj / 6 : (xcd == 0 ? 18 : (xcd - 5) * 6) + gj / 5;
  const int g1dt = lowd ? gj % 6 : 6 + gj % 5;
  const int head = xcd * 4 + (idx & 3), qb = idx >> 2;
  const int st = (xcd >> 1) * 8 + (idx >> 2), dt = (xcd & 1) * 4 + (idx & 3);
  unsigned fq_off;
  { const int l = lane < 12 ? lane : 3, wh = l >> 2; int tmi = l & 3; if (wh == 0 || qb == 0) tmi |= 2;
    fq_off = WS_FL + (FL_Q + ((wh * 4 + (head >> 3)) * 16 + 2 * qb - 2 + tmi) * 32) * 4; }
  const unsigned fu_off = WS_FL + (FL_U + (((lane & 32) ? (4 * dt + 3) / 3 : (4 * dt) / 3) * 32 + min(lane & 31, 21)) * 32) * 4;
  const unsigned fo_off = WS_FL + (FL_O + ((st >> 2) * 32 + (lane & 31)) * 32) * 4;
  const unsigned fh_off = WS_FL + FL_H * 4 + lane * 128;
  const rsrc_t rws = __builtin_amdgcn_make_buffer_rsrc(ws, 0, WS_END, 0x00020000);
  volatile LAS unsigned* relay = (volatile LAS unsigned*)(L + L_C + 64);
  unsigned eoff = WS_FL + FL_EPOCH * 4;
  asm volatile("" : "+v"(eoff));
  const unsigned epoch = ld32(rws, eoff);
#define TOKEN_OF(e) ((e) * 1664525u + 1013904223u)

  if (w >= 8) {
    const int j = w - 8;
#define token TOKEN_OF(epoch)
    const unsigned m0b = lds0 + j * 4096;
    const unsigned vl = lane * 16;
    unsigned so = 0;
    const char* hp = ws + WS_H + (size_t)g1dt * 64 * 3072 + j * 1536 - 2048;
    const char* w1p = w1 + (size_t)(j * 16) * 2752 + g1ft * 128;
    const unsigned u0 = (lane >> 3) * 2752 + ((((lane & 7) ^ (((lane >> 4) & 3) << 1)) & (g1ft == 21 ? 3 : 7)) << 4), u1 = u0 + 8 * 2752 - 1024;
    int cw = lowd ? g1dt * 6 : (g1dt - 6) * 6 + 3, ch = cw;
    auto fill_g1 = [&]() { dma_g1(m0b + so, w1p + (size_t)cw * (64 * 2752), u0, u1, hp + cw * 6144, vl); cw = (cw + 1) & 31; NEXT_SO(); };
    auto fill_g1w = [&]() { dma_wh(m0b + so, w1p + (size_t)cw * (64 * 2752), u0, u1); cw = (cw + 1) & 31; NEXT_SO(); };
    auto fill_g1h3 = [&](unsigned so0) {
      const unsigned keep = so; so = so0;
#pragma unroll 1
      for (int i = 0; i < 3; ++i) { dma_hh(m0b + so, hp + ch * 6144, vl); ch = (ch + 1) & 31; NEXT_SO(); }
      so = keep;
    };
    const unsigned swz = ((lane & 7) ^ (((lane >> 4) & 1) << 2)) << 4;
    unsigned vw0 = (lane >> 3) * 4096 + swz, vw1 = vw0 + 8 * 4096 - 1024;
    const char* w2p = w2 + (size_t)(j * 16) * 4096 + st * 128;
    const char* up = ws + WS_U + (size_t)dt * 86 * 2048 + j * 2048 - 2048;
    const size_t w2half = (size_t)(j < 2 ? 21 * 64 : 20 * 64) * 4096;
    const int uhalf = j < 2 ? 21 * 8192 : 20 * 8192;
    auto fill_kv = [&]() {
      int t0 = qb * 8 - 8 + 4 * j; t0 = t0 < 0 ? t0 + 8 : t0;
      dma_lin4_sc1(lds0 + L_K + j * 4096, ws + WS_K + (size_t)(head * 64 + t0) * 1024, vl);
      dma_lin4_sc1(lds0 + L_V + j * 4096, ws + WS_V + (size_t)(head * 64 + t0) * 1024, vl);
    };
    int nwh = 0;
    const int r2 = (((dt & 3) * 2 + (dt >> 2)) * 21) >> 3;
    int c2 = r2;
    auto fill_wh = [&]() {
      dma_wh(m0b + so, nwh == 0 ? w2p + w2half : w2p + (size_t)c2 * (64 * 4096), vw0, vw1);
      if (nwh) c2 = c2 == 20 ? 0 : c2 + 1;
      ++nwh; NEXT_SO();
    };
    unsigned so_g2 = 0;
    auto warm_g2 = [&]() {
      if (j == 1) dma_touch(lds0 + L_P + 1024, wo + (size_t)(idx * 16) * 1024 + (xcd & 1) * 512, (lane >> 2) * 1024 + (lane & 3) * 128);
    };

    if (hasQ) {
      auto fill_a = [&]() { dma_2x2(m0b + so, xp + kc * 128, x0, x1, qp + (size_t)kc * 64 * 3072, q0, q1); NEXT_SO(); };
      auto fill_b = [&]() { dma_2x2(m0b + so, xp + kc * 128 + 32 * 1024, x0, x1, qp + (size_t)kc * 64 * 3072 + 32 * 3072, q0, q1); kc = (kc + 1) & 7; NEXT_SO(); };
      VBAR(20);
#pragma unroll 1
      for (int c = 1; c < 5; ++c) { VBAR(12); fill_b(); fill_a(); }
      VBAR(12); fill_b();
      if (hasG1) {
        const unsigned so0 = so;
        fill_g1w();
        VBAR(10); fill_g1w(); fill_g1w();
        VBAR(6); fill_g1h3(so0); fill_g1(); fill_g1();
      } else {
        VBAR(8); VBAR(0);
      }
    } else {
      BAR();
      VBAR(0);
#pragma unroll 1
      for (int s = 0; s < 3; ++s) fill_g1w();
      if (j == 0) spin1(rws, fh_off, token);
      BAR();
      fill_g1h3(0); fill_g1(); fill_g1();
    }
    if (hasG1) {
      VBAR(12); fill_g1(); VBAR(14); fill_g1();
#pragma unroll 1
      for (int s = 2; s <= KVS; ++s) { VBAR(16); fill_g1(); }
      fill_kv();
#pragma unroll 1
      for (int s = KVS + 1; s < 27; ++s) { VBAR(16); fill_g1(); }
      so_g2 = so;
      VBAR(16); fill_wh(); VBAR(14); fill_wh(); VBAR(12); fill_wh(); VBAR(10); fill_wh(); VBAR(8); fill_wh();
      BAR(); fill_wh(); fill_wh();
      warm_g2();
      BAR();
      BAR();
    } else {
      BAR();
      so_g2 = so;
#pragma unroll 1
      for (int i = 0; i < 7; ++i) fill_wh();
      warm_g2();
      if (j == 0) spin1(rws, fq_off, token);
      BAR();
      fill_kv();
      VBAR(0);
    }
    {
      if (j == 0) { spin1(rws, fu_off, token); *relay = 1u; }
      else { for (unsigned spins = 0; spins < 64 * SPIN_CAP && *relay == 0u; ++spins) __builtin_amdgcn_s_sleep(1); }
      so = so_g2;
      dma_fh(m0b + so, up + uhalf, vl); NEXT_SO();
#pragma unroll 1
      for (int i = 0, c = r2; i < 6; ++i) { dma_fh(m0b + so, up + c * 8192, vl); c = c == 20 ? 0 : c + 1; NEXT_SO(); }
    }
    unsigned wsh = 18;
    int ci = 7;
    auto fill_g2 = [&]() {
      if (ci == 22) {
        w2p = wo + (size_t)(j * 16) * 1024 + dt * 128; up = ws + WS_O + (size_t)st * 32 * 2048 + j * 2048 - 2048; wsh = 16; c2 = 0;
        vw0 = (lane >> 3) * 1024 + swz; vw1 = vw0 + 8 * 1024 - 1024;
      }
      dma_wh(m0b + so, w2p + ((size_t)c2 << wsh), vw0, vw1); dma_fh(m0b + so, up + c2 * 8192, vl);
      c2 = c2 == 20 ? 0 : c2 + 1; NEXT_SO(); ++ci;
    };
    VBAR(12); VBAR(10);
    VBAR(8); fill_g2(); VBAR(10); fill_g2(); VBAR(12); fill_g2(); VBAR(14); fill_g2();
#pragma unroll 1
    for (int s = 6; s < 25; ++s) { VBAR(16); fill_g2(); }
    const unsigned fa_off = WS_FL + FL_O * 4 + (j * 64 + lane) * 128;
    if (lead) peek_issue(lds0 + L_X + j * 1024, ws, fa_off);
    VBAR(16); VBAR(12); VBAR(8); VBAR(4); VBAR(0);
    if (lead) peek_check(L + L_X + j * 1024 + lane * 16, rws, fa_off, token);
    BAR();
    if (lead && j == 0 && lane == 0) st32(rws, WS_FL + FL_EPOCH * 4, token);
    BAR();
    return;
  }

#undef token
  unsigned token;
  u32x4 xv[4], gv[4];
  if (!hasQ) {
#pragma unroll
    for (int i = 0; i < 4; ++i) xv[i] = *(const u32x4*)(x + (size_t)(lnb * 32 + w * 4 + (lane >> 4)) * 1024 + (lane & 15) * 16 + i * 256);
#pragma unroll
    for (int i = 0; i < 4; ++i) gv[i] = *(const u32x4*)(gw + (lane & 15) * 16 + i * 256);
  }
  asm volatile("" ::: "memory");
  SB();
  const int r = lane & 31, h = lane >> 5;
  const int g4 = lane >> 4, q4 = (lane & 15) >> 2, p4 = lane & 3, blk = (lane >> 4) & 1;
  int so = 0;
  if (threadIdx.x == 4) *relay = 0u;
  if (threadIdx.x < 4) {
    const unsigned cv = (threadIdx.x & 1) ? 0u : 0x3f803f80u;
    *(LAS u32x4*)(L + L_C + (threadIdx.x >> 1) * 512 + (threadIdx.x & 1) * 16) = u32x4{cv, cv, cv, cv};
  }
  lptr pk = L + L_P + lane * 16;

  const int kh = w >> 2, fb = w & 3;
  int ha[3];
#pragma unroll
  for (int db = 0; db < 3; ++db) { const int o = (kh * 3 + db) * 1024 + lane * 16, rg = o / 1536; ha[db] = rg * 4096 + 2048 + (o - rg * 1536); }
  const int wa = kh * 8192 + (4 * g4 + q4) * 128 + ((fb ^ ((2 * g4 + (q4 >> 1)) & 3)) << 5) + p4 * 8;
  f32x4 acc[3] = {};
  bf16x8 wA = {}, hA[3] = {}, wB = {}, hB[3] = {};
#define G1RD(W, H) do { lptr sl = L + so; NEXT_SO(); W = cat8(ldstr(sl + wa), ldstr(sl + wa + 4096)); \
  _Pragma("unroll") for (int db = 0; db < 3; ++db) H[db] = ldsr128(sl + ha[db]); SB(); } while (0)
#define G1MM(W, H) do { _Pragma("unroll") for (int db = 0; db < 3; ++db) acc[db] = MFMA16(W, H[db], acc[db]); SB(); } while (0)

  token = TOKEN_OF(epoch);
  asm volatile("" : "+v"(token), "+v"(gv[3]));
  if (hasQ) {
    if (w == 7) *(LAS unsigned*)pk = ~token;
    f32x16 qacc[2] = {};
    const bool poller = w == 7 && hasG1;
    const unsigned post = poller ? (1u << 4) | (1u << 5) | (1u << 6) : 0u;
    auto hook = [&](int s) {
      LBAR();
      if (RARE((post >> s) & 1)) peek_issue(lds0 + L_P, ws, fh_off);
      SB();
    };
    auto hook14 = [&]() { if (poller) peek_check(pk, rws, fh_off, token); LBAR(); };
    auto mid = [&]() { if (hasG1) { LBAR(); G1RD(wA, hA); } };
    if (tn >= 8) qkv_consume<true>(L, so, w, lane, qacc, hook, hook14, mid);
    else qkv_consume<false>(L, so, w, lane, qacc, hook, hook14, mid);
    const int wm = w >> 2, wn = w & 3;
    const int hd0 = (tn & 3) * 8 + wn * 2;
    const float sc = tn < 4 ? QSCALE : 1.0f;
    const unsigned dst = tn >= 8 ? WS_V + (((hd0 + (r >> 4)) * 64 + tm * 4 + wm * 2) * 64 + h * 16 + (r & 15)) * 16
                                 : (tn < 4 ? WS_Q : WS_K) + ((hd0 * 64 + tm * 4 + wm * 2) * 64 + lane) * 16;
    const unsigned dstep = tn >= 8 ? 512 : 65536;
#pragma unroll
    for (int mi = 0; mi < 2; ++mi) {
#pragma unroll
      for (int s = 0; s < 2; ++s) {
        u32x4 v;
#pragma unroll
        for (int jj = 0; jj < 4; ++jj) v[jj] = pk2(qacc[mi][8 * s + 2 * jj] * sc, qacc[mi][8 * s + 2 * jj + 1] * sc);
        st128(rws, dst + mi * 1024 + s * dstep, v);
      }
      if (mi == 0 && hasG1) { SB(); LBAR(); G1RD(wB, hB); G1MM(wA, hA); }
    }
  } else {
    constexpr int RS = 1056;
    f32x2 v[16], s2 = {0.f, 0.f}, g2 = {0.f, 0.f};
#pragma unroll
    for (int i = 0; i < 16; ++i) {
      v[i] = f32x2{bflo(xv[i >> 2][i & 3]), bfhi(xv[i >> 2][i & 3])};
      s2 += v[i];
      g2 += v[i] * f32x2{bflo(gv[i >> 2][i & 3]), bfhi(gv[i >> 2][i & 3])};
    }
    const float mean = row16_sum(s2[0] + s2[1]) * (1.0f / 512), gs = row16_sum(g2[0] + g2[1]);
    f32x2 q2 = {0.f, 0.f};
#pragma unroll
    for (int i = 0; i < 16; ++i) { v[i] -= f32x2{mean, mean}; q2 += v[i] * v[i]; }
    const float rs = __builtin_amdgcn_rsqf(row16_sum(q2[0] + q2[1]) * (1.0f / 512) + 1e-5f);
#pragma unroll
    for (int i = 0; i < 4; ++i) {
      u32x4 ov;
#pragma unroll
      for (int jj = 0; jj < 4; ++jj) { const f32x2 t = v[4 * i + jj] * f32x2{rs, rs}; ov[jj] = pk2(t[0], t[1]); }
      *(LAS u32x4*)(L + (w * 4 + g4) * RS + (lane & 15) * 16 + i * 256) = ov;
    }
    if ((lane & 15) == 0) st32(rws, WS_G + (lnb * 32 + w * 4 + g4) * 4, __float_as_uint(1.0f / (1.0f + __expf(-gs))));
    LBAR();
#pragma unroll
    for (int i = 0; i < 4; ++i) {
      const int D = w * 4 + i, d48 = D / 3;
      lptr a = L + (4 * g4 + q4) * RS + D * 32 + p4 * 8;
      const bf16x8 f = cat8(ldstr(a), ldstr(a + 16 * RS));
      st128(rws, WS_H + ((d48 * 64 + lnb) * 3 + (D - d48 * 3)) * 1024 + lane * 16, __builtin_bit_cast(u32x4, f));
      if (i == 3 && w == 7) st128(rws, WS_H + ((10 * 64 + lnb) * 3 + 2) * 1024 + lane * 16, __builtin_bit_cast(u32x4, f));
    }
    DBAR();
    if (threadIdx.x == 0) flag_st(ws, FL_H + lnb * 32, token);
    LBAR();
    LBAR(); G1RD(wA, hA);
    LBAR(); G1RD(wB, hB); G1MM(wA, hA);
  }

  bf16x8 qf;
  if (hasG1) {
    const unsigned pre = (1u << QDS) | (w == 7 ? 1u << KVS : 0u);
    const unsigned post = (w == 0 && hasQ ? 1u << QDS : 0u) | (w == 7 ? (1u << QDS) | (1u << (KVS - 6)) | (1u << (KVS - 4)) | (1u << (KVS - 2)) : 0u);
    auto hook = [&](int s) {
      if (RARE((pre >> s) & 1)) { if (s == QDS) asm volatile("s_waitcnt vmcnt(0)" ::: "memory"); else peek_check(pk, rws, fq_off, token); }
      CBAR(5);
      if (RARE((post >> s) & 1)) {
        if (s == QDS) { if (w == 7) *(LAS unsigned*)pk = ~token; else if (lane == 0) flag_st(ws, FL_Q + (tn * 16 + tm) * 32, token); }
        else peek_issue(lds0 + L_P, ws, fq_off);
      }
      SB();
    };
#pragma unroll 1
    for (int s = 2; s < KVS + 2; s += 2) {
      hook(s); G1RD(wA, hA); G1MM(wB, hB);
      CBAR(5); G1RD(wB, hB); G1MM(wA, hA);
    }
    qf = __builtin_bit_cast(bf16x8, ld128(rws, WS_Q + (head * 64 + qb * 8 + w) * 1024 + lane * 16));
    const int fblk = g1ft * 4 + fb;
    u32x2 bb = *(const u32x2*)(b1 + min(fblk * 16 + 4 * g4, 1372) * 2);
    unsigned ua[2];
#pragma unroll
    for (int e = 0; e < 2; ++e) {
      const int D = g1dt * 3 + (kh ? 2 : e);
      ua[e] = WS_U + ((((D >> 2) * 86 + fblk) * 2 + ((D >> 1) & 1)) * 64 + (g4 >> 1) * 32 + (D & 1) * 16 + (lane & 15)) * 16 + (g4 & 1) * 8;
      if (fblk >= 86 || D >= 32) ua[e] = WS_END;
    }
    lptr xw = L + L_X + (fb * 3 * 64 + lane) * 16;
#pragma unroll 1
    for (int s = KVS + 2; s < 32; s += 2) {
      CBAR(5); G1RD(wA, hA); G1MM(wB, hB);
      CBAR(5); G1RD(wB, hB); G1MM(wA, hA);
    }
    asm volatile("" : "+v"(qf), "+v"(bb), "+v"(ua[0]), "+v"(ua[1]));
    const f32x4 bv = {bflo(bb[0]), bfhi(bb[0]), bflo(bb[1]), bfhi(bb[1])};
    LBAR();
    G1MM(wB, hB);
    if (kh == 1) { *(LAS f32x4*)(xw) = acc[0]; *(LAS f32x4*)(xw + 1024) = acc[1]; }
    else *(LAS f32x4*)(xw + 2048) = acc[2];
    LBAR();
    auto fin = [&](const f32x4& a, int off, unsigned addr) {
      const f32x4 t = a + *(const LAS f32x4*)(xw + off) + bv;
      st64(rws, addr, u32x2{pk2(gelu_tanh(t[0]), gelu_tanh(t[1])), pk2(gelu_tanh(t[2]), gelu_tanh(t[3]))});
    };
    if (kh == 1) fin(acc[2], 2048, ua[0]);
    else { fin(acc[0], 0, ua[0]); fin(acc[1], 1024, ua[1]); }
  } else {
    DBAR();
    if (threadIdx.x == 0) flag_st(ws, FL_Q + (tn * 16 + tm) * 32, token);
    LBAR();
    qf = __builtin_bit_cast(bf16x8, ld128(rws, WS_Q + (head * 64 + qb * 8 + w) * 1024 + lane * 16));
    asm volatile("" : "+v"(qf));
    LBAR();
  }

  {
    lptr kp = L + L_K + w * 1024 + lane * 16;
    const int i_lo = qb > 0 ? 1 : 8 - w;
    const f32x16 zero = {};
    bf16x8 kf[9];
#pragma unroll
    for (int i = 0; i < 9; ++i) kf[i] = ldsr128(kp + i * 1024);
    f32x16 sb, sa, sc;
    float mx[4];
#define TMAX(S, i) (((i) >= i_lo) ? max16(S, -INFINITY) : -INFINITY)
    sa = MFMA32(kf[8], qf, zero); sc = MFMA32(kf[0], qf, zero);
    f32x16 s1 = MFMA32(kf[1], qf, zero), s2 = MFMA32(kf[2], qf, zero); SB();
#pragma unroll
    for (int i = 0; i < 16; ++i) sb[i] = __builtin_amdgcn_inverse_ballot_w64(diag_mask(i)) ? sa[i] : (qb > 0 ? sc[i] : -INFINITY);
    SB();
    sa = MFMA32(kf[3], qf, zero); sc = MFMA32(kf[4], qf, zero); SB();
    mx[0] = max16(sb, -INFINITY); mx[1] = TMAX(s1, 1); mx[2] = TMAX(s2, 2); SB();
    s1 = MFMA32(kf[5], qf, zero); s2 = MFMA32(kf[6], qf, zero); SB();
    mx[3] = TMAX(sa, 3); mx[0] = __builtin_fmaxf(mx[0], TMAX(sc, 4)); SB();
    sa = MFMA32(kf[7], qf, zero); SB();
    mx[1] = __builtin_fmaxf(mx[1], TMAX(s1, 5)); mx[2] = __builtin_fmaxf(mx[2], TMAX(s2, 6)); mx[3] = __builtin_fmaxf(mx[3], TMAX(sa, 7));
    float m = __builtin_fmaxf(__builtin_fmaxf(mx[0], mx[1]), __builtin_fmaxf(mx[2], mx[3]));
    {
      auto rr = __builtin_amdgcn_permlane32_swap(__float_as_uint(m), __float_as_uint(m), false, false);
      m = __builtin_fmaxf(__uint_as_float(rr[0]), __uint_as_float(rr[1]));
    }
    if (hasG1) {
      DBAR();
      if (threadIdx.x == 0) flag_st(ws, FL_U + (g1dt * 32 + g1ft) * 32, token);
    }
    const int vstr = r < 16 ? 1024 : 0;
    lptr vp0 = L + (r < 16 ? L_V + w * 1024 + h * 256 + r * 16 : (L_C + ((r == 16 || r == 20) ? 0 : 16)));
    f32x16 cin;
#pragma unroll
    for (int i = 0; i < 16; ++i) cin[i] = -m;
    f32x16 s = MFMA32(ldsr128(kp + i_lo * 1024), qf, cin);
    f32x16 o = {};
    {
      u32x4 pd[2], pf[2];
#pragma unroll
      for (int i = 0; i < 16; i += 2) {
        const unsigned pp = pk2(__builtin_amdgcn_exp2f(sb[i] - m), __builtin_amdgcn_exp2f(sb[i + 1] - m));
        const int dlt = r - crow(i, h);
        const unsigned mk = dlt >= 1 ? 0xffffffffu : (dlt == 0 ? 0x0000ffffu : 0u);
        pd[i >> 3][(i >> 1) & 3] = pp & mk;
        pf[i >> 3][(i >> 1) & 3] = pp & ~mk;
      }
      lptr vp = vp0 + 8 * vstr;
      o = MFMA32(ldsr128(vp), __builtin_bit_cast(bf16x8, pd[0]), o);
      o = MFMA32(ldsr128(vp + 512), __builtin_bit_cast(bf16x8, pd[1]), o);
      o = MFMA32(ldsr128(vp0), __builtin_bit_cast(bf16x8, pf[0]), o);
      o = MFMA32(ldsr128(vp0 + 512), __builtin_bit_cast(bf16x8, pf[1]), o);
    }
#pragma unroll 1
    for (int i = i_lo; i < 8; ++i) {
      lptr vp = vp0 + i * vstr;
      const bf16x8 kn = ldsr128(kp + (i + 1) * 1024), v0 = ldsr128(vp), v1 = ldsr128(vp + 512);
      u32x4 p0, p1;
#pragma unroll
      for (int e = 0; e < 8; e += 2) p0[e >> 1] = pk2(__builtin_amdgcn_exp2f(s[e]), __builtin_amdgcn_exp2f(s[e + 1]));
      o = MFMA32(v0, __builtin_bit_cast(bf16x8, p0), o);
#pragma unroll
      for (int e = 8; e < 16; e += 2) p1[(e >> 1) & 3] = pk2(__builtin_amdgcn_exp2f(s[e]), __builtin_amdgcn_exp2f(s[e + 1]));
      s = MFMA32(kn, qf, cin);
      o = MFMA32(v1, __builtin_bit_cast(bf16x8, p1), o);
    }
    const float il = __builtin_amdgcn_rcpf(o[8]);
    u32x4 ov;
#pragma unroll
    for (int jj = 0; jj < 4; ++jj) ov[jj] = pk2(o[2 * jj] * il, o[2 * jj + 1] * il);
    const int B = qb * 8 + w;
    st128(rws, WS_O + (((B >> 1) * 32 + head) * 2 + (B & 1)) * 1024 + lane * 16, ov);
  }

  {
    const int mt = (w >> 1) & 1, nt = w & 1;
    const int tok = st * 64 + nt * 32 + r;
    const int ta2 = kh * 8192 + (8 * h + q4) * 128 + (((nt * 4 + blk * 2 + (p4 >> 1)) ^ ((q4 >> 1) << 2)) << 4) + (p4 & 1) * 8;
    const int tao = kh * 8192 + (4 * h + q4) * 128 + (((mt * 4 + blk * 2 + (p4 >> 1)) ^ ((q4 >> 1) << 2)) << 4) + (p4 & 1) * 8;
    const int fa2 = kh * 8192 + 2048 + mt * 1024 + lane * 16;
    const int fao = kh * 8192 + 2048 + nt * 1024 + lane * 16;
    f32x16 ay = {}, aa = {};
    bf16x8 pa0, pb0, pa1, pb1, qa0, qb0, qa1, qb1;
    float g = 0.f; unsigned b2v = 0;
#define RD2(A0, B0, A1, B1) do { lptr sl = L + so; NEXT_SO(); B0 = cat8(ldstr(sl + ta2), ldstr(sl + ta2 + 512)); A0 = ldsr128(sl + fa2); \
    B1 = cat8(ldstr(sl + ta2 + 4096), ldstr(sl + ta2 + 4096 + 512)); A1 = ldsr128(sl + fa2 + 4096); SB(); } while (0)
#define RDO(A0, B0, A1, B1) do { lptr sl = L + so; NEXT_SO(); A0 = cat8(ldstr(sl + tao), ldstr(sl + tao + 1024)); B0 = ldsr128(sl + fao); \
    A1 = cat8(ldstr(sl + tao + 4096), ldstr(sl + tao + 4096 + 1024)); B1 = ldsr128(sl + fao + 4096); SB(); } while (0)
#define MM2(ACC, A0, B0, A1, B1) do { ACC = MFMA32(A0, B0, ACC); ACC = MFMA32(A1, B1, ACC); SB(); } while (0)
    const unsigned pre = (1u << ODS) | (w == 7 ? 1u << 16 : 0u);
    const unsigned post = (w == 0 ? 1u << ODS : 0u) | (w == 7 ? (1u << ODS) | (1u << 10) | (1u << 12) | (1u << 14) : 0u);
    auto hook = [&](int s) {
      if (RARE((pre >> s) & 1)) { if (s == ODS) asm volatile("s_waitcnt vmcnt(0)" ::: "memory"); else peek_check(pk, rws, fo_off, token); }
      CBAR(6);
      if (RARE((post >> s) & 1)) {
        if (s == ODS) { if (w == 7) *(LAS unsigned*)pk = ~token; else if (lane == 0) flag_st(ws, FL_O + (qb * 32 + head) * 32, token); }
        else peek_issue(lds0 + L_P, ws, fo_off);
      }
      SB();
    };
    LBAR();
    RD2(pa0, pb0, pa1, pb1);
    if (kh) { pa0 = bf16x8{}; pa1 = bf16x8{}; pb0 = bf16x8{}; pb1 = bf16x8{}; }
#pragma unroll 1
    for (int s = 2; s < 22; s += 2) {
      CBAR(6); RD2(qa0, qb0, qa1, qb1); MM2(ay, pa0, pb0, pa1, pb1);
      hook(s); RD2(pa0, pb0, pa1, pb1); MM2(ay, qa0, qb0, qa1, qb1);
    }
    g = __uint_as_float(ld32(rws, WS_G + tok * 4));
    b2v = ((const unsigned short*)b2)[tok];
    CBAR(6); RD2(qa0, qb0, qa1, qb1); MM2(ay, pa0, pb0, pa1, pb1);
    CBAR(6); RDO(pa0, pb0, pa1, pb1); MM2(ay, qa0, qb0, qa1, qb1);
#pragma unroll 1
    for (int s = 23; s < 29; s += 2) {
      CBAR(6); RDO(qa0, qb0, qa1, qb1); MM2(aa, pa0, pb0, pa1, pb1);
      CBAR(6); RDO(pa0, pb0, pa1, pb1); MM2(aa, qa0, qb0, qa1, qb1);
    }
    CBAR(6); RDO(qa0, qb0, qa1, qb1); MM2(aa, pa0, pb0, pa1, pb1);
    MM2(aa, qa0, qb0, qa1, qb1);
    const float og = 1.0f - g;
    float v[16];
#pragma unroll
    for (int i = 0; i < 16; ++i) v[i] = ay[i] * og + aa[i] * g;
    LBAR();
    {
      lptr ex = L + w * 2048 + lane * 16;
      f32x4 s0, s1;
#pragma unroll
      for (int jj = 0; jj < 4; ++jj) { s0[jj] = kh ? v[jj] : v[8 + jj]; s1[jj] = kh ? v[4 + jj] : v[12 + jj]; }
      *(LAS f32x4*)(ex) = s0; *(LAS f32x4*)(ex + 1024) = s1;
    }
    LBAR();
    {
      lptr ex = L + (w ^ 4) * 2048 + lane * 16;
      const f32x4 r0 = *(const LAS f32x4*)(ex), r1 = *(const LAS f32x4*)(ex + 1024);
      const float bo = bflo(b2v) * og;
      float f[8];
#pragma unroll
      for (int jj = 0; jj < 4; ++jj) { f[jj] = (kh ? v[8 + jj] : v[jj]) + r0[jj] + bo; f[4 + jj] = (kh ? v[12 + jj] : v[4 + jj]) + r1[jj] + bo; }
      const unsigned a0 = pk2(f[0], f[1]), a1 = pk2(f[2], f[3]), c0 = pk2(f[4], f[5]), c1 = pk2(f[6], f[7]);
      auto x0 = __builtin_amdgcn_permlane32_swap(a0, c0, false, false);
      auto x1 = __builtin_amdgcn_permlane32_swap(a1, c1, false, false);
      const u32x4 ov = {x0[0], x1[0], x0[1], x1[1]};
      __builtin_amdgcn_raw_buffer_store_b128(ov, __builtin_amdgcn_make_buffer_rsrc(out, 0, 2048 * 1024, 0x00020000), tok * 1024 + (dt * 64 + mt * 32 + kh * 16 + h * 8) * 2, 0, 2);
    }
  }
}

extern "C" void kernel_launch(void* const* d_in, const int* in_sizes, int n_in, void* d_out, int out_size, void* d_ws, size_t ws_size,
                              hipStream_t stream) {
  (void)hipFuncSetAttribute((const void*)fused_kernel, hipFuncAttributeMaxDynamicSharedMemorySize, LDS_BYTES);
  fused_kernel<<<256, 768, LDS_BYTES, stream>>>((const char*)d_in[0], (const char*)d_in[1], (const char*)d_in[2], (const char*)d_in[4],
                                                (const char*)d_in[5], (const char*)d_in[6], (const char*)d_in[7], (const char*)d_in[8],
                                                (char*)d_ws, (char*)d_out);
}
```

```cpp
#include <hip/hip_runtime.h>
#include <hip/hip_bf16.h>
#include <stdint.h>

#define LAS __attribute__((address_space(3)))
#define GAS __attribute__((address_space(1)))
#define DI __device__ __forceinline__
typedef __attribute__((ext_vector_type(8))) __bf16 bf16x8;
typedef __attribute__((ext_vector_type(2))) __bf16 bf16x2;
typedef __attribute__((ext_vector_type(4))) short s16x4;
typedef __attribute__((ext_vector_type(2))) float f32x2;
typedef __attribute__((ext_vector_type(4))) float f32x4;
typedef __attribute__((ext_vector_type(16))) float f32x16;
typedef __attribute__((ext_vector_type(2))) unsigned u32x2;
typedef __attribute__((ext_vector_type(4))) unsigned u32x4;
typedef LAS char* lptr;
typedef __amdgpu_buffer_rsrc_t rsrc_t;

constexpr unsigned WS_Q = 0, WS_K = 2u << 20, WS_V = 4u << 20, WS_O = 6u << 20, WS_U = 8u << 20, WS_H = 10u << 20,
                   WS_G = 13u << 20, WS_FL = 14u << 20, WS_END = 15u << 20;
constexpr int FL_H = 0;
constexpr int FL_Q = 64 * 32;
constexpr int FL_U = FL_Q + 192 * 32;
constexpr int FL_O = FL_U + 352 * 32;
constexpr int FL_EPOCH = FL_O + 256 * 32;
constexpr float QSCALE = 0.25f * 1.4426950408889634f;
constexpr int NS = 7, SLOT = 16384, RING = NS * SLOT;
constexpr int L_K = RING, L_V = RING + 16384, L_X = RING + 32768  , L_C = L_X + 12288  , L_P = L_C + 1024  ;
constexpr int LDS_BYTES = 163840;
constexpr unsigned SPIN_CAP = 200000;
constexpr int KVS = 22;
constexpr int QDS = 10;
constexpr int ODS = 4;

#define MFMA32(a, b, c) __builtin_amdgcn_mfma_f32_32x32x16_bf16(a, b, c, 0, 0, 0)
#define MFMA16(a, b, c) __builtin_amdgcn_mfma_f32_16x16x32_bf16(a, b, c, 0, 0, 0)
#define SB() __builtin_amdgcn_sched_barrier(0)
#define LBAR() do { asm volatile("s_waitcnt lgkmcnt(0)\n\ts_barrier" ::: "memory"); SB(); } while (0)
#define CBAR(N) do { asm volatile("s_waitcnt lgkmcnt(" #N ")\n\ts_barrier" ::: "memory"); SB(); } while (0)
#define DBAR() do { asm volatile("s_waitcnt vmcnt(0) lgkmcnt(0)\n\ts_barrier" ::: "memory"); SB(); } while (0)
#define VBAR(N) asm volatile("s_waitcnt vmcnt(" #N ")\n\ts_barrier" ::: "memory")
#define BAR() asm volatile("s_barrier" ::: "memory")

#define STAMP(i) do {} while (0)
#define STAMP_OUT(ws, slot) do {} while (0)

DI int rfl(int x) { return __builtin_amdgcn_readfirstlane(x); }
DI bf16x8 ldsr128(lptr p) { return *(const LAS bf16x8*)p; }
DI s16x4 ldstr(lptr p) { return __builtin_amdgcn_ds_read_tr16_b64_v4i16((LAS s16x4*)p); }
DI bf16x8 cat8(s16x4 a, s16x4 b) { return __builtin_bit_cast(bf16x8, __builtin_shufflevector(a, b, 0, 1, 2, 3, 4, 5, 6, 7)); }
DI unsigned pk2(float a, float b) { f32x2 f = {a, b}; return __builtin_bit_cast(unsigned, __builtin_convertvector(f, bf16x2)); }
DI float bflo(unsigned u) { return __uint_as_float(u << 16); }
DI float bfhi(unsigned u) { return __uint_as_float(u & 0xffff0000u); }
DI int crow(int i, int h) { return (i & 3) + 8 * (i >> 2) + 4 * h; }
DI void st128(rsrc_t r, unsigned off, u32x4 v) { __builtin_amdgcn_raw_buffer_store_b128(v, r, off, 0, 16); }
DI void st64(rsrc_t r, unsigned off, u32x2 v) { __builtin_amdgcn_raw_buffer_store_b64(v, r, off, 0, 16); }
DI void st32(rsrc_t r, unsigned off, unsigned v) { __builtin_amdgcn_raw_buffer_store_b32(v, r, off, 0, 16); }
DI u32x4 ld128(rsrc_t r, unsigned off) { return __builtin_amdgcn_raw_buffer_load_b128(r, off, 0, 16); }
DI unsigned ld32(rsrc_t r, unsigned off) { return __builtin_amdgcn_raw_buffer_load_b32(r, off, 0, 16); }
DI void flag_st(char* ws, int idx, unsigned v) { __hip_atomic_store((GAS unsigned*)(ws + WS_FL) + idx, v, __ATOMIC_RELAXED, __HIP_MEMORY_SCOPE_AGENT); }

#define M0_IN "s_mov_b32 %0, m0\n\ts_mov_b32 m0, %1\n\ts_nop 2\n\t"
#define M0_OUT "s_mov_b32 m0, %0"
DI void dma_2x2(unsigned m0v, const void* b0, unsigned v0, unsigned v1, const void* b1, unsigned v2, unsigned v3) {
  unsigned sv;
  asm volatile(M0_IN "global_load_lds_dwordx4 %4, %2\n\tglobal_load_lds_dwordx4 %5, %2 offset:1024\n\t"
               "global_load_lds_dwordx4 %6, %3 offset:2048\n\tglobal_load_lds_dwordx4 %7, %3 offset:3072\n\t" M0_OUT
               : "=&s"(sv) : "s"(m0v), "s"(b0), "s"(b1), "v"(v0), "v"(v1), "v"(v2), "v"(v3) : "memory");
}
DI void dma_lin4_sc1(unsigned m0v, const void* b, unsigned vl) {
  unsigned sv;
  asm volatile(M0_IN "global_load_lds_dwordx4 %3, %2 sc1\n\tglobal_load_lds_dwordx4 %3, %2 offset:1024 sc1\n\t"
               "global_load_lds_dwordx4 %3, %2 offset:2048 sc1\n\tglobal_load_lds_dwordx4 %3, %2 offset:3072 sc1\n\t" M0_OUT
               : "=&s"(sv) : "s"(m0v), "s"(b), "v"(vl) : "memory");
}
DI void dma_g1(unsigned m0v, const void* wb, unsigned v0, unsigned v1, const void* hb_m2048, unsigned vl) {
  unsigned sv;
  asm volatile(M0_IN "global_load_lds_dwordx4 %4, %2\n\tglobal_load_lds_dwordx4 %5, %2 offset:1024\n\t"
               "global_load_lds_dwordx4 %6, %3 offset:2048 sc1\n\t"
               "s_mov_b32 exec_hi, 0\n\tglobal_load_lds_dwordx4 %6, %3 offset:3072 sc1\n\ts_mov_b32 exec_hi, -1\n\t" M0_OUT
               : "=&s"(sv) : "s"(m0v), "s"(wb), "s"(hb_m2048), "v"(v0), "v"(v1), "v"(vl) : "memory");
}
DI void dma_hh(unsigned m0v, const void* hb_m2048, unsigned vl) {
  unsigned sv;
  asm volatile(M0_IN "global_load_lds_dwordx4 %3, %2 offset:2048 sc1\n\t"
               "s_mov_b32 exec_hi, 0\n\tglobal_load_lds_dwordx4 %3, %2 offset:3072 sc1\n\ts_mov_b32 exec_hi, -1\n\t" M0_OUT
               : "=&s"(sv) : "s"(m0v), "s"(hb_m2048), "v"(vl) : "memory");
}
DI void dma_touch(unsigned m0v, const void* b, unsigned voff) {
  unsigned sv;
  asm volatile(M0_IN "global_load_lds_dword %3, %2\n\t" M0_OUT : "=&s"(sv) : "s"(m0v), "s"(b), "v"(voff) : "memory");
}
DI void dma_wh(unsigned m0v, const void* wb, unsigned v0, unsigned v1) {
  unsigned sv;
  asm volatile(M0_IN "global_load_lds_dwordx4 %3, %2\n\tglobal_load_lds_dwordx4 %4, %2 offset:1024\n\t" M0_OUT
               : "=&s"(sv) : "s"(m0v), "s"(wb), "v"(v0), "v"(v1) : "memory");
}
DI void dma_fh(unsigned m0v, const void* fb_m2048, unsigned vl) {
  unsigned sv;
  asm volatile(M0_IN "global_load_lds_dwordx4 %3, %2 offset:2048 sc1\n\tglobal_load_lds_dwordx4 %3, %2 offset:3072 sc1\n\t" M0_OUT
               : "=&s"(sv) : "s"(m0v), "s"(fb_m2048), "v"(vl) : "memory");
}

DI void spin1(rsrc_t r, unsigned off, unsigned tok) {
  unsigned n = 0;
#pragma nounroll
  do { const unsigned v = ld32(r, off); if (__all(v == tok)) break; __builtin_amdgcn_s_sleep(4); asm volatile("" ::: "memory"); } while (++n < SPIN_CAP);
  __builtin_amdgcn_fence(__ATOMIC_ACQUIRE, "wavefront");
}
DI void peek_issue(unsigned m0v, const void* ws, unsigned off) {
  unsigned sv;
  asm volatile(M0_IN "global_load_lds_dwordx4 %3, %2 sc1\n\t" M0_OUT : "=&s"(sv) : "s"(m0v), "s"(ws), "v"(off) : "memory");
}
DI void peek_check(lptr p, rsrc_t r, unsigned off, unsigned tok) {
  if (__all(*(const volatile LAS unsigned*)p == tok)) return;
  asm volatile("s_waitcnt vmcnt(0)" ::: "memory");
  if (__all(*(const volatile LAS unsigned*)p == tok)) return;
  spin1(r, off, tok);
}
#define RARE(c) __builtin_expect((c) != 0, 0)

DI float row16_sum(float v) {
#define DPPX(ctrl) v += __int_as_float(__builtin_amdgcn_update_dpp(0, __float_as_int(v), ctrl, 0xf, 0xf, true))
  DPPX(0xB1); DPPX(0x4E); DPPX(0x141); DPPX(0x140);
#undef DPPX
  return v;
}
DI float gelu_tanh(float u) {
  const float z = u * (0.7978845608028654f + 0.7978845608028654f * 0.044715f * u * u);
  const float e = __builtin_amdgcn_exp2f(z * (-2.0f * 1.4426950408889634f));
  return u * __builtin_amdgcn_rcpf(1.0f + e);
}
constexpr unsigned long long diag_mask(int i) {
  unsigned long long mk = 0;
  for (int l = 0; l < 64; ++l) if ((i & 3) + 8 * (i >> 2) + 4 * (l >> 5) <= (l & 31)) mk |= 1ull << l;
  return mk;
}
DI float max16(const f32x16& s, float m) {
#pragma unroll
  for (int i = 0; i < 16; i += 2) m = __builtin_fmaxf(__builtin_fmaxf(s[i], s[i + 1]), m);
  return m;
}

#define NEXT_SO() so = so == (NS - 1) * SLOT ? 0 : so + SLOT

template <bool ISV, class Hook, class Hook14, class Mid>
DI void qkv_consume(lptr L, int& so, int w, int lane, f32x16 (&acc)[2], Hook hook, Hook14 hook14, Mid mid) {
  const int r = lane & 31, h = lane >> 5, wm = w >> 2, wn = w & 3;
  int xa[4];
#pragma unroll
  for (int s = 0; s < 4; ++s) xa[s] = (wm * 2 + (r >> 4)) * 4096 + (r & 15) * 128 + (((2 * s + h) ^ ((r >> 1) & 7)) << 4);
  const int q = (lane & 15) >> 2, p = lane & 3, blk = (lane >> 4) & 1;
  const int wa = h * 4096 + 2048 + q * 256 + (((wn * 4 + blk * 2 + (p >> 1)) ^ (q << 2)) << 4) + (p & 1) * 8;
  bf16x8 xA[2][4], xB[2][4], wA[4], wB[4];
#define QM(X, W, mi, s) do { if (ISV) acc[mi] = MFMA32(X[mi][s], W[s], acc[mi]); else acc[mi] = MFMA32(W[s], X[mi][s], acc[mi]); SB(); } while (0)
#define QRX(X, mi, s) X[mi][s] = ldsr128(sl + xa[s])
#define QRW(W, mi, e) W[2 * mi + e] = cat8(ldstr(sl + wa + e * 8192), ldstr(sl + wa + e * 8192 + 1024))
#define QSTEP(mi, XN, WN, XP, WP, MM) do { lptr sl = L + so; NEXT_SO(); \
    if (MM) QM(XP, WP, 0, 2 * mi); \
    QRX(XN, mi, 0); QRX(XN, mi, 1); SB(); if (MM) QM(XP, WP, 1, 2 * mi); \
    QRX(XN, mi, 2); QRX(XN, mi, 3); QRW(WN, mi, 0); SB(); if (MM) QM(XP, WP, 0, 2 * mi + 1); \
    QRW(WN, mi, 1); SB(); if (MM) QM(XP, WP, 1, 2 * mi + 1); } while (0)
  LBAR(); QSTEP(0, xA, wA, xB, wB, false); QSTEP(1, xA, wA, xB, wB, false);
#pragma unroll 1
  for (int c = 1; c < 7; c += 2) {
    hook(c);     QSTEP(0, xB, wB, xA, wA, true); QSTEP(1, xB, wB, xA, wA, true);
    hook(c + 1); QSTEP(0, xA, wA, xB, wB, true); QSTEP(1, xA, wA, xB, wB, true);
  }
  hook14(); QSTEP(0, xB, wB, xA, wA, true); QSTEP(1, xB, wB, xA, wA, true);
#pragma unroll
  for (int s = 0; s < 4; ++s) QM(xB, wB, 0, s);
  mid();
#pragma unroll
  for (int s = 0; s < 4; ++s) QM(xB, wB, 1, s);
}

__global__ __launch_bounds__(768, 3) void fused_kernel(const char* __restrict__ x, const char* __restrict__ wqkv, const char* __restrict__ wo,
                                                       const char* __restrict__ w1, const char* __restrict__ b1, const char* __restrict__ w2,
                                                       const char* __restrict__ b2, const char* __restrict__ gw, char* __restrict__ ws,
                                                       char* __restrict__ out) {
  extern __shared__ __attribute__((aligned(16))) char smem[];
  asm volatile("" :: "s"(x), "s"(wqkv), "s"(wo), "s"(w1), "s"(b1), "s"(w2), "s"(b2), "s"(gw), "s"(ws), "s"(out));
  STAMP(0);
  const int b = blockIdx.x, xcd = b & 7, idx = ((b >> 3) + 24) & 31;
  const bool lead = b == 65;
  const int lane = threadIdx.x & 63, w = rfl(threadIdx.x >> 6);
  const unsigned lds0 = (unsigned)(uintptr_t)smem;
  lptr L = (lptr)smem;
  const int tm = (xcd >> 1) * 4 + idx / 6, tn = (xcd & 1) * 6 + idx % 6;
  const int lnb = xcd * 8 + (idx - 24);
  const unsigned x0 = (lane >> 3) * 1024 + (((lane & 7) ^ (lane >> 4)) << 4), x1 = (8 + (lane >> 3)) * 1024 + (((lane & 7) ^ (4 + (lane >> 4))) << 4) - 1024;
  const unsigned q0 = (lane >> 4) * 3072 + (((lane & 15) ^ ((lane >> 4) << 2)) << 4), q1 = q0 + 4 * 3072 - 1024;
  const char* xp = x + (size_t)(tm * 128 + ((w >> 1) & 1) * 64 + (w & 1) * 16) * 1024;
  const char* qp = wqkv + (size_t)((w & 3) * 8) * 3072 + tn * 256 - 2048;
  int kc = ((idx / 6 + idx % 6) % 3) * 3;
  if (w >= 8) {
    if (idx < 24) {
#pragma unroll 1
      for (int f = 0; f < 7; ++f) {
        dma_2x2(lds0 + (w & 3) * 4096 + f * SLOT, xp + kc * 128 + (f & 1) * 32 * 1024, x0, x1, qp + (size_t)(kc * 2 + (f & 1)) * (32 * 3072), q0, q1);
        kc = (kc + (f & 1)) & 7;
      }
    }
  }
  asm volatile("" ::: "memory");
  SB();
  const int gi = 31 - idx;
  const bool hasQ = idx < 24, hasG1 = xcd == 0 || gi < 30;
  const bool lowd = xcd == 0 ? gi < 12 : xcd < 5;
  const int gj = xcd == 0 && !lowd ? gi - 12 : gi;
  const int g1ft = lowd ? (xcd == 0 ? 20 : (xcd - 1) * 5) + gj / 6 : (xcd == 0 ? 18 : (xcd - 5) * 6) + gj / 5;
  const int g1dt = lowd ? gj % 6 : 6 + gj % 5;
  const int head = xcd * 4 + (idx & 3), qb = idx >> 2;
  const int st = (xcd >> 1) * 8 + (idx >> 2), dt = (xcd & 1) * 4 + (idx & 3);
  unsigned fq_off;
  { const int l = lane < 12 ? lane : 3, wh = l >> 2; int tmi = l & 3; if (wh == 0 || qb == 0) tmi |= 2;
    fq_off = WS_FL + (FL_Q + ((wh * 4 + (head >> 3)) * 16 + 2 * qb - 2 + tmi) * 32) * 4; }
  const unsigned fu_off = WS_FL + (FL_U + (((lane & 32) ? (4 * dt + 3) / 3 : (4 * dt) / 3) * 32 + min(lane & 31, 21)) * 32) * 4;
  const unsigned fo_off = WS_FL + (FL_O + ((st >> 2) * 32 + (lane & 31)) * 32) * 4;
  const unsigned fh_off = WS_FL + FL_H * 4 + lane * 128;
  const rsrc_t rws = __builtin_amdgcn_make_buffer_rsrc(ws, 0, WS_END, 0x00020000);
  volatile LAS unsigned* relay = (volatile LAS unsigned*)(L + L_C + 64);
  unsigned eoff = WS_FL + FL_EPOCH * 4;
  asm volatile("" : "+v"(eoff));
  const unsigned epoch = ld32(rws, eoff);
#define TOKEN_OF(e) ((e) * 1664525u + 1013904223u)

  if (w >= 8) {
    const int j = w - 8;
#define token TOKEN_OF(epoch)
    const unsigned m0b = lds0 + j * 4096;
    const unsigned vl = lane * 16;
    unsigned so = 0;
    const char* hp = ws + WS_H + (size_t)g1dt * 64 * 3072 + j * 1536 - 2048;
    const char* w1p = w1 + (size_t)(j * 16) * 2752 + g1ft * 128;
    const unsigned u0 = (lane >> 3) * 2752 + ((((lane & 7) ^ (((lane >> 4) & 3) << 1)) & (g1ft == 21 ? 3 : 7)) << 4), u1 = u0 + 8 * 2752 - 1024;
    int cw = lowd ? g1dt * 6 : (g1dt - 6) * 6 + 3, ch = cw;
    auto fill_g1 = [&]() { dma_g1(m0b + so, w1p + (size_t)cw * (64 * 2752), u0, u1, hp + cw * 6144, vl); cw = (cw + 1) & 31; NEXT_SO(); };
    auto fill_g1w = [&]() { dma_wh(m0b + so, w1p + (size_t)cw * (64 * 2752), u0, u1); cw = (cw + 1) & 31; NEXT_SO(); };
    auto fill_g1h3 = [&](unsigned so0) {
      const unsigned keep = so; so = so0;
#pragma unroll 1
      for (int i = 0; i < 3; ++i) { dma_hh(m0b + so, hp + ch * 6144, vl); ch = (ch + 1) & 31; NEXT_SO(); }
      so = keep;
    };
    const unsigned swz = ((lane & 7) ^ (((lane >> 4) & 1) << 2)) << 4;
    unsigned vw0 = (lane >> 3) * 4096 + swz, vw1 = vw0 + 8 * 4096 - 1024;
    const char* w2p = w2 + (size_t)(j * 16) * 4096 + st * 128;
    const char* up = ws + WS_U + (size_t)dt * 86 * 2048 + j * 2048 - 2048;
    const size_t w2half = (size_t)(j < 2 ? 21 * 64 : 20 * 64) * 4096;
    const int uhalf = j < 2 ? 21 * 8192 : 20 * 8192;
    auto fill_kv = [&]() {
      int t0 = qb * 8 - 8 + 4 * j; t0 = t0 < 0 ? t0 + 8 : t0;
      dma_lin4_sc1(lds0 + L_K + j * 4096, ws + WS_K + (size_t)(head * 64 + t0) * 1024, vl);
      dma_lin4_sc1(lds0 + L_V + j * 4096, ws + WS_V + (size_t)(head * 64 + t0) * 1024, vl);
    };
    int nwh = 0;
    const int r2 = (((dt & 3) * 2 + (dt >> 2)) * 21) >> 3;
    int c2 = r2;
    auto fill_wh = [&]() {
      dma_wh(m0b + so, nwh == 0 ? w2p + w2half : w2p + (size_t)c2 * (64 * 4096), vw0, vw1);
      if (nwh) c2 = c2 == 20 ? 0 : c2 + 1;
      ++nwh; NEXT_SO();
    };
    unsigned so_g2 = 0;
    auto warm_g2 = [&]() {
      if (j == 1) dma_touch(lds0 + L_P + 1024, wo + (size_t)(idx * 16) * 1024 + (xcd & 1) * 512, (lane >> 2) * 1024 + (lane & 3) * 128);
    };

    if (hasQ) {
      auto fill_a = [&]() { dma_2x2(m0b + so, xp + kc * 128, x0, x1, qp + (size_t)kc * 64 * 3072, q0, q1); NEXT_SO(); };
      auto fill_b = [&]() { dma_2x2(m0b + so, xp + kc * 128 + 32 * 1024, x0, x1, qp + (size_t)kc * 64 * 3072 + 32 * 3072, q0, q1); kc = (kc + 1) & 7; NEXT_SO(); };
      VBAR(20);
#pragma unroll 1
      for (int c = 1; c < 5; ++c) { VBAR(12); fill_b(); fill_a(); }
      VBAR(12); fill_b();
      if (hasG1) {
        const unsigned so0 = so;
        fill_g1w();
        VBAR(10); fill_g1w(); fill_g1w();
        VBAR(6); fill_g1h3(so0); fill_g1(); fill_g1();
      } else {
        VBAR(8); VBAR(0);
      }
    } else {
      BAR();
      VBAR(0);
#pragma unroll 1
      for (int s = 0; s < 3; ++s) fill_g1w();
      if (j == 0) spin1(rws, fh_off, token);
      BAR();
      fill_g1h3(0); fill_g1(); fill_g1();
    }
    STAMP(1);
    if (hasG1) {
      VBAR(12); fill_g1(); VBAR(14); fill_g1();
#pragma unroll 1
      for (int s = 2; s <= KVS; ++s) { VBAR(16); fill_g1(); }
      fill_kv();
#pragma unroll 1
      for (int s = KVS + 1; s < 27; ++s) { VBAR(16); fill_g1(); }
      so_g2 = so;
      VBAR(16); fill_wh(); VBAR(14); fill_wh(); VBAR(12); fill_wh(); VBAR(10); fill_wh(); VBAR(8); fill_wh();
      BAR(); fill_wh(); fill_wh();
      warm_g2();
      BAR();
      BAR();
    } else {
      BAR();
      so_g2 = so;
#pragma unroll 1
      for (int i = 0; i < 7; ++i) fill_wh();
      warm_g2();
      if (j == 0) spin1(rws, fq_off, token);
      BAR();
      fill_kv();
      VBAR(0);
    }
    STAMP(2);
    {
      if (j == 0) { spin1(rws, fu_off, token); *relay = 1u; }
      else { for (unsigned spins = 0; spins < 64 * SPIN_CAP && *relay == 0u; ++spins) __builtin_amdgcn_s_sleep(1); }
      STAMP(3);
      so = so_g2;
      dma_fh(m0b + so, up + uhalf, vl); NEXT_SO();
#pragma unroll 1
      for (int i = 0, c = r2; i < 6; ++i) { dma_fh(m0b + so, up + c * 8192, vl); c = c == 20 ? 0 : c + 1; NEXT_SO(); }
    }
    unsigned wsh = 18;
    int ci = 7;
    auto fill_g2 = [&]() {
      if (ci == 22) {
        w2p = wo + (size_t)(j * 16) * 1024 + dt * 128; up = ws + WS_O + (size_t)st * 32 * 2048 + j * 2048 - 2048; wsh = 16; c2 = 0;
        vw0 = (lane >> 3) * 1024 + swz; vw1 = vw0 + 8 * 1024 - 1024;
      }
      dma_wh(m0b + so, w2p + ((size_t)c2 << wsh), vw0, vw1); dma_fh(m0b + so, up + c2 * 8192, vl);
      c2 = c2 == 20 ? 0 : c2 + 1; NEXT_SO(); ++ci;
    };
    VBAR(12); VBAR(10);
    VBAR(8); fill_g2(); VBAR(10); fill_g2(); VBAR(12); fill_g2(); VBAR(14); fill_g2();
#pragma unroll 1
    for (int s = 6; s < 25; ++s) { VBAR(16); fill_g2(); }
    const unsigned fa_off = WS_FL + FL_O * 4 + (j * 64 + lane) * 128;
    if (lead) peek_issue(lds0 + L_X + j * 1024, ws, fa_off);
    VBAR(16); VBAR(12); VBAR(8); VBAR(4); VBAR(0);
    if (lead) peek_check(L + L_X + j * 1024 + lane * 16, rws, fa_off, token);
    BAR();
    if (lead && j == 0 && lane == 0) st32(rws, WS_FL + FL_EPOCH * 4, token);
    BAR();
    STAMP(4);
    if (j == 0) STAMP_OUT(ws, 1);
    return;
  }

#undef token
  unsigned token;
  u32x4 xv[4], gv[4];
  if (!hasQ) {
#pragma unroll
    for (int i = 0; i < 4; ++i) xv[i] = *(const u32x4*)(x + (size_t)(lnb * 32 + w * 4 + (lane >> 4)) * 1024 + (lane & 15) * 16 + i * 256);
#pragma unroll
    for (int i = 0; i < 4; ++i) gv[i] = *(const u32x4*)(gw + (lane & 15) * 16 + i * 256);
  }
  asm volatile("" ::: "memory");
  SB();
  const int r = lane & 31, h = lane >> 5;
  const int g4 = lane >> 4, q4 = (lane & 15) >> 2, p4 = lane & 3, blk = (lane >> 4) & 1;
  int so = 0;
  if (threadIdx.x == 4) *relay = 0u;
  if (threadIdx.x < 4) {
    const unsigned cv = (threadIdx.x & 1) ? 0u : 0x3f803f80u;
    *(LAS u32x4*)(L + L_C + (threadIdx.x >> 1) * 512 + (threadIdx.x & 1) * 16) = u32x4{cv, cv, cv, cv};
  }
  lptr pk = L + L_P + lane * 16;

  const int kh = w >> 2, fb = w & 3;
  int ha[3];
#pragma unroll
  for (int db = 0; db < 3; ++db) { const int o = (kh * 3 + db) * 1024 + lane * 16, rg = o / 1536; ha[db] = rg * 4096 + 2048 + (o - rg * 1536); }
  const int wa = kh * 8192 + (4 * g4 + q4) * 128 + ((fb ^ ((2 * g4 + (q4 >> 1)) & 3)) << 5) + p4 * 8;
  f32x4 acc[3] = {};
  bf16x8 wA = {}, hA[3] = {}, wB = {}, hB[3] = {};
#define G1RD(W, H) do { lptr sl = L + so; NEXT_SO(); W = cat8(ldstr(sl + wa), ldstr(sl + wa + 4096)); \
  _Pragma("unroll") for (int db = 0; db < 3; ++db) H[db] = ldsr128(sl + ha[db]); SB(); } while (0)
#define G1MM(W, H) do { _Pragma("unroll") for (int db = 0; db < 3; ++db) acc[db] = MFMA16(W, H[db], acc[db]); SB(); } while (0)

  token = TOKEN_OF(epoch);
  asm volatile("" : "+v"(token), "+v"(gv[3]));
  if (hasQ) {
    if (w == 7) *(LAS unsigned*)pk = ~token;
    STAMP(1);
    f32x16 qacc[2] = {};
    const bool poller = w == 7 && hasG1;
    const unsigned post = poller ? (1u << 4) | (1u << 5) | (1u << 6) : 0u;
    auto hook = [&](int s) {
      LBAR();
      if (RARE((post >> s) & 1)) peek_issue(lds0 + L_P, ws, fh_off);
      SB();
    };
    auto hook14 = [&]() { if (poller) peek_check(pk, rws, fh_off, token); LBAR(); };
    auto mid = [&]() { if (hasG1) { LBAR(); G1RD(wA, hA); } };
    if (tn >= 8) qkv_consume<true>(L, so, w, lane, qacc, hook, hook14, mid);
    else qkv_consume<false>(L, so, w, lane, qacc, hook, hook14, mid);
    STAMP(2);
    const int wm = w >> 2, wn = w & 3;
    const int hd0 = (tn & 3) * 8 + wn * 2;
    const float sc = tn < 4 ? QSCALE : 1.0f;
    const unsigned dst = tn >= 8 ? WS_V + (((hd0 + (r >> 4)) * 64 + tm * 4 + wm * 2) * 64 + h * 16 + (r & 15)) * 16
                                 : (tn < 4 ? WS_Q : WS_K) + ((hd0 * 64 + tm * 4 + wm * 2) * 64 + lane) * 16;
    const unsigned dstep = tn >= 8 ? 512 : 65536;
#pragma unroll
    for (int mi = 0; mi < 2; ++mi) {
#pragma unroll
      for (int s = 0; s < 2; ++s) {
        u32x4 v;
#pragma unroll
        for (int jj = 0; jj < 4; ++jj) v[jj] = pk2(qacc[mi][8 * s + 2 * jj] * sc, qacc[mi][8 * s + 2 * jj + 1] * sc);
        st128(rws, dst + mi * 1024 + s * dstep, v);
      }
      if (mi == 0 && hasG1) { SB(); LBAR(); G1RD(wB, hB); G1MM(wA, hA); }
    }
  } else {
    constexpr int RS = 1056;
    f32x2 v[16], s2 = {0.f, 0.f}, g2 = {0.f, 0.f};
#pragma unroll
    for (int i = 0; i < 16; ++i) {
      v[i] = f32x2{bflo(xv[i >> 2][i & 3]), bfhi(xv[i >> 2][i & 3])};
      s2 += v[i];
      g2 += v[i] * f32x2{bflo(gv[i >> 2][i & 3]), bfhi(gv[i >> 2][i & 3])};
    }
    const float mean = row16_sum(s2[0] + s2[1]) * (1.0f / 512), gs = row16_sum(g2[0] + g2[1]);
    f32x2 q2 = {0.f, 0.f};
#pragma unroll
    for (int i = 0; i < 16; ++i) { v[i] -= f32x2{mean, mean}; q2 += v[i] * v[i]; }
    const float rs = __builtin_amdgcn_rsqf(row16_sum(q2[0] + q2[1]) * (1.0f / 512) + 1e-5f);
#pragma unroll
    for (int i = 0; i < 4; ++i) {
      u32x4 ov;
#pragma unroll
      for (int jj = 0; jj < 4; ++jj) { const f32x2 t = v[4 * i + jj] * f32x2{rs, rs}; ov[jj] = pk2(t[0], t[1]); }
      *(LAS u32x4*)(L + (w * 4 + g4) * RS + (lane & 15) * 16 + i * 256) = ov;
    }
    if ((lane & 15) == 0) st32(rws, WS_G + (lnb * 32 + w * 4 + g4) * 4, __float_as_uint(1.0f / (1.0f + __expf(-gs))));
    STAMP(1);
    LBAR();
#pragma unroll
    for (int i = 0; i < 4; ++i) {
      const int D = w * 4 + i, d48 = D / 3;
      lptr a = L + (4 * g4 + q4) * RS + D * 32 + p4 * 8;
      const bf16x8 f = cat8(ldstr(a), ldstr(a + 16 * RS));
      st128(rws, WS_H + ((d48 * 64 + lnb) * 3 + (D - d48 * 3)) * 1024 + lane * 16, __builtin_bit_cast(u32x4, f));
      if (i == 3 && w == 7) st128(rws, WS_H + ((10 * 64 + lnb) * 3 + 2) * 1024 + lane * 16, __builtin_bit_cast(u32x4, f));
    }
    DBAR();
    if (threadIdx.x == 0) flag_st(ws, FL_H + lnb * 32, token);
    STAMP(2);
    LBAR();
    LBAR(); G1RD(wA, hA);
    LBAR(); G1RD(wB, hB); G1MM(wA, hA);
  }

  bf16x8 qf;
  if (hasG1) {
    const unsigned pre = (1u << QDS) | (w == 7 ? 1u << KVS : 0u);
    const unsigned post = (w == 0 && hasQ ? 1u << QDS : 0u) | (w == 7 ? (1u << QDS) | (1u << (KVS - 6)) | (1u << (KVS - 4)) | (1u << (KVS - 2)) : 0u);
    auto hook = [&](int s) {
      if (RARE((pre >> s) & 1)) { if (s == QDS) asm volatile("s_waitcnt vmcnt(0)" ::: "memory"); else peek_check(pk, rws, fq_off, token); }
      CBAR(5);
      if (RARE((post >> s) & 1)) {
        if (s == QDS) { if (w == 7) *(LAS unsigned*)pk = ~token; else if (lane == 0) flag_st(ws, FL_Q + (tn * 16 + tm) * 32, token); }
        else peek_issue(lds0 + L_P, ws, fq_off);
      }
      SB();
    };
#pragma unroll 1
    for (int s = 2; s < KVS + 2; s += 2) {
      hook(s); G1RD(wA, hA); G1MM(wB, hB);
      CBAR(5); G1RD(wB, hB); G1MM(wA, hA);
    }
    STAMP(10);
    qf = __builtin_bit_cast(bf16x8, ld128(rws, WS_Q + (head * 64 + qb * 8 + w) * 1024 + lane * 16));
    const int fblk = g1ft * 4 + fb;
    u32x2 bb = *(const u32x2*)(b1 + min(fblk * 16 + 4 * g4, 1372) * 2);
    unsigned ua[2];
#pragma unroll
    for (int e = 0; e < 2; ++e) {
      const int D = g1dt * 3 + (kh ? 2 : e);
      ua[e] = WS_U + ((((D >> 2) * 86 + fblk) * 2 + ((D >> 1) & 1)) * 64 + (g4 >> 1) * 32 + (D & 1) * 16 + (lane & 15)) * 16 + (g4 & 1) * 8;
      if (fblk >= 86 || D >= 32) ua[e] = WS_END;
    }
    lptr xw = L + L_X + (fb * 3 * 64 + lane) * 16;
#pragma unroll 1
    for (int s = KVS + 2; s < 32; s += 2) {
      CBAR(5); G1RD(wA, hA); G1MM(wB, hB);
      CBAR(5); G1RD(wB, hB); G1MM(wA, hA);
    }
    asm volatile("" : "+v"(qf), "+v"(bb), "+v"(ua[0]), "+v"(ua[1]));
    const f32x4 bv = {bflo(bb[0]), bfhi(bb[0]), bflo(bb[1]), bfhi(bb[1])};
    LBAR();
    G1MM(wB, hB);
    STAMP(3);
    if (kh == 1) { *(LAS f32x4*)(xw) = acc[0]; *(LAS f32x4*)(xw + 1024) = acc[1]; }
    else *(LAS f32x4*)(xw + 2048) = acc[2];
    LBAR();
    auto fin = [&](const f32x4& a, int off, unsigned addr) {
      const f32x4 t = a + *(const LAS f32x4*)(xw + off) + bv;
      st64(rws, addr, u32x2{pk2(gelu_tanh(t[0]), gelu_tanh(t[1])), pk2(gelu_tanh(t[2]), gelu_tanh(t[3]))});
    };
    if (kh == 1) fin(acc[2], 2048, ua[0]);
    else { fin(acc[0], 0, ua[0]); fin(acc[1], 1024, ua[1]); }
  } else {
    DBAR();
    if (threadIdx.x == 0) flag_st(ws, FL_Q + (tn * 16 + tm) * 32, token);
    LBAR();
    qf = __builtin_bit_cast(bf16x8, ld128(rws, WS_Q + (head * 64 + qb * 8 + w) * 1024 + lane * 16));
    asm volatile("" : "+v"(qf));
    LBAR();
    STAMP(3);
  }

  {
    lptr kp = L + L_K + w * 1024 + lane * 16;
    const int i_lo = qb > 0 ? 1 : 8 - w;
    const f32x16 zero = {};
    bf16x8 kf[9];
#pragma unroll
    for (int i = 0; i < 9; ++i) kf[i] = ldsr128(kp + i * 1024);
    f32x16 sb, sa, sc;
    float mx[4];
#define TMAX(S, i) (((i) >= i_lo) ? max16(S, -INFINITY) : -INFINITY)
    sa = MFMA32(kf[8], qf, zero); sc = MFMA32(kf[0], qf, zero);
    f32x16 s1 = MFMA32(kf[1], qf, zero), s2 = MFMA32(kf[2], qf, zero); SB();
#pragma unroll
    for (int i = 0; i < 16; ++i) sb[i] = __builtin_amdgcn_inverse_ballot_w64(diag_mask(i)) ? sa[i] : (qb > 0 ? sc[i] : -INFINITY);
    SB();
    sa = MFMA32(kf[3], qf, zero); sc = MFMA32(kf[4], qf, zero); SB();
    mx[0] = max16(sb, -INFINITY); mx[1] = TMAX(s1, 1); mx[2] = TMAX(s2, 2); SB();
    s1 = MFMA32(kf[5], qf, zero); s2 = MFMA32(kf[6], qf, zero); SB();
    mx[3] = TMAX(sa, 3); mx[0] = __builtin_fmaxf(mx[0], TMAX(sc, 4)); SB();
    sa = MFMA32(kf[7], qf, zero); SB();
    mx[1] = __builtin_fmaxf(mx[1], TMAX(s1, 5)); mx[2] = __builtin_fmaxf(mx[2], TMAX(s2, 6)); mx[3] = __builtin_fmaxf(mx[3], TMAX(sa, 7));
    float m = __builtin_fmaxf(__builtin_fmaxf(mx[0], mx[1]), __builtin_fmaxf(mx[2], mx[3]));
    {
      auto rr = __builtin_amdgcn_permlane32_swap(__float_as_uint(m), __float_as_uint(m), false, false);
      m = __builtin_fmaxf(__uint_as_float(rr[0]), __uint_as_float(rr[1]));
    }
    STAMP(11);
    if (hasG1) {
      DBAR();
      if (threadIdx.x == 0) flag_st(ws, FL_U + (g1dt * 32 + g1ft) * 32, token);
    }
    STAMP(4);
    const int vstr = r < 16 ? 1024 : 0;
    lptr vp0 = L + (r < 16 ? L_V + w * 1024 + h * 256 + r * 16 : (L_C + ((r == 16 || r == 20) ? 0 : 16)));
    f32x16 cin;
#pragma unroll
    for (int i = 0; i < 16; ++i) cin[i] = -m;
    f32x16 s = MFMA32(ldsr128(kp + i_lo * 1024), qf, cin);
    f32x16 o = {};
    {
      u32x4 pd[2], pf[2];
#pragma unroll
      for (int i = 0; i < 16; i += 2) {
        const unsigned pp = pk2(__builtin_amdgcn_exp2f(sb[i] - m), __builtin_amdgcn_exp2f(sb[i + 1] - m));
        const int dlt = r - crow(i, h);
        const unsigned mk = dlt >= 1 ? 0xffffffffu : (dlt == 0 ? 0x0000ffffu : 0u);
        pd[i >> 3][(i >> 1) & 3] = pp & mk;
        pf[i >> 3][(i >> 1) & 3] = pp & ~mk;
      }
      lptr vp = vp0 + 8 * vstr;
      o = MFMA32(ldsr128(vp), __builtin_bit_cast(bf16x8, pd[0]), o);
      o = MFMA32(ldsr128(vp + 512), __builtin_bit_cast(bf16x8, pd[1]), o);
      o = MFMA32(ldsr128(vp0), __builtin_bit_cast(bf16x8, pf[0]), o);
      o = MFMA32(ldsr128(vp0 + 512), __builtin_bit_cast(bf16x8, pf[1]), o);
    }
#pragma unroll 1
    for (int i = i_lo; i < 8; ++i) {
      lptr vp = vp0 + i * vstr;
      const bf16x8 kn = ldsr128(kp + (i + 1) * 1024), v0 = ldsr128(vp), v1 = ldsr128(vp + 512);
      u32x4 p0, p1;
#pragma unroll
      for (int e = 0; e < 8; e += 2) p0[e >> 1] = pk2(__builtin_amdgcn_exp2f(s[e]), __builtin_amdgcn_exp2f(s[e + 1]));
      o = MFMA32(v0, __builtin_bit_cast(bf16x8, p0), o);
#pragma unroll
      for (int e = 8; e < 16; e += 2) p1[(e >> 1) & 3] = pk2(__builtin_amdgcn_exp2f(s[e]), __builtin_amdgcn_exp2f(s[e + 1]));
      s = MFMA32(kn, qf, cin);
      o = MFMA32(v1, __builtin_bit_cast(bf16x8, p1), o);
    }
    const float il = __builtin_amdgcn_rcpf(o[8]);
    u32x4 ov;
#pragma unroll
    for (int jj = 0; jj < 4; ++jj) ov[jj] = pk2(o[2 * jj] * il, o[2 * jj + 1] * il);
    const int B = qb * 8 + w;
    st128(rws, WS_O + (((B >> 1) * 32 + head) * 2 + (B & 1)) * 1024 + lane * 16, ov);
  }
  STAMP(5);

  {
    const int mt = (w >> 1) & 1, nt = w & 1;
    const int tok = st * 64 + nt * 32 + r;
    const int ta2 = kh * 8192 + (8 * h + q4) * 128 + (((nt * 4 + blk * 2 + (p4 >> 1)) ^ ((q4 >> 1) << 2)) << 4) + (p4 & 1) * 8;
    const int tao = kh * 8192 + (4 * h + q4) * 128 + (((mt * 4 + blk * 2 + (p4 >> 1)) ^ ((q4 >> 1) << 2)) << 4) + (p4 & 1) * 8;
    const int fa2 = kh * 8192 + 2048 + mt * 1024 + lane * 16;
    const int fao = kh * 8192 + 2048 + nt * 1024 + lane * 16;
    f32x16 ay = {}, aa = {};
    bf16x8 pa0, pb0, pa1, pb1, qa0, qb0, qa1, qb1;
    float g = 0.f; unsigned b2v = 0;
#define RD2(A0, B0, A1, B1) do { lptr sl = L + so; NEXT_SO(); B0 = cat8(ldstr(sl + ta2), ldstr(sl + ta2 + 512)); A0 = ldsr128(sl + fa2); \
    B1 = cat8(ldstr(sl + ta2 + 4096), ldstr(sl + ta2 + 4096 + 512)); A1 = ldsr128(sl + fa2 + 4096); SB(); } while (0)
#define RDO(A0, B0, A1, B1) do { lptr sl = L + so; NEXT_SO(); A0 = cat8(ldstr(sl + tao), ldstr(sl + tao + 1024)); B0 = ldsr128(sl + fao); \
    A1 = cat8(ldstr(sl + tao + 4096), ldstr(sl + tao + 4096 + 1024)); B1 = ldsr128(sl + fao + 4096); SB(); } while (0)
#define MM2(ACC, A0, B0, A1, B1) do { ACC = MFMA32(A0, B0, ACC); ACC = MFMA32(A1, B1, ACC); SB(); } while (0)
    const unsigned pre = (1u << ODS) | (w == 7 ? 1u << 16 : 0u);
    const unsigned post = (w == 0 ? 1u << ODS : 0u) | (w == 7 ? (1u << ODS) | (1u << 10) | (1u << 12) | (1u << 14) : 0u);
    auto hook = [&](int s) {
      if (RARE((pre >> s) & 1)) { if (s == ODS) asm volatile("s_waitcnt vmcnt(0)" ::: "memory"); else peek_check(pk, rws, fo_off, token); }
      CBAR(6);
      if (RARE((post >> s) & 1)) {
        if (s == ODS) { if (w == 7) *(LAS unsigned*)pk = ~token; else if (lane == 0) flag_st(ws, FL_O + (qb * 32 + head) * 32, token); }
        else peek_issue(lds0 + L_P, ws, fo_off);
      }
      SB();
    };
    LBAR(); STAMP(6);
    RD2(pa0, pb0, pa1, pb1);
    if (kh) { pa0 = bf16x8{}; pa1 = bf16x8{}; pb0 = bf16x8{}; pb1 = bf16x8{}; }
#pragma unroll 1
    for (int s = 2; s < 22; s += 2) {
      CBAR(6); RD2(qa0, qb0, qa1, qb1); MM2(ay, pa0, pb0, pa1, pb1);
      hook(s); RD2(pa0, pb0, pa1, pb1); MM2(ay, qa0, qb0, qa1, qb1);
    }
    g = __uint_as_float(ld32(rws, WS_G + tok * 4));
    b2v = ((const unsigned short*)b2)[tok];
    CBAR(6); RD2(qa0, qb0, qa1, qb1); MM2(ay, pa0, pb0, pa1, pb1);
    STAMP(7);
    CBAR(6); RDO(pa0, pb0, pa1, pb1); MM2(ay, qa0, qb0, qa1, qb1);
#pragma unroll 1
    for (int s = 23; s < 29; s += 2) {
      CBAR(6); RDO(qa0, qb0, qa1, qb1); MM2(aa, pa0, pb0, pa1, pb1);
      CBAR(6); RDO(pa0, pb0, pa1, pb1); MM2(aa, qa0, qb0, qa1, qb1);
    }
    CBAR(6); RDO(qa0, qb0, qa1, qb1); MM2(aa, pa0, pb0, pa1, pb1);
    MM2(aa, qa0, qb0, qa1, qb1);
    STAMP(8);
    const float og = 1.0f - g;
    float v[16];
#pragma unroll
    for (int i = 0; i < 16; ++i) v[i] = ay[i] * og + aa[i] * g;
    LBAR();
    {
      lptr ex = L + w * 2048 + lane * 16;
      f32x4 s0, s1;
#pragma unroll
      for (int jj = 0; jj < 4; ++jj) { s0[jj] = kh ? v[jj] : v[8 + jj]; s1[jj] = kh ? v[4 + jj] : v[12 + jj]; }
      *(LAS f32x4*)(ex) = s0; *(LAS f32x4*)(ex + 1024) = s1;
    }
    LBAR();
    {
      lptr ex = L + (w ^ 4) * 2048 + lane * 16;
      const f32x4 r0 = *(const LAS f32x4*)(ex), r1 = *(const LAS f32x4*)(ex + 1024);
      const float bo = bflo(b2v) * og;
      float f[8];
#pragma unroll
      for (int jj = 0; jj < 4; ++jj) { f[jj] = (kh ? v[8 + jj] : v[jj]) + r0[jj] + bo; f[4 + jj] = (kh ? v[12 + jj] : v[4 + jj]) + r1[jj] + bo; }
      const unsigned a0 = pk2(f[0], f[1]), a1 = pk2(f[2], f[3]), c0 = pk2(f[4], f[5]), c1 = pk2(f[6], f[7]);
      auto x0 = __builtin_amdgcn_permlane32_swap(a0, c0, false, false);
      auto x1 = __builtin_amdgcn_permlane32_swap(a1, c1, false, false);
      const u32x4 ov = {x0[0], x1[0], x0[1], x1[1]};
      __builtin_amdgcn_raw_buffer_store_b128(ov, __builtin_amdgcn_make_buffer_rsrc(out, 0, 2048 * 1024, 0x00020000), tok * 1024 + (dt * 64 + mt * 32 + kh * 16 + h * 8) * 2, 0, 2);
    }
  }
  STAMP(9);
  if (w == 0) STAMP_OUT(ws, 0);
}

extern "C" void kernel_launch(void* const* d_in, const int* in_sizes, int n_in, void* d_out, int out_size, void* d_ws, size_t ws_size,
                              hipStream_t stream) {
  (void)hipFuncSetAttribute((const void*)fused_kernel, hipFuncAttributeMaxDynamicSharedMemorySize, LDS_BYTES);
  fused_kernel<<<256, 768, LDS_BYTES, stream>>>((const char*)d_in[0], (const char*)d_in[1], (const char*)d_in[2], (const char*)d_in[4],
                                                (const char*)d_in[5], (const char*)d_in[6], (const char*)d_in[7], (const char*)d_in[8],
                                                (char*)d_ws, (char*)d_out);
}
```

```cpp
#include <hip/hip_runtime.h>
#include <hip/hip_bf16.h>
#include <stdint.h>

#define LAS __attribute__((address_space(3)))
#define GAS __attribute__((address_space(1)))
#define DI __device__ __forceinline__
typedef __attribute__((ext_vector_type(8))) __bf16 bf16x8;
typedef __attribute__((ext_vector_type(2))) __bf16 bf16x2;
typedef __attribute__((ext_vector_type(4))) short s16x4;
typedef __attribute__((ext_vector_type(2))) float f32x2;
typedef __attribute__((ext_vector_type(4))) float f32x4;
typedef __attribute__((ext_vector_type(16))) float f32x16;
typedef __attribute__((ext_vector_type(2))) unsigned u32x2;
typedef __attribute__((ext_vector_type(4))) unsigned u32x4;
typedef LAS char* lptr;
typedef __amdgpu_buffer_rsrc_t rsrc_t;

constexpr unsigned WS_Q = 0, WS_K = 2u << 20, WS_V = 4u << 20, WS_O = 6u << 20, WS_U = 8u << 20, WS_H = 10u << 20,
                   WS_G = 13u << 20, WS_FL = 14u << 20, WS_END = 15u << 20;
constexpr int FL_H = 0;
constexpr int FL_Q = 64 * 32;
constexpr int FL_U = FL_Q + 192 * 32;
constexpr int FL_O = FL_U + 352 * 32;
constexpr int FL_EPOCH = FL_O + 256 * 32;
constexpr float QSCALE = 0.25f * 1.4426950408889634f;
constexpr int NS = 7, SLOT = 16384, RING = NS * SLOT;
constexpr int L_K = RING, L_V = RING + 16384, L_X = RING + 32768  , L_C = L_X + 12288  , L_P = L_C + 1024  ;
constexpr int LDS_BYTES = 163840;
constexpr unsigned SPIN_CAP = 200000;
constexpr int KVS = 22;
constexpr int QDS = 10;
constexpr int ODS = 4;

#define MFMA32(a, b, c) __builtin_amdgcn_mfma_f32_32x32x16_bf16(a, b, c, 0, 0, 0)
#define MFMA16(a, b, c) __builtin_amdgcn_mfma_f32_16x16x32_bf16(a, b, c, 0, 0, 0)
#define SB() __builtin_amdgcn_sched_barrier(0)
#define LBAR() do { asm volatile("s_waitcnt lgkmcnt(0)\n\ts_barrier" ::: "memory"); SB(); } while (0)
#define CBAR(N) do { asm volatile("s_waitcnt lgkmcnt(" #N ")\n\ts_barrier" ::: "memory"); SB(); } while (0)
#define DBAR() do { asm volatile("s_waitcnt vmcnt(0) lgkmcnt(0)\n\ts_barrier" ::: "memory"); SB(); } while (0)
#define VBAR(N) asm volatile("s_waitcnt vmcnt(" #N ")\n\ts_barrier" ::: "memory")
#define BAR() asm volatile("s_barrier" ::: "memory")

DI int rfl(int x) { return __builtin_amdgcn_readfirstlane(x); }
DI bf16x8 ldsr128(lptr p) { return *(const LAS bf16x8*)p; }
DI s16x4 ldstr(lptr p) { return __builtin_amdgcn_ds_read_tr16_b64_v4i16((LAS s16x4*)p); }
DI bf16x8 cat8(s16x4 a, s16x4 b) { return __builtin_bit_cast(bf16x8, __builtin_shufflevector(a, b, 0, 1, 2, 3, 4, 5, 6, 7)); }
DI unsigned pk2(float a, float b) { f32x2 f = {a, b}; return __builtin_bit_cast(unsigned, __builtin_convertvector(f, bf16x2)); }
DI float bflo(unsigned u) { return __uint_as_float(u << 16); }
DI float bfhi(unsigned u) { return __uint_as_float(u & 0xffff0000u); }
DI int crow(int i, int h) { return (i & 3) + 8 * (i >> 2) + 4 * h; }
DI void st128(rsrc_t r, unsigned off, u32x4 v) { __builtin_amdgcn_raw_buffer_store_b128(v, r, off, 0, 16); }
DI void st64(rsrc_t r, unsigned off, u32x2 v) { __builtin_amdgcn_raw_buffer_store_b64(v, r, off, 0, 16); }
DI void st32(rsrc_t r, unsigned off, unsigned v) { __builtin_amdgcn_raw_buffer_store_b32(v, r, off, 0, 16); }
DI u32x4 ld128(rsrc_t r, unsigned off) { return __builtin_amdgcn_raw_buffer_load_b128(r, off, 0, 16); }
DI unsigned ld32(rsrc_t r, unsigned off) { return __builtin_amdgcn_raw_buffer_load_b32(r, off, 0, 16); }
DI void flag_st(char* ws, int idx, unsigned v) { __hip_atomic_store((GAS unsigned*)(ws + WS_FL) + idx, v, __ATOMIC_RELAXED, __HIP_MEMORY_SCOPE_AGENT); }

#define M0_IN "s_mov_b32 %0, m0\n\ts_mov_b32 m0, %1\n\ts_nop 2\n\t"
#define M0_OUT "s_mov_b32 m0, %0"
DI void dma_2x2(unsigned m0v, const void* b0, unsigned v0, unsigned v1, const void* b1, unsigned v2, unsigned v3) {
  unsigned sv;
  asm volatile(M0_IN "global_load_lds_dwordx4 %4, %2\n\tglobal_load_lds_dwordx4 %5, %2 offset:1024\n\t"
               "global_load_lds_dwordx4 %6, %3 offset:2048\n\tglobal_load_lds_dwordx4 %7, %3 offset:3072\n\t" M0_OUT
               : "=&s"(sv) : "s"(m0v), "s"(b0), "s"(b1), "v"(v0), "v"(v1), "v"(v2), "v"(v3) : "memory");
}
DI void dma_lin4_sc1(unsigned m0v, const void* b, unsigned vl) {
  unsigned sv;
  asm volatile(M0_IN "global_load_lds_dwordx4 %3, %2 sc1\n\tglobal_load_lds_dwordx4 %3, %2 offset:1024 sc1\n\t"
               "global_load_lds_dwordx4 %3, %2 offset:2048 sc1\n\tglobal_load_lds_dwordx4 %3, %2 offset:3072 sc1\n\t" M0_OUT
               : "=&s"(sv) : "s"(m0v), "s"(b), "v"(vl) : "memory");
}
DI void dma_g1(unsigned m0v, const void* wb, unsigned v0, unsigned v1, const void* hb_m2048, unsigned vl) {
  unsigned sv;
  asm volatile(M0_IN "global_load_lds_dwordx4 %4, %2\n\tglobal_load_lds_dwordx4 %5, %2 offset:1024\n\t"
               "global_load_lds_dwordx4 %6, %3 offset:2048 sc1\n\t"
               "s_mov_b32 exec_hi, 0\n\tglobal_load_lds_dwordx4 %6, %3 offset:3072 sc1\n\ts_mov_b32 exec_hi, -1\n\t" M0_OUT
               : "=&s"(sv) : "s"(m0v), "s"(wb), "s"(hb_m2048), "v"(v0), "v"(v1), "v"(vl) : "memory");
}
DI void dma_hh(unsigned m0v, const void* hb_m2048, unsigned vl) {
  unsigned sv;
  asm volatile(M0_IN "global_load_lds_dwordx4 %3, %2 offset:2048 sc1\n\t"
               "s_mov_b32 exec_hi, 0\n\tglobal_load_lds_dwordx4 %3, %2 offset:3072 sc1\n\ts_mov_b32 exec_hi, -1\n\t" M0_OUT
               : "=&s"(sv) : "s"(m0v), "s"(hb_m2048), "v"(vl) : "memory");
}
DI void dma_touch(unsigned m0v, const void* b, unsigned voff) {
  unsigned sv;
  asm volatile(M0_IN "global_load_lds_dword %3, %2\n\t" M0_OUT : "=&s"(sv) : "s"(m0v), "s"(b), "v"(voff) : "memory");
}
DI void dma_wh(unsigned m0v, const void* wb, unsigned v0, unsigned v1) {
  unsigned sv;
  asm volatile(M0_IN "global_load_lds_dwordx4 %3, %2\n\tglobal_load_lds_dwordx4 %4, %2 offset:1024\n\t" M0_OUT
               : "=&s"(sv) : "s"(m0v), "s"(wb), "v"(v0), "v"(v1) : "memory");
}
DI void dma_fh(unsigned m0v, const void* fb_m2048, unsigned vl) {
  unsigned sv;
  asm volatile(M0_IN "global_load_lds_dwordx4 %3, %2 offset:2048 sc1\n\tglobal_load_lds_dwordx4 %3, %2 offset:3072 sc1\n\t" M0_OUT
               : "=&s"(sv) : "s"(m0v), "s"(fb_m2048), "v"(vl) : "memory");
}

DI void spin1(rsrc_t r, unsigned off, unsigned tok) {
  unsigned n = 0;
#pragma nounroll
  do { const unsigned v = ld32(r, off); if (__all(v == tok)) break; __builtin_amdgcn_s_sleep(4); asm volatile("" ::: "memory"); } while (++n < SPIN_CAP);
  __builtin_amdgcn_fence(__ATOMIC_ACQUIRE, "wavefront");
}
DI void peek_issue(unsigned m0v, const void* ws, unsigned off) {
  unsigned sv;
  asm volatile(M0_IN "global_load_lds_dwordx4 %3, %2 sc1\n\t" M0_OUT : "=&s"(sv) : "s"(m0v), "s"(ws), "v"(off) : "memory");
}
DI void peek_check(lptr p, rsrc_t r, unsigned off, unsigned tok) {
  if (__all(*(const volatile LAS unsigned*)p == tok)) return;
  asm volatile("s_waitcnt vmcnt(0)" ::: "memory");
  if (__all(*(const volatile LAS unsigned*)p == tok)) return;
  spin1(r, off, tok);
}
#define RARE(c) __builtin_expect((c) != 0, 0)

DI float row16_sum(float v) {
#define DPPX(ctrl) v += __int_as_float(__builtin_amdgcn_update_dpp(0, __float_as_int(v), ctrl, 0xf, 0xf, true))
  DPPX(0xB1); DPPX(0x4E); DPPX(0x141); DPPX(0x140);
#undef DPPX
  return v;
}
DI float gelu_tanh(float u) {
  const float z = u * (0.7978845608028654f + 0.7978845608028654f * 0.044715f * u * u);
  const float e = __builtin_amdgcn_exp2f(z * (-2.0f * 1.4426950408889634f));
  return u * __builtin_amdgcn_rcpf(1.0f + e);
}
constexpr unsigned long long diag_mask(int i) {
  unsigned long long mk = 0;
  for (int l = 0; l < 64; ++l) if ((i & 3) + 8 * (i >> 2) + 4 * (l >> 5) <= (l & 31)) mk |= 1ull << l;
  return mk;
}
DI float max16(const f32x16& s, float m) {
#pragma unroll
  for (int i = 0; i < 16; i += 2) m = __builtin_fmaxf(__builtin_fmaxf(s[i], s[i + 1]), m);
  return m;
}

#define NEXT_SO() so = so == (NS - 1) * SLOT ? 0 : so + SLOT

template <bool ISV, class Hook, class Hook14, class Mid>
DI void qkv_consume(lptr L, int& so, int w, int lane, f32x16 (&acc)[2], Hook hook, Hook14 hook14, Mid mid) {
  const int r = lane & 31, h = lane >> 5, wm = w >> 2, wn = w & 3;
  int xa[4];
#pragma unroll
  for (int s = 0; s < 4; ++s) xa[s] = (wm * 2 + (r >> 4)) * 4096 + (r & 15) * 128 + (((2 * s + h) ^ ((r >> 1) & 7)) << 4);
  const int q = (lane & 15) >> 2, p = lane & 3, blk = (lane >> 4) & 1;
  const int wa = h * 4096 + 2048 + q * 256 + (((wn * 4 + blk * 2 + (p >> 1)) ^ (q << 2)) << 4) + (p & 1) * 8;
  bf16x8 xA[2][4], xB[2][4], wA[4], wB[4];
#define QM(X, W, mi, s) do { if (ISV) acc[mi] = MFMA32(X[mi][s], W[s], acc[mi]); else acc[mi] = MFMA32(W[s], X[mi][s], acc[mi]); SB(); } while (0)
#define QRX(X, mi, s) X[mi][s] = ldsr128(sl + xa[s])
#define QRW(W, mi, e) W[2 * mi + e] = cat8(ldstr(sl + wa + e * 8192), ldstr(sl + wa + e * 8192 + 1024))
#define QSTEP(mi, XN, WN, XP, WP, MM) do { lptr sl = L + so; NEXT_SO(); \
    if (MM) QM(XP, WP, 0, 2 * mi); \
    QRX(XN, mi, 0); QRX(XN, mi, 1); SB(); if (MM) QM(XP, WP, 1, 2 * mi); \
    QRX(XN, mi, 2); QRX(XN, mi, 3); QRW(WN, mi, 0); SB(); if (MM) QM(XP, WP, 0, 2 * mi + 1); \
    QRW(WN, mi, 1); SB(); if (MM) QM(XP, WP, 1, 2 * mi + 1); } while (0)
  LBAR(); QSTEP(0, xA, wA, xB, wB, false); QSTEP(1, xA, wA, xB, wB, false);
#pragma unroll 1
  for (int c = 1; c < 7; c += 2) {
    hook(c);     QSTEP(0, xB, wB, xA, wA, true); QSTEP(1, xB, wB, xA, wA, true);
    hook(c + 1); QSTEP(0, xA, wA, xB, wB, true); QSTEP(1, xA, wA, xB, wB, true);
  }
  hook14(); QSTEP(0, xB, wB, xA, wA, true); QSTEP(1, xB, wB, xA, wA, true);
#pragma unroll
  for (int s = 0; s < 4; ++s) QM(xB, wB, 0, s);
  mid();
#pragma unroll
  for (int s = 0; s < 4; ++s) QM(xB, wB, 1, s);
}

__global__ __launch_bounds__(768, 3) void fused_kernel(const char* __restrict__ x, const char* __restrict__ wqkv, const char* __restrict__ wo,
                                                       const char* __restrict__ w1, const char* __restrict__ b1, const char* __restrict__ w2,
                                                       const char* __restrict__ b2, const char* __restrict__ gw, char* __restrict__ ws,
                                                       char* __restrict__ out) {
  extern __shared__ __attribute__((aligned(16))) char smem[];
  asm volatile("" :: "s"(x), "s"(wqkv), "s"(wo), "s"(w1), "s"(b1), "s"(w2), "s"(b2), "s"(gw), "s"(ws), "s"(out));
  const int b = blockIdx.x, xcd = b & 7, idx = ((b >> 3) + 24) & 31;
  const bool lead = b == 65;
  const int lane = threadIdx.x & 63, w = rfl(threadIdx.x >> 6);
  const unsigned lds0 = (unsigned)(uintptr_t)smem;
  lptr L = (lptr)smem;
  const int tm = (xcd >> 1) * 4 + idx / 6, tn = (xcd & 1) * 6 + idx % 6;
  const int lnb = xcd * 8 + (idx - 24);
  const unsigned x0 = (lane >> 3) * 1024 + (((lane & 7) ^ (lane >> 4)) << 4), x1 = (8 + (lane >> 3)) * 1024 + (((lane & 7) ^ (4 + (lane >> 4))) << 4) - 1024;
  const unsigned q0 = (lane >> 4) * 3072 + (((lane & 15) ^ ((lane >> 4) << 2)) << 4), q1 = q0 + 4 * 3072 - 1024;
  const char* xp = x + (size_t)(tm * 128 + ((w >> 1) & 1) * 64 + (w & 1) * 16) * 1024;
  const char* qp = wqkv + (size_t)((w & 3) * 8) * 3072 + tn * 256 - 2048;
  int kc = ((idx / 6 + idx % 6) % 3) * 3;
  if (w >= 8) {
    if (idx < 24) {
#pragma unroll 1
      for (int f = 0; f < 7; ++f) {
        dma_2x2(lds0 + (w & 3) * 4096 + f * SLOT, xp + kc * 128 + (f & 1) * 32 * 1024, x0, x1, qp + (size_t)(kc * 2 + (f & 1)) * (32 * 3072), q0, q1);
        kc = (kc + (f & 1)) & 7;
      }
    }
  }
  asm volatile("" ::: "memory");
  SB();
  const int gi = 31 - idx;
  const bool hasQ = idx < 24, hasG1 = xcd == 0 || gi < 30;
  const bool lowd = xcd == 0 ? gi < 12 : xcd < 5;
  const int gj = xcd == 0 && !lowd ? gi - 12 : gi;
  const int g1ft = lowd ? (xcd == 0 ? 20 : (xcd - 1) * 5) + gj / 6 : (xcd == 0 ? 18 : (xcd - 5) * 6) + gj / 5;
  const int g1dt = lowd ? gj % 6 : 6 + gj % 5;
  const int head = xcd * 4 + (idx & 3), qb = idx >> 2;
  const int st = (xcd >> 1) * 8 + (idx >> 2), dt = (xcd & 1) * 4 + (idx & 3);
  unsigned fq_off;
  { const int l = lane < 12 ? lane : 3, wh = l >> 2; int tmi = l & 3; if (wh == 0 || qb == 0) tmi |= 2;
    fq_off = WS_FL + (FL_Q + ((wh * 4 + (head >> 3)) * 16 + 2 * qb - 2 + tmi) * 32) * 4; }
  const unsigned fu_off = WS_FL + (FL_U + (((lane & 32) ? (4 * dt + 3) / 3 : (4 * dt) / 3) * 32 + min(lane & 31, 21)) * 32) * 4;
  const unsigned fo_off = WS_FL + (FL_O + ((st >> 2) * 32 + (lane & 31)) * 32) * 4;
  const unsigned fh_off = WS_FL + FL_H * 4 + lane * 128;
  const rsrc_t rws = __builtin_amdgcn_make_buffer_rsrc(ws, 0, WS_END, 0x00020000);
  volatile LAS unsigned* relay = (volatile LAS unsigned*)(L + L_C + 64);
  unsigned eoff = WS_FL + FL_EPOCH * 4;
  asm volatile("" : "+v"(eoff));
  const unsigned epoch = ld32(rws, eoff);
#define TOKEN_OF(e) ((e) * 1664525u + 1013904223u)

  if (w >= 8) {
    const int j = w - 8;
#define token TOKEN_OF(epoch)
    const unsigned m0b = lds0 + j * 4096;
    const unsigned vl = lane * 16;
    unsigned so = 0;
    const char* hp = ws + WS_H + (size_t)g1dt * 64 * 3072 + j * 1536 - 2048;
    const char* w1p = w1 + (size_t)(j * 16) * 2752 + g1ft * 128;
    const unsigned u0 = (lane >> 3) * 2752 + ((((lane & 7) ^ (((lane >> 4) & 3) << 1)) & (g1ft == 21 ? 3 : 7)) << 4), u1 = u0 + 8 * 2752 - 1024;
    int cw = lowd ? g1dt * 6 : (g1dt - 6) * 6 + 3, ch = cw;
    auto fill_g1 = [&]() { dma_g1(m0b + so, w1p + (size_t)cw * (64 * 2752), u0, u1, hp + cw * 6144, vl); cw = (cw + 1) & 31; NEXT_SO(); };
    auto fill_g1w = [&]() { dma_wh(m0b + so, w1p + (size_t)cw * (64 * 2752), u0, u1); cw = (cw + 1) & 31; NEXT_SO(); };
    auto fill_g1h3 = [&](unsigned so0) {
      const unsigned keep = so; so = so0;
#pragma unroll 1
      for (int i = 0; i < 3; ++i) { dma_hh(m0b + so, hp + ch * 6144, vl); ch = (ch + 1) & 31; NEXT_SO(); }
      so = keep;
    };
    const unsigned swz = ((lane & 7) ^ (((lane >> 4) & 1) << 2)) << 4;
    unsigned vw0 = (lane >> 3) * 4096 + swz, vw1 = vw0 + 8 * 4096 - 1024;
    const char* w2p = w2 + (size_t)(j * 16) * 4096 + st * 128;
    const char* up = ws + WS_U + (size_t)dt * 86 * 2048 + j * 2048 - 2048;
    const size_t w2half = (size_t)(j < 2 ? 21 * 64 : 20 * 64) * 4096;
    const int uhalf = j < 2 ? 21 * 8192 : 20 * 8192;
    auto fill_kv = [&]() {
      int t0 = qb * 8 - 8 + 4 * j; t0 = t0 < 0 ? t0 + 8 : t0;
      dma_lin4_sc1(lds0 + L_K + j * 4096, ws + WS_K + (size_t)(head * 64 + t0) * 1024, vl);
      dma_lin4_sc1(lds0 + L_V + j * 4096, ws + WS_V + (size_t)(head * 64 + t0) * 1024, vl);
    };
    int nwh = 0;
    const int r2 = (((dt & 3) * 2 + (dt >> 2)) * 21) >> 3;
    int c2 = r2;
    auto fill_wh = [&]() {
      dma_wh(m0b + so, nwh == 0 ? w2p + w2half : w2p + (size_t)c2 * (64 * 4096), vw0, vw1);
      if (nwh) c2 = c2 == 20 ? 0 : c2 + 1;
      ++nwh; NEXT_SO();
    };
    unsigned so_g2 = 0;
    auto warm_g2 = [&]() {
      if (j == 1) dma_touch(lds0 + L_P + 1024, wo + (size_t)(idx * 16) * 1024 + (xcd & 1) * 512, (lane >> 2) * 1024 + (lane & 3) * 128);
    };

    if (hasQ) {
      auto fill_a = [&]() { dma_2x2(m0b + so, xp + kc * 128, x0, x1, qp + (size_t)kc * 64 * 3072, q0, q1); NEXT_SO(); };
      auto fill_b = [&]() { dma_2x2(m0b + so, xp + kc * 128 + 32 * 1024, x0, x1, qp + (size_t)kc * 64 * 3072 + 32 * 3072, q0, q1); kc = (kc + 1) & 7; NEXT_SO(); };
      VBAR(20);
#pragma unroll 1
      for (int c = 1; c < 5; ++c) { VBAR(12); fill_b(); fill_a(); }
      VBAR(12); fill_b();
      if (hasG1) {
        const unsigned so0 = so;
        fill_g1w();
        VBAR(10); fill_g1w(); fill_g1w();
        VBAR(6); fill_g1h3(so0); fill_g1(); fill_g1();
      } else {
        VBAR(8); VBAR(0);
      }
    } else {
      BAR();
      VBAR(0);
#pragma unroll 1
      for (int s = 0; s < 3; ++s) fill_g1w();
      if (j == 0) spin1(rws, fh_off, token);
      BAR();
      fill_g1h3(0); fill_g1(); fill_g1();
    }
    if (hasG1) {
      VBAR(12); fill_g1(); VBAR(14); fill_g1();
#pragma unroll 1
      for (int s = 2; s <= KVS; ++s) { VBAR(16); fill_g1(); }
      fill_kv();
#pragma unroll 1
      for (int s = KVS + 1; s < 27; ++s) { VBAR(16); fill_g1(); }
      so_g2 = so;
      VBAR(16); fill_wh(); VBAR(14); fill_wh(); VBAR(12); fill_wh(); VBAR(10); fill_wh(); VBAR(8); fill_wh();
      BAR(); fill_wh(); fill_wh();
      warm_g2();
      BAR();
      BAR();
    } else {
      BAR();
      so_g2 = so;
#pragma unroll 1
      for (int i = 0; i < 7; ++i) fill_wh();
      warm_g2();
      if (j == 0) spin1(rws, fq_off, token);
      BAR();
      fill_kv();
      VBAR(0);
    }
    unsigned tokl = token;
    asm volatile("" : "+v"(tokl));
    {
      if (j == 0) { spin1(rws, fu_off, tokl); *relay = 1u; }
      else { for (unsigned spins = 0; spins < 64 * SPIN_CAP && *relay == 0u; ++spins) __builtin_amdgcn_s_sleep(1); }
      so = so_g2;
      dma_fh(m0b + so, up + uhalf, vl); NEXT_SO();
#pragma unroll 1
      for (int i = 0, c = r2; i < 6; ++i) { dma_fh(m0b + so, up + c * 8192, vl); c = c == 20 ? 0 : c + 1; NEXT_SO(); }
    }
    unsigned wsh = 18;
    int ci = 7;
    auto fill_g2 = [&]() {
      if (ci == 22) {
        w2p = wo + (size_t)(j * 16) * 1024 + dt * 128; up = ws + WS_O + (size_t)st * 32 * 2048 + j * 2048 - 2048; wsh = 16; c2 = 0;
        vw0 = (lane >> 3) * 1024 + swz; vw1 = vw0 + 8 * 1024 - 1024;
      }
      dma_wh(m0b + so, w2p + ((size_t)c2 << wsh), vw0, vw1); dma_fh(m0b + so, up + c2 * 8192, vl);
      c2 = c2 == 20 ? 0 : c2 + 1; NEXT_SO(); ++ci;
    };
    VBAR(12); VBAR(10);
    VBAR(8); fill_g2(); VBAR(10); fill_g2(); VBAR(12); fill_g2(); VBAR(14); fill_g2();
    const unsigned fa_off = WS_FL + FL_O * 4 + (j * 64 + lane) * 128;
    lptr pa = L + L_X + j * 1024 + lane * 16;
    if (lead) *(volatile LAS unsigned*)pa = ~tokl;
#pragma unroll 1
    for (int s = 6; s < 20; ++s) { VBAR(16); fill_g2(); }
    if (lead) peek_issue(lds0 + L_X + j * 1024, ws, fa_off);
#pragma unroll 1
    for (int s = 20; s < 25; ++s) { VBAR(16); fill_g2(); }
    VBAR(16);
    if (lead) peek_check(pa, rws, fa_off, tokl);
    VBAR(12); VBAR(8); VBAR(4); VBAR(0);
    BAR();
    return;
  }

#undef token
  unsigned token;
  u32x4 xv[4], gv[4];
  if (!hasQ) {
#pragma unroll
    for (int i = 0; i < 4; ++i) xv[i] = *(const u32x4*)(x + (size_t)(lnb * 32 + w * 4 + (lane >> 4)) * 1024 + (lane & 15) * 16 + i * 256);
#pragma unroll
    for (int i = 0; i < 4; ++i) gv[i] = *(const u32x4*)(gw + (lane & 15) * 16 + i * 256);
  }
  asm volatile("" ::: "memory");
  SB();
  const int r = lane & 31, h = lane >> 5;
  const int g4 = lane >> 4, q4 = (lane & 15) >> 2, p4 = lane & 3, blk = (lane >> 4) & 1;
  int so = 0;
  if (threadIdx.x == 4) *relay = 0u;
  if (threadIdx.x < 4) {
    const unsigned cv = (threadIdx.x & 1) ? 0u : 0x3f803f80u;
    *(LAS u32x4*)(L + L_C + (threadIdx.x >> 1) * 512 + (threadIdx.x & 1) * 16) = u32x4{cv, cv, cv, cv};
  }
  lptr pk = L + L_P + lane * 16;

  const int kh = w >> 2, fb = w & 3;
  int ha[3];
#pragma unroll
  for (int db = 0; db < 3; ++db) { const int o = (kh * 3 + db) * 1024 + lane * 16, rg = o / 1536; ha[db] = rg * 4096 + 2048 + (o - rg * 1536); }
  const int wa = kh * 8192 + (4 * g4 + q4) * 128 + ((fb ^ ((2 * g4 + (q4 >> 1)) & 3)) << 5) + p4 * 8;
  f32x4 acc[3] = {};
  bf16x8 wA = {}, hA[3] = {}, wB = {}, hB[3] = {};
#define G1RD(W, H) do { lptr sl = L + so; NEXT_SO(); W = cat8(ldstr(sl + wa), ldstr(sl + wa + 4096)); \
  _Pragma("unroll") for (int db = 0; db < 3; ++db) H[db] = ldsr128(sl + ha[db]); SB(); } while (0)
#define G1MM(W, H) do { _Pragma("unroll") for (int db = 0; db < 3; ++db) acc[db] = MFMA16(W, H[db], acc[db]); SB(); } while (0)

  token = TOKEN_OF(epoch);
  asm volatile("" : "+v"(token), "+v"(gv[3]));
  if (hasQ) {
    if (w == 7) *(LAS unsigned*)pk = ~token;
    f32x16 qacc[2] = {};
    const bool poller = w == 7 && hasG1;
    const unsigned post = poller ? (1u << 4) | (1u << 5) | (1u << 6) : 0u;
    auto hook = [&](int s) {
      LBAR();
      if (RARE((post >> s) & 1)) peek_issue(lds0 + L_P, ws, fh_off);
      SB();
    };
    auto hook14 = [&]() { if (poller) peek_check(pk, rws, fh_off, token); LBAR(); };
    auto mid = [&]() { if (hasG1) { LBAR(); G1RD(wA, hA); } };
    if (tn >= 8) qkv_consume<true>(L, so, w, lane, qacc, hook, hook14, mid);
    else qkv_consume<false>(L, so, w, lane, qacc, hook, hook14, mid);
    const int wm = w >> 2, wn = w & 3;
    const int hd0 = (tn & 3) * 8 + wn * 2;
    const float sc = tn < 4 ? QSCALE : 1.0f;
    const unsigned dst = tn >= 8 ? WS_V + (((hd0 + (r >> 4)) * 64 + tm * 4 + wm * 2) * 64 + h * 16 + (r & 15)) * 16
                                 : (tn < 4 ? WS_Q : WS_K) + ((hd0 * 64 + tm * 4 + wm * 2) * 64 + lane) * 16;
    const unsigned dstep = tn >= 8 ? 512 : 65536;
#pragma unroll
    for (int mi = 0; mi < 2; ++mi) {
#pragma unroll
      for (int s = 0; s < 2; ++s) {
        u32x4 v;
#pragma unroll
        for (int jj = 0; jj < 4; ++jj) v[jj] = pk2(qacc[mi][8 * s + 2 * jj] * sc, qacc[mi][8 * s + 2 * jj + 1] * sc);
        st128(rws, dst + mi * 1024 + s * dstep, v);
      }
      if (mi == 0 && hasG1) { SB(); LBAR(); G1RD(wB, hB); G1MM(wA, hA); }
    }
  } else {
    constexpr int RS = 1056;
    f32x2 v[16], s2 = {0.f, 0.f}, g2 = {0.f, 0.f};
#pragma unroll
    for (int i = 0; i < 16; ++i) {
      v[i] = f32x2{bflo(xv[i >> 2][i & 3]), bfhi(xv[i >> 2][i & 3])};
      s2 += v[i];
      g2 += v[i] * f32x2{bflo(gv[i >> 2][i & 3]), bfhi(gv[i >> 2][i & 3])};
    }
    const float mean = row16_sum(s2[0] + s2[1]) * (1.0f / 512), gs = row16_sum(g2[0] + g2[1]);
    f32x2 q2 = {0.f, 0.f};
#pragma unroll
    for (int i = 0; i < 16; ++i) { v[i] -= f32x2{mean, mean}; q2 += v[i] * v[i]; }
    const float rs = __builtin_amdgcn_rsqf(row16_sum(q2[0] + q2[1]) * (1.0f / 512) + 1e-5f);
#pragma unroll
    for (int i = 0; i < 4; ++i) {
      u32x4 ov;
#pragma unroll
      for (int jj = 0; jj < 4; ++jj) { const f32x2 t = v[4 * i + jj] * f32x2{rs, rs}; ov[jj] = pk2(t[0], t[1]); }
      *(LAS u32x4*)(L + (w * 4 + g4) * RS + (lane & 15) * 16 + i * 256) = ov;
    }
    if ((lane & 15) == 0) st32(rws, WS_G + (lnb * 32 + w * 4 + g4) * 4, __float_as_uint(1.0f / (1.0f + __expf(-gs))));
    LBAR();
#pragma unroll
    for (int i = 0; i < 4; ++i) {
      const int D = w * 4 + i, d48 = D / 3;
      lptr a = L + (4 * g4 + q4) * RS + D * 32 + p4 * 8;
      const bf16x8 f = cat8(ldstr(a), ldstr(a + 16 * RS));
      st128(rws, WS_H + ((d48 * 64 + lnb) * 3 + (D - d48 * 3)) * 1024 + lane * 16, __builtin_bit_cast(u32x4, f));
      if (i == 3 && w == 7) st128(rws, WS_H + ((10 * 64 + lnb) * 3 + 2) * 1024 + lane * 16, __builtin_bit_cast(u32x4, f));
    }
    DBAR();
    if (threadIdx.x == 0) flag_st(ws, FL_H + lnb * 32, token);
    LBAR();
    LBAR(); G1RD(wA, hA);
    LBAR(); G1RD(wB, hB); G1MM(wA, hA);
  }

  bf16x8 qf;
  if (hasG1) {
    const unsigned pre = (1u << QDS) | (w == 7 ? 1u << KVS : 0u);
    const unsigned post = (w == 0 && hasQ ? 1u << QDS : 0u) | (w == 7 ? (1u << QDS) | (1u << (KVS - 6)) | (1u << (KVS - 4)) | (1u << (KVS - 2)) : 0u);
    auto hook = [&](int s) {
      if (RARE((pre >> s) & 1)) { if (s == QDS) asm volatile("s_waitcnt vmcnt(0)" ::: "memory"); else peek_check(pk, rws, fq_off, token); }
      CBAR(5);
      if (RARE((post >> s) & 1)) {
        if (s == QDS) { if (w == 7) *(LAS unsigned*)pk = ~token; else if (lane == 0) flag_st(ws, FL_Q + (tn * 16 + tm) * 32, token); }
        else peek_issue(lds0 + L_P, ws, fq_off);
      }
      SB();
    };
#pragma unroll 1
    for (int s = 2; s < KVS + 2; s += 2) {
      hook(s); G1RD(wA, hA); G1MM(wB, hB);
      CBAR(5); G1RD(wB, hB); G1MM(wA, hA);
    }
    qf = __builtin_bit_cast(bf16x8, ld128(rws, WS_Q + (head * 64 + qb * 8 + w) * 1024 + lane * 16));
    const int fblk = g1ft * 4 + fb;
    u32x2 bb = *(const u32x2*)(b1 + min(fblk * 16 + 4 * g4, 1372) * 2);
    unsigned ua[2];
#pragma unroll
    for (int e = 0; e < 2; ++e) {
      const int D = g1dt * 3 + (kh ? 2 : e);
      ua[e] = WS_U + ((((D >> 2) * 86 + fblk) * 2 + ((D >> 1) & 1)) * 64 + (g4 >> 1) * 32 + (D & 1) * 16 + (lane & 15)) * 16 + (g4 & 1) * 8;
      if (fblk >= 86 || D >= 32) ua[e] = WS_END;
    }
    lptr xw = L + L_X + (fb * 3 * 64 + lane) * 16;
#pragma unroll 1
    for (int s = KVS + 2; s < 32; s += 2) {
      CBAR(5); G1RD(wA, hA); G1MM(wB, hB);
      CBAR(5); G1RD(wB, hB); G1MM(wA, hA);
    }
    asm volatile("" : "+v"(qf), "+v"(bb), "+v"(ua[0]), "+v"(ua[1]));
    const f32x4 bv = {bflo(bb[0]), bfhi(bb[0]), bflo(bb[1]), bfhi(bb[1])};
    LBAR();
    G1MM(wB, hB);
    if (kh == 1) { *(LAS f32x4*)(xw) = acc[0]; *(LAS f32x4*)(xw + 1024) = acc[1]; }
    else *(LAS f32x4*)(xw + 2048) = acc[2];
    LBAR();
    auto fin = [&](const f32x4& a, int off, unsigned addr) {
      const f32x4 t = a + *(const LAS f32x4*)(xw + off) + bv;
      st64(rws, addr, u32x2{pk2(gelu_tanh(t[0]), gelu_tanh(t[1])), pk2(gelu_tanh(t[2]), gelu_tanh(t[3]))});
    };
    if (kh == 1) fin(acc[2], 2048, ua[0]);
    else { fin(acc[0], 0, ua[0]); fin(acc[1], 1024, ua[1]); }
  } else {
    DBAR();
    if (threadIdx.x == 0) flag_st(ws, FL_Q + (tn * 16 + tm) * 32, token);
    LBAR();
    qf = __builtin_bit_cast(bf16x8, ld128(rws, WS_Q + (head * 64 + qb * 8 + w) * 1024 + lane * 16));
    asm volatile("" : "+v"(qf));
    LBAR();
  }

  {
    lptr kp = L + L_K + w * 1024 + lane * 16;
    const int i_lo = qb > 0 ? 1 : 8 - w;
    const f32x16 zero = {};
    bf16x8 kf[9];
#pragma unroll
    for (int i = 0; i < 9; ++i) kf[i] = ldsr128(kp + i * 1024);
    f32x16 sb, sa, sc;
    float mx[4];
#define TMAX(S, i) (((i) >= i_lo) ? max16(S, -INFINITY) : -INFINITY)
    sa = MFMA32(kf[8], qf, zero); sc = MFMA32(kf[0], qf, zero);
    f32x16 s1 = MFMA32(kf[1], qf, zero), s2 = MFMA32(kf[2], qf, zero); SB();
#pragma unroll
    for (int i = 0; i < 16; ++i) sb[i] = __builtin_amdgcn_inverse_ballot_w64(diag_mask(i)) ? sa[i] : (qb > 0 ? sc[i] : -INFINITY);
    SB();
    sa = MFMA32(kf[3], qf, zero); sc = MFMA32(kf[4], qf, zero); SB();
    mx[0] = max16(sb, -INFINITY); mx[1] = TMAX(s1, 1); mx[2] = TMAX(s2, 2); SB();
    s1 = MFMA32(kf[5], qf, zero); s2 = MFMA32(kf[6], qf, zero); SB();
    mx[3] = TMAX(sa, 3); mx[0] = __builtin_fmaxf(mx[0], TMAX(sc, 4)); SB();
    sa = MFMA32(kf[7], qf, zero); SB();
    mx[1] = __builtin_fmaxf(mx[1], TMAX(s1, 5)); mx[2] = __builtin_fmaxf(mx[2], TMAX(s2, 6)); mx[3] = __builtin_fmaxf(mx[3], TMAX(sa, 7));
    float m = __builtin_fmaxf(__builtin_fmaxf(mx[0], mx[1]), __builtin_fmaxf(mx[2], mx[3]));
    {
      auto rr = __builtin_amdgcn_permlane32_swap(__float_as_uint(m), __float_as_uint(m), false, false);
      m = __builtin_fmaxf(__uint_as_float(rr[0]), __uint_as_float(rr[1]));
    }
    if (hasG1) {
      DBAR();
      if (threadIdx.x == 0) flag_st(ws, FL_U + (g1dt * 32 + g1ft) * 32, token);
    }
    const int vstr = r < 16 ? 1024 : 0;
    lptr vp0 = L + (r < 16 ? L_V + w * 1024 + h * 256 + r * 16 : (L_C + ((r == 16 || r == 20) ? 0 : 16)));
    f32x16 cin;
#pragma unroll
    for (int i = 0; i < 16; ++i) cin[i] = -m;
    f32x16 s = MFMA32(ldsr128(kp + i_lo * 1024), qf, cin);
    f32x16 o = {};
    {
      u32x4 pd[2], pf[2];
#pragma unroll
      for (int i = 0; i < 16; i += 2) {
        const unsigned pp = pk2(__builtin_amdgcn_exp2f(sb[i] - m), __builtin_amdgcn_exp2f(sb[i + 1] - m));
        const int dlt = r - crow(i, h);
        const unsigned mk = dlt >= 1 ? 0xffffffffu : (dlt == 0 ? 0x0000ffffu : 0u);
        pd[i >> 3][(i >> 1) & 3] = pp & mk;
        pf[i >> 3][(i >> 1) & 3] = pp & ~mk;
      }
      lptr vp = vp0 + 8 * vstr;
      o = MFMA32(ldsr128(vp), __builtin_bit_cast(bf16x8, pd[0]), o);
      o = MFMA32(ldsr128(vp + 512), __builtin_bit_cast(bf16x8, pd[1]), o);
      o = MFMA32(ldsr128(vp0), __builtin_bit_cast(bf16x8, pf[0]), o);
      o = MFMA32(ldsr128(vp0 + 512), __builtin_bit_cast(bf16x8, pf[1]), o);
    }
#pragma unroll 1
    for (int i = i_lo; i < 8; ++i) {
      lptr vp = vp0 + i * vstr;
      const bf16x8 kn = ldsr128(kp + (i + 1) * 1024), v0 = ldsr128(vp), v1 = ldsr128(vp + 512);
      u32x4 p0, p1;
#pragma unroll
      for (int e = 0; e < 8; e += 2) p0[e >> 1] = pk2(__builtin_amdgcn_exp2f(s[e]), __builtin_amdgcn_exp2f(s[e + 1]));
      o = MFMA32(v0, __builtin_bit_cast(bf16x8, p0), o);
#pragma unroll
      for (int e = 8; e < 16; e += 2) p1[(e >> 1) & 3] = pk2(__builtin_amdgcn_exp2f(s[e]), __builtin_amdgcn_exp2f(s[e + 1]));
      s = MFMA32(kn, qf, cin);
      o = MFMA32(v1, __builtin_bit_cast(bf16x8, p1), o);
    }
    const float il = __builtin_amdgcn_rcpf(o[8]);
    u32x4 ov;
#pragma unroll
    for (int jj = 0; jj < 4; ++jj) ov[jj] = pk2(o[2 * jj] * il, o[2 * jj + 1] * il);
    const int B = qb * 8 + w;
    st128(rws, WS_O + (((B >> 1) * 32 + head) * 2 + (B & 1)) * 1024 + lane * 16, ov);
  }

  {
    const int mt = (w >> 1) & 1, nt = w & 1;
    const int tok = st * 64 + nt * 32 + r;
    const int ta2 = kh * 8192 + (8 * h + q4) * 128 + (((nt * 4 + blk * 2 + (p4 >> 1)) ^ ((q4 >> 1) << 2)) << 4) + (p4 & 1) * 8;
    const int tao = kh * 8192 + (4 * h + q4) * 128 + (((mt * 4 + blk * 2 + (p4 >> 1)) ^ ((q4 >> 1) << 2)) << 4) + (p4 & 1) * 8;
    const int fa2 = kh * 8192 + 2048 + mt * 1024 + lane * 16;
    const int fao = kh * 8192 + 2048 + nt * 1024 + lane * 16;
    f32x16 ay = {}, aa = {};
    bf16x8 pa0, pb0, pa1, pb1, qa0, qb0, qa1, qb1;
    float g = 0.f; unsigned b2v = 0;
#define RD2(A0, B0, A1, B1) do { lptr sl = L + so; NEXT_SO(); B0 = cat8(ldstr(sl + ta2), ldstr(sl + ta2 + 512)); A0 = ldsr128(sl + fa2); \
    B1 = cat8(ldstr(sl + ta2 + 4096), ldstr(sl + ta2 + 4096 + 512)); A1 = ldsr128(sl + fa2 + 4096); SB(); } while (0)
#define RDO(A0, B0, A1, B1) do { lptr sl = L + so; NEXT_SO(); A0 = cat8(ldstr(sl + tao), ldstr(sl + tao + 1024)); B0 = ldsr128(sl + fao); \
    A1 = cat8(ldstr(sl + tao + 4096), ldstr(sl + tao + 4096 + 1024)); B1 = ldsr128(sl + fao + 4096); SB(); } while (0)
#define MM2(ACC, A0, B0, A1, B1) do { ACC = MFMA32(A0, B0, ACC); ACC = MFMA32(A1, B1, ACC); SB(); } while (0)
    const unsigned pre = (1u << ODS) | (w == 7 ? 1u << 16 : 0u);
    const unsigned post = (w == 0 ? 1u << ODS : 0u) | (w == 7 ? (1u << ODS) | (1u << 10) | (1u << 12) | (1u << 14) : 0u);
    auto hook = [&](int s) {
      if (RARE((pre >> s) & 1)) { if (s == ODS) asm volatile("s_waitcnt vmcnt(0)" ::: "memory"); else peek_check(pk, rws, fo_off, token); }
      CBAR(6);
      if (RARE((post >> s) & 1)) {
        if (s == ODS) { if (w == 7) *(LAS unsigned*)pk = ~token; else if (lane == 0) flag_st(ws, FL_O + (qb * 32 + head) * 32, token); }
        else peek_issue(lds0 + L_P, ws, fo_off);
      }
      SB();
    };
    LBAR();
    RD2(pa0, pb0, pa1, pb1);
    if (kh) { pa0 = bf16x8{}; pa1 = bf16x8{}; pb0 = bf16x8{}; pb1 = bf16x8{}; }
#pragma unroll 1
    for (int s = 2; s < 22; s += 2) {
      CBAR(6); RD2(qa0, qb0, qa1, qb1); MM2(ay, pa0, pb0, pa1, pb1);
      hook(s); RD2(pa0, pb0, pa1, pb1); MM2(ay, qa0, qb0, qa1, qb1);
    }
    g = __uint_as_float(ld32(rws, WS_G + tok * 4));
    b2v = ((const unsigned short*)b2)[tok];
    CBAR(6); RD2(qa0, qb0, qa1, qb1); MM2(ay, pa0, pb0, pa1, pb1);
    CBAR(6); RDO(pa0, pb0, pa1, pb1); MM2(ay, qa0, qb0, qa1, qb1);
#pragma unroll 1
    for (int s = 23; s < 29; s += 2) {
      CBAR(6); RDO(qa0, qb0, qa1, qb1); MM2(aa, pa0, pb0, pa1, pb1);
      CBAR(6); RDO(pa0, pb0, pa1, pb1); MM2(aa, qa0, qb0, qa1, qb1);
    }
    asm volatile("" : "+v"(g), "+v"(b2v));
    if (lead && threadIdx.x == 384) st32(rws, WS_FL + FL_EPOCH * 4, token);
    CBAR(6); RDO(qa0, qb0, qa1, qb1); MM2(aa, pa0, pb0, pa1, pb1);
    MM2(aa, qa0, qb0, qa1, qb1);
    const float og = 1.0f - g;
    float v[16];
#pragma unroll
    for (int i = 0; i < 16; ++i) v[i] = ay[i] * og + aa[i] * g;
    {
      lptr ex = L + L_K + w * 2048 + lane * 16;
      f32x4 s0, s1;
#pragma unroll
      for (int jj = 0; jj < 4; ++jj) { s0[jj] = kh ? v[jj] : v[8 + jj]; s1[jj] = kh ? v[4 + jj] : v[12 + jj]; }
      *(LAS f32x4*)(ex) = s0; *(LAS f32x4*)(ex + 1024) = s1;
    }
    LBAR();
    {
      lptr ex = L + L_K + (w ^ 4) * 2048 + lane * 16;
      const f32x4 r0 = *(const LAS f32x4*)(ex), r1 = *(const LAS f32x4*)(ex + 1024);
      const float bo = bflo(b2v) * og;
      float f[8];
#pragma unroll
      for (int jj = 0; jj < 4; ++jj) { f[jj] = (kh ? v[8 + jj] : v[jj]) + r0[jj] + bo; f[4 + jj] = (kh ? v[12 + jj] : v[4 + jj]) + r1[jj] + bo; }
      const unsigned a0 = pk2(f[0], f[1]), a1 = pk2(f[2], f[3]), c0 = pk2(f[4], f[5]), c1 = pk2(f[6], f[7]);
      auto x0 = __builtin_amdgcn_permlane32_swap(a0, c0, false, false);
      auto x1 = __builtin_amdgcn_permlane32_swap(a1, c1, false, false);
      const u32x4 ov = {x0[0], x1[0], x0[1], x1[1]};
      __builtin_amdgcn_raw_buffer_store_b128(ov, __builtin_amdgcn_make_buffer_rsrc(out, 0, 2048 * 1024, 0x00020000), tok * 1024 + (dt * 64 + mt * 32 + kh * 16 + h * 8) * 2, 0, 0);
    }
  }
}

extern "C" void kernel_launch(void* const* d_in, const int* in_sizes, int n_in, void* d_out, int out_size, void* d_ws, size_t ws_size,
                              hipStream_t stream) {
  (void)hipFuncSetAttribute((const void*)fused_kernel, hipFuncAttributeMaxDynamicSharedMemorySize, LDS_BYTES);
  fused_kernel<<<256, 768, LDS_BYTES, stream>>>((const char*)d_in[0], (const char*)d_in[1], (const char*)d_in[2], (const char*)d_in[4],
                                                (const char*)d_in[5], (const char*)d_in[6], (const char*)d_in[7], (const char*)d_in[8],
                                                (char*)d_ws, (char*)d_out);
}
```

```cpp
#include <hip/hip_runtime.h>
#include <hip/hip_bf16.h>
#include <stdint.h>

#define LAS __attribute__((address_space(3)))
#define GAS __attribute__((address_space(1)))
#define DI __device__ __forceinline__
typedef __attribute__((ext_vector_type(8))) __bf16 bf16x8;
typedef __attribute__((ext_vector_type(2))) __bf16 bf16x2;
typedef __attribute__((ext_vector_type(4))) short s16x4;
typedef __attribute__((ext_vector_type(2))) float f32x2;
typedef __attribute__((ext_vector_type(4))) float f32x4;
typedef __attribute__((ext_vector_type(16))) float f32x16;
typedef __attribute__((ext_vector_type(2))) unsigned u32x2;
typedef __attribute__((ext_vector_type(4))) unsigned u32x4;
typedef LAS char* lptr;
typedef __amdgpu_buffer_rsrc_t rsrc_t;

constexpr unsigned WS_Q = 0, WS_K = 2u << 20, WS_V = 4u << 20, WS_O = 6u << 20, WS_U = 8u << 20, WS_H = 10u << 20,
                   WS_G = 13u << 20, WS_FL = 14u << 20, WS_END = 15u << 20;
constexpr int FL_H = 0;
constexpr int FL_Q = 64 * 32;
constexpr int FL_U = FL_Q + 192 * 32;
constexpr int FL_O = FL_U + 352 * 32;
constexpr int FL_EPOCH = FL_O + 256 * 32;
constexpr float QSCALE = 0.25f * 1.4426950408889634f;
constexpr int NS = 7, SLOT = 16384, RING = NS * SLOT;
constexpr int L_K = RING, L_V = RING + 16384, L_X = RING + 32768  , L_C = L_X + 12288  , L_P = L_C + 1024  ;
constexpr int LDS_BYTES = 163840;
constexpr unsigned SPIN_CAP = 200000;
constexpr int KVS = 24;
constexpr int QDS = 8;
constexpr int ODS = 4;

#define MFMA32(a, b, c) __builtin_amdgcn_mfma_f32_32x32x16_bf16(a, b, c, 0, 0, 0)
#define MFMA16(a, b, c) __builtin_amdgcn_mfma_f32_16x16x32_bf16(a, b, c, 0, 0, 0)
#define SB() __builtin_amdgcn_sched_barrier(0)
#define LBAR() do { asm volatile("s_waitcnt lgkmcnt(0)\n\ts_barrier" ::: "memory"); SB(); } while (0)
#define CBAR(N) do { asm volatile("s_waitcnt lgkmcnt(" #N ")\n\ts_barrier" ::: "memory"); SB(); } while (0)
#define DBAR() do { asm volatile("s_waitcnt vmcnt(0) lgkmcnt(0)\n\ts_barrier" ::: "memory"); SB(); } while (0)
#define VBAR(N) asm volatile("s_waitcnt vmcnt(" #N ")\n\ts_barrier" ::: "memory")
#define BAR() asm volatile("s_barrier" ::: "memory")

DI int rfl(int x) { return __builtin_amdgcn_readfirstlane(x); }
DI bf16x8 ldsr128(lptr p) { return *(const LAS bf16x8*)p; }
DI s16x4 ldstr(lptr p) { return __builtin_amdgcn_ds_read_tr16_b64_v4i16((LAS s16x4*)p); }
DI bf16x8 cat8(s16x4 a, s16x4 b) { return __builtin_bit_cast(bf16x8, __builtin_shufflevector(a, b, 0, 1, 2, 3, 4, 5, 6, 7)); }
DI unsigned pk2(float a, float b) { f32x2 f = {a, b}; return __builtin_bit_cast(unsigned, __builtin_convertvector(f, bf16x2)); }
DI float bflo(unsigned u) { return __uint_as_float(u << 16); }
DI float bfhi(unsigned u) { return __uint_as_float(u & 0xffff0000u); }
DI int crow(int i, int h) { return (i & 3) + 8 * (i >> 2) + 4 * h; }
DI void st128(rsrc_t r, unsigned off, u32x4 v) { __builtin_amdgcn_raw_buffer_store_b128(v, r, off, 0, 16); }
DI void st64(rsrc_t r, unsigned off, u32x2 v) { __builtin_amdgcn_raw_buffer_store_b64(v, r, off, 0, 16); }
DI void st32(rsrc_t r, unsigned off, unsigned v) { __builtin_amdgcn_raw_buffer_store_b32(v, r, off, 0, 16); }
DI u32x4 ld128(rsrc_t r, unsigned off) { return __builtin_amdgcn_raw_buffer_load_b128(r, off, 0, 16); }
DI unsigned ld32(rsrc_t r, unsigned off) { return __builtin_amdgcn_raw_buffer_load_b32(r, off, 0, 16); }
DI void flag_st(char* ws, int idx, unsigned v) { __hip_atomic_store((GAS unsigned*)(ws + WS_FL) + idx, v, __ATOMIC_RELAXED, __HIP_MEMORY_SCOPE_AGENT); }

#define M0_IN "s_mov_b32 %0, m0\n\ts_mov_b32 m0, %1\n\ts_nop 2\n\t"
#define M0_OUT "s_mov_b32 m0, %0"
DI void dma_2x2(unsigned m0v, const void* b0, unsigned v0, unsigned v1, const void* b1, unsigned v2, unsigned v3) {
  unsigned sv;
  asm volatile(M0_IN "global_load_lds_dwordx4 %4, %2\n\tglobal_load_lds_dwordx4 %5, %2 offset:1024\n\t"
               "global_load_lds_dwordx4 %6, %3 offset:2048\n\tglobal_load_lds_dwordx4 %7, %3 offset:3072\n\t" M0_OUT
               : "=&s"(sv) : "s"(m0v), "s"(b0), "s"(b1), "v"(v0), "v"(v1), "v"(v2), "v"(v3) : "memory");
}
DI void dma_lin4_sc1(unsigned m0v, const void* b, unsigned vl) {
  unsigned sv;
  asm volatile(M0_IN "global_load_lds_dwordx4 %3, %2 sc1\n\tglobal_load_lds_dwordx4 %3, %2 offset:1024 sc1\n\t"
               "global_load_lds_dwordx4 %3, %2 offset:2048 sc1\n\tglobal_load_lds_dwordx4 %3, %2 offset:3072 sc1\n\t" M0_OUT
               : "=&s"(sv) : "s"(m0v), "s"(b), "v"(vl) : "memory");
}
DI void dma_g1(unsigned m0v, const void* wb, unsigned v0, unsigned v1, const void* hb_m2048, unsigned vl) {
  unsigned sv;
  asm volatile(M0_IN "global_load_lds_dwordx4 %4, %2\n\tglobal_load_lds_dwordx4 %5, %2 offset:1024\n\t"
               "global_load_lds_dwordx4 %6, %3 offset:2048 sc1\n\t"
               "s_mov_b32 exec_hi, 0\n\tglobal_load_lds_dwordx4 %6, %3 offset:3072 sc1\n\ts_mov_b32 exec_hi, -1\n\t" M0_OUT
               : "=&s"(sv) : "s"(m0v), "s"(wb), "s"(hb_m2048), "v"(v0), "v"(v1), "v"(vl) : "memory");
}
DI void dma_hh(unsigned m0v, const void* hb_m2048, unsigned vl) {
  unsigned sv;
  asm volatile(M0_IN "global_load_lds_dwordx4 %3, %2 offset:2048 sc1\n\t"
               "s_mov_b32 exec_hi, 0\n\tglobal_load_lds_dwordx4 %3, %2 offset:3072 sc1\n\ts_mov_b32 exec_hi, -1\n\t" M0_OUT
               : "=&s"(sv) : "s"(m0v), "s"(hb_m2048), "v"(vl) : "memory");
}
DI void dma_touch(unsigned m0v, const void* b, unsigned voff) {
  unsigned sv;
  asm volatile(M0_IN "global_load_lds_dword %3, %2\n\t" M0_OUT : "=&s"(sv) : "s"(m0v), "s"(b), "v"(voff) : "memory");
}
DI void dma_wh(unsigned m0v, const void* wb, unsigned v0, unsigned v1) {
  unsigned sv;
  asm volatile(M0_IN "global_load_lds_dwordx4 %3, %2\n\tglobal_load_lds_dwordx4 %4, %2 offset:1024\n\t" M0_OUT
               : "=&s"(sv) : "s"(m0v), "s"(wb), "v"(v0), "v"(v1) : "memory");
}
DI void dma_fh(unsigned m0v, const void* fb_m2048, unsigned vl) {
  unsigned sv;
  asm volatile(M0_IN "global_load_lds_dwordx4 %3, %2 offset:2048 sc1\n\tglobal_load_lds_dwordx4 %3, %2 offset:3072 sc1\n\t" M0_OUT
               : "=&s"(sv) : "s"(m0v), "s"(fb_m2048), "v"(vl) : "memory");
}

DI void spin1(rsrc_t r, unsigned off, unsigned tok) {
  unsigned n = 0;
#pragma nounroll
  do { const unsigned v = ld32(r, off); if (__all(v == tok)) break; __builtin_amdgcn_s_sleep(4); asm volatile("" ::: "memory"); } while (++n < SPIN_CAP);
  __builtin_amdgcn_fence(__ATOMIC_ACQUIRE, "wavefront");
}
DI void peek_issue(unsigned m0v, const void* ws, unsigned off) {
  unsigned sv;
  asm volatile(M0_IN "global_load_lds_dwordx4 %3, %2 sc1\n\t" M0_OUT : "=&s"(sv) : "s"(m0v), "s"(ws), "v"(off) : "memory");
}
DI void peek_check(lptr p, rsrc_t r, unsigned off, unsigned tok) {
  if (__all(*(const volatile LAS unsigned*)p == tok)) return;
  asm volatile("s_waitcnt vmcnt(0)" ::: "memory");
  if (__all(*(const volatile LAS unsigned*)p == tok)) return;
  spin1(r, off, tok);
}
#define RARE(c) __builtin_expect((c) != 0, 0)

DI float row16_sum(float v) {
#define DPPX(ctrl) v += __int_as_float(__builtin_amdgcn_update_dpp(0, __float_as_int(v), ctrl, 0xf, 0xf, true))
  DPPX(0xB1); DPPX(0x4E); DPPX(0x141); DPPX(0x140);
#undef DPPX
  return v;
}
DI float gelu_tanh(float u) {
  const float z = u * (0.7978845608028654f + 0.7978845608028654f * 0.044715f * u * u);
  const float e = __builtin_amdgcn_exp2f(z * (-2.0f * 1.4426950408889634f));
  return u * __builtin_amdgcn_rcpf(1.0f + e);
}
constexpr unsigned long long diag_mask(int i) {
  unsigned long long mk = 0;
  for (int l = 0; l < 64; ++l) if ((i & 3) + 8 * (i >> 2) + 4 * (l >> 5) <= (l & 31)) mk |= 1ull << l;
  return mk;
}
DI float max16(const f32x16& s, float m) {
#pragma unroll
  for (int i = 0; i < 16; i += 2) m = __builtin_fmaxf(__builtin_fmaxf(s[i], s[i + 1]), m);
  return m;
}

#define NEXT_SO() so = so == (NS - 1) * SLOT ? 0 : so + SLOT

template <bool ISV, class Hook, class Hook14, class Mid>
DI void qkv_consume(lptr L, int& so, int w, int lane, f32x16 (&acc)[2], Hook hook, Hook14 hook14, Mid mid) {
  const int r = lane & 31, h = lane >> 5, wm = w >> 2, wn = w & 3;
  int xa[4];
#pragma unroll
  for (int s = 0; s < 4; ++s) xa[s] = (wm * 2 + (r >> 4)) * 4096 + (r & 15) * 128 + (((2 * s + h) ^ ((r >> 1) & 7)) << 4);
  const int q = (lane & 15) >> 2, p = lane & 3, blk = (lane >> 4) & 1;
  const int wa = h * 4096 + 2048 + q * 256 + (((wn * 4 + blk * 2 + (p >> 1)) ^ (q << 2)) << 4) + (p & 1) * 8;
  bf16x8 xA[2][4], xB[2][4], wA[4], wB[4];
#define QM(X, W, mi, s) do { if (ISV) acc[mi] = MFMA32(X[mi][s], W[s], acc[mi]); else acc[mi] = MFMA32(W[s], X[mi][s], acc[mi]); SB(); } while (0)
#define QRX(X, mi, s) X[mi][s] = ldsr128(sl + xa[s])
#define QRW(W, mi, e) W[2 * mi + e] = cat8(ldstr(sl + wa + e * 8192), ldstr(sl + wa + e * 8192 + 1024))
#define QSTEP(mi, XN, WN, XP, WP, MM) do { lptr sl = L + so; NEXT_SO(); \
    if (MM) QM(XP, WP, 0, 2 * mi); \
    QRX(XN, mi, 0); QRX(XN, mi, 1); SB(); if (MM) QM(XP, WP, 1, 2 * mi); \
    QRX(XN, mi, 2); QRX(XN, mi, 3); QRW(WN, mi, 0); SB(); if (MM) QM(XP, WP, 0, 2 * mi + 1); \
    QRW(WN, mi, 1); SB(); if (MM) QM(XP, WP, 1, 2 * mi + 1); } while (0)
  LBAR(); QSTEP(0, xA, wA, xB, wB, false); QSTEP(1, xA, wA, xB, wB, false);
#pragma unroll 1
  for (int c = 1; c < 7; c += 2) {
    hook(c);     QSTEP(0, xB, wB, xA, wA, true); QSTEP(1, xB, wB, xA, wA, true);
    hook(c + 1); QSTEP(0, xA, wA, xB, wB, true); QSTEP(1, xA, wA, xB, wB, true);
  }
  hook14(); QSTEP(0, xB, wB, xA, wA, true); QSTEP(1, xB, wB, xA, wA, true);
#pragma unroll
  for (int s = 0; s < 4; ++s) QM(xB, wB, 0, s);
  mid();
#pragma unroll
  for (int s = 0; s < 4; ++s) QM(xB, wB, 1, s);
}

__global__ __launch_bounds__(768, 3) void fused_kernel(const char* __restrict__ x, const char* __restrict__ wqkv, const char* __restrict__ wo,
                                                       const char* __restrict__ w1, const char* __restrict__ b1, const char* __restrict__ w2,
                                                       const char* __restrict__ b2, const char* __restrict__ gw, char* __restrict__ ws,
                                                       char* __restrict__ out) {
  extern __shared__ __attribute__((aligned(16))) char smem[];
  asm volatile("" :: "s"(x), "s"(wqkv), "s"(wo), "s"(w1), "s"(b1), "s"(w2), "s"(b2), "s"(gw), "s"(ws), "s"(out));
  const int b = blockIdx.x, xcd = b & 7, idx = ((b >> 3) + 24) & 31;
  const bool lead = b == 65;
  const int lane = threadIdx.x & 63, w = rfl(threadIdx.x >> 6);
  const unsigned lds0 = (unsigned)(uintptr_t)smem;
  lptr L = (lptr)smem;
  const int tm = (xcd >> 1) * 4 + idx / 6, tn = (xcd & 1) * 6 + idx % 6;
  const int lnb = xcd * 8 + (idx - 24);
  const unsigned x0 = (lane >> 3) * 1024 + (((lane & 7) ^ (lane >> 4)) << 4), x1 = (8 + (lane >> 3)) * 1024 + (((lane & 7) ^ (4 + (lane >> 4))) << 4) - 1024;
  const unsigned q0 = (lane >> 4) * 3072 + (((lane & 15) ^ ((lane >> 4) << 2)) << 4), q1 = q0 + 4 * 3072 - 1024;
  const char* xp = x + (size_t)(tm * 128 + ((w >> 1) & 1) * 64 + (w & 1) * 16) * 1024;
  const char* qp = wqkv + (size_t)((w & 3) * 8) * 3072 + tn * 256 - 2048;
  int kc = ((idx / 6 + idx % 6) % 3) * 3;
  if (w >= 8) {
    if (idx < 24) {
#pragma unroll 1
      for (int f = 0; f < 7; ++f) {
        dma_2x2(lds0 + (w & 3) * 4096 + f * SLOT, xp + kc * 128 + (f & 1) * 32 * 1024, x0, x1, qp + (size_t)(kc * 2 + (f & 1)) * (32 * 3072), q0, q1);
        kc = (kc + (f & 1)) & 7;
      }
    }
  }
  asm volatile("" ::: "memory");
  SB();
  const int gi = 31 - idx;
  const bool hasQ = idx < 24, hasG1 = xcd == 0 || gi < 30;
  const bool lowd = xcd == 0 ? gi < 12 : xcd < 5;
  const int gj = xcd == 0 && !lowd ? gi - 12 : gi;
  const int g1ft = lowd ? (xcd == 0 ? 20 : (xcd - 1) * 5) + gj / 6 : (xcd == 0 ? 18 : (xcd - 5) * 6) + gj / 5;
  const int g1dt = lowd ? gj % 6 : 6 + gj % 5;
  const int head = xcd * 4 + (idx & 3), qb = idx >> 2;
  const int st = (xcd >> 1) * 8 + (idx >> 2), dt = (xcd & 1) * 4 + (idx & 3);
  unsigned fq_off;
  { const int l = lane < 12 ? lane : 3, wh = l >> 2; int tmi = l & 3; if (wh == 0 || qb == 0) tmi |= 2;
    fq_off = WS_FL + (FL_Q + ((wh * 4 + (head >> 3)) * 16 + 2 * qb - 2 + tmi) * 32) * 4; }
  const unsigned fu_off = WS_FL + (FL_U + (((lane & 32) ? (4 * dt + 3) / 3 : (4 * dt) / 3) * 32 + min(lane & 31, 21)) * 32) * 4;
  const unsigned fo_off = WS_FL + (FL_O + ((st >> 2) * 32 + (lane & 31)) * 32) * 4;
  const unsigned fh_off = WS_FL + FL_H * 4 + lane * 128;
  const rsrc_t rws = __builtin_amdgcn_make_buffer_rsrc(ws, 0, WS_END, 0x00020000);
  volatile LAS unsigned* relay = (volatile LAS unsigned*)(L + L_C + 64);
  unsigned eoff = WS_FL + FL_EPOCH * 4;
  asm volatile("" : "+v"(eoff));
  const unsigned epoch = ld32(rws, eoff);
#define TOKEN_OF(e) ((e) * 1664525u + 1013904223u)

  if (w >= 8) {
    const int j = w - 8;
#define token TOKEN_OF(epoch)
    const unsigned m0b = lds0 + j * 4096;
    const unsigned vl = lane * 16;
    unsigned so = 0;
    const char* hp = ws + WS_H + (size_t)g1dt * 64 * 3072 + j * 1536 - 2048;
    const char* w1p = w1 + (size_t)(j * 16) * 2752 + g1ft * 128;
    const unsigned u0 = (lane >> 3) * 2752 + ((((lane & 7) ^ (((lane >> 4) & 3) << 1)) & (g1ft == 21 ? 3 : 7)) << 4), u1 = u0 + 8 * 2752 - 1024;
    int cw = lowd ? g1dt * 6 : (g1dt - 6) * 6 + 3, ch = cw;
    auto fill_g1 = [&]() { dma_g1(m0b + so, w1p + (size_t)cw * (64 * 2752), u0, u1, hp + cw * 6144, vl); cw = (cw + 1) & 31; NEXT_SO(); };
    auto fill_g1w = [&]() { dma_wh(m0b + so, w1p + (size_t)cw * (64 * 2752), u0, u1); cw = (cw + 1) & 31; NEXT_SO(); };
    auto fill_g1h3 = [&](unsigned so0) {
      const unsigned keep = so; so = so0;
#pragma unroll 1
      for (int i = 0; i < 3; ++i) { dma_hh(m0b + so, hp + ch * 6144, vl); ch = (ch + 1) & 31; NEXT_SO(); }
      so = keep;
    };
    const unsigned swz = ((lane & 7) ^ (((lane >> 4) & 1) << 2)) << 4;
    unsigned vw0 = (lane >> 3) * 4096 + swz, vw1 = vw0 + 8 * 4096 - 1024;
    const char* w2p = w2 + (size_t)(j * 16) * 4096 + st * 128;
    const char* up = ws + WS_U + (size_t)dt * 86 * 2048 + j * 2048 - 2048;
    const size_t w2half = (size_t)(j < 2 ? 21 * 64 : 20 * 64) * 4096;
    const int uhalf = j < 2 ? 21 * 8192 : 20 * 8192;
    auto fill_kv = [&]() {
      int t0 = qb * 8 - 8 + 4 * j; t0 = t0 < 0 ? t0 + 8 : t0;
      dma_lin4_sc1(lds0 + L_K + j * 4096, ws + WS_K + (size_t)(head * 64 + t0) * 1024, vl);
      dma_lin4_sc1(lds0 + L_V + j * 4096, ws + WS_V + (size_t)(head * 64 + t0) * 1024, vl);
    };
    int nwh = 0;
    const int r2 = (((dt & 3) * 2 + (dt >> 2)) * 21) >> 3;
    int c2 = r2;
    auto fill_wh = [&]() {
      dma_wh(m0b + so, nwh == 0 ? w2p + w2half : w2p + (size_t)c2 * (64 * 4096), vw0, vw1);
      if (nwh) c2 = c2 == 20 ? 0 : c2 + 1;
      ++nwh; NEXT_SO();
    };
    unsigned so_g2 = 0;
    auto warm_g2 = [&]() {
      if (j == 1) dma_touch(lds0 + L_P + 1024, wo + (size_t)(idx * 16) * 1024 + (xcd & 1) * 512, (lane >> 2) * 1024 + (lane & 3) * 128);
    };

    if (hasQ) {
      auto fill_a = [&]() { dma_2x2(m0b + so, xp + kc * 128, x0, x1, qp + (size_t)kc * 64 * 3072, q0, q1); NEXT_SO(); };
      auto fill_b = [&]() { dma_2x2(m0b + so, xp + kc * 128 + 32 * 1024, x0, x1, qp + (size_t)kc * 64 * 3072 + 32 * 3072, q0, q1); kc = (kc + 1) & 7; NEXT_SO(); };
      VBAR(20);
#pragma unroll 1
      for (int c = 1; c < 5; ++c) { VBAR(12); fill_b(); fill_a(); }
      VBAR(12); fill_b();
      if (hasG1) {
        const unsigned so0 = so;
        fill_g1w();
        VBAR(10); fill_g1w(); fill_g1w();
        VBAR(6); fill_g1h3(so0); fill_g1(); fill_g1();
      } else {
        VBAR(8); VBAR(0);
      }
    } else {
      BAR();
      VBAR(0);
#pragma unroll 1
      for (int s = 0; s < 3; ++s) fill_g1w();
      if (j == 0) spin1(rws, fh_off, token);
      BAR();
      fill_g1h3(0); fill_g1(); fill_g1();
    }
    if (hasG1) {
      VBAR(12); fill_g1(); VBAR(14); fill_g1();
#pragma unroll 1
      for (int s = 2; s <= KVS; ++s) { VBAR(16); fill_g1(); }
      fill_kv();
#pragma unroll 1
      for (int s = KVS + 1; s < 27; ++s) { VBAR(16); fill_g1(); }
      so_g2 = so;
      VBAR(16); fill_wh(); VBAR(14); fill_wh(); VBAR(12); fill_wh(); VBAR(10); fill_wh(); VBAR(8); fill_wh();
      BAR(); fill_wh(); fill_wh();
      warm_g2();
      BAR();
      BAR();
    } else {
      BAR();
      so_g2 = so;
#pragma unroll 1
      for (int i = 0; i < 7; ++i) fill_wh();
      warm_g2();
      if (j == 0) spin1(rws, fq_off, token);
      BAR();
      fill_kv();
      VBAR(0);
    }
    unsigned tokl = token;
    asm volatile("" : "+v"(tokl));
    {
      if (j == 0) { spin1(rws, fu_off, tokl); *relay = 1u; }
      else { for (unsigned spins = 0; spins < 64 * SPIN_CAP && *relay == 0u; ++spins) __builtin_amdgcn_s_sleep(1); }
      so = so_g2;
      dma_fh(m0b + so, up + uhalf, vl); NEXT_SO();
#pragma unroll 1
      for (int i = 0, c = r2; i < 6; ++i) { dma_fh(m0b + so, up + c * 8192, vl); c = c == 20 ? 0 : c + 1; NEXT_SO(); }
    }
    unsigned wsh = 18;
    int ci = 7;
    auto fill_g2 = [&]() {
      if (ci == 22) {
        w2p = wo + (size_t)(j * 16) * 1024 + dt * 128; up = ws + WS_O + (size_t)st * 32 * 2048 + j * 2048 - 2048; wsh = 16; c2 = 0;
        vw0 = (lane >> 3) * 1024 + swz; vw1 = vw0 + 8 * 1024 - 1024;
      }
      dma_wh(m0b + so, w2p + ((size_t)c2 << wsh), vw0, vw1); dma_fh(m0b + so, up + c2 * 8192, vl);
      c2 = c2 == 20 ? 0 : c2 + 1; NEXT_SO(); ++ci;
    };
    VBAR(12); VBAR(10);
    VBAR(8); fill_g2(); VBAR(10); fill_g2(); VBAR(12); fill_g2(); VBAR(14); fill_g2();
    const unsigned fa_off = WS_FL + FL_O * 4 + (j * 64 + lane) * 128;
    lptr pa = L + L_X + j * 1024 + lane * 16;
    if (lead) *(volatile LAS unsigned*)pa = ~tokl;
#pragma unroll 1
    for (int s = 6; s < 20; ++s) { VBAR(16); fill_g2(); }
    if (lead) peek_issue(lds0 + L_X + j * 1024, ws, fa_off);
#pragma unroll 1
    for (int s = 20; s < 25; ++s) { VBAR(16); fill_g2(); }
    VBAR(16);
    if (lead) peek_check(pa, rws, fa_off, tokl);
    VBAR(12); VBAR(8); VBAR(4); VBAR(0);
    BAR();
    return;
  }

#undef token
  unsigned token;
  u32x4 xv[4], gv[4];
  if (!hasQ) {
#pragma unroll
    for (int i = 0; i < 4; ++i) xv[i] = *(const u32x4*)(x + (size_t)(lnb * 32 + w * 4 + (lane >> 4)) * 1024 + (lane & 15) * 16 + i * 256);
#pragma unroll
    for (int i = 0; i < 4; ++i) gv[i] = *(const u32x4*)(gw + (lane & 15) * 16 + i * 256);
  }
  asm volatile("" ::: "memory");
  SB();
  const int r = lane & 31, h = lane >> 5;
  const int g4 = lane >> 4, q4 = (lane & 15) >> 2, p4 = lane & 3, blk = (lane >> 4) & 1;
  int so = 0;
  if (threadIdx.x == 4) *relay = 0u;
  if (threadIdx.x < 4) {
    const unsigned cv = (threadIdx.x & 1) ? 0u : 0x3f803f80u;
    *(LAS u32x4*)(L + L_C + (threadIdx.x >> 1) * 512 + (threadIdx.x & 1) * 16) = u32x4{cv, cv, cv, cv};
  }
  lptr pk = L + L_P + lane * 16;

  const int kh = w >> 2, fb = w & 3;
  int ha[3];
#pragma unroll
  for (int db = 0; db < 3; ++db) { const int o = (kh * 3 + db) * 1024 + lane * 16, rg = o / 1536; ha[db] = rg * 4096 + 2048 + (o - rg * 1536); }
  const int wa = kh * 8192 + (4 * g4 + q4) * 128 + ((fb ^ ((2 * g4 + (q4 >> 1)) & 3)) << 5) + p4 * 8;
  f32x4 acc[3] = {};
  bf16x8 wA = {}, hA[3] = {}, wB = {}, hB[3] = {};
#define G1RD(W, H) do { lptr sl = L + so; NEXT_SO(); W = cat8(ldstr(sl + wa), ldstr(sl + wa + 4096)); \
  _Pragma("unroll") for (int db = 0; db < 3; ++db) H[db] = ldsr128(sl + ha[db]); SB(); } while (0)
#define G1MM(W, H) do { _Pragma("unroll") for (int db = 0; db < 3; ++db) acc[db] = MFMA16(W, H[db], acc[db]); SB(); } while (0)

  token = TOKEN_OF(epoch);
  asm volatile("" : "+v"(token), "+v"(gv[3]));
  if (hasQ) {
    if (w == 7) *(LAS unsigned*)pk = ~token;
    f32x16 qacc[2] = {};
    const bool poller = w == 7 && hasG1;
    const unsigned post = poller ? (1u << 4) | (1u << 5) | (1u << 6) : 0u;
    auto hook = [&](int s) {
      LBAR();
      if (RARE((post >> s) & 1)) peek_issue(lds0 + L_P, ws, fh_off);
      SB();
    };
    auto hook14 = [&]() { if (poller) peek_check(pk, rws, fh_off, token); LBAR(); };
    auto mid = [&]() { if (hasG1) { LBAR(); G1RD(wA, hA); } };
    if (tn >= 8) qkv_consume<true>(L, so, w, lane, qacc, hook, hook14, mid);
    else qkv_consume<false>(L, so, w, lane, qacc, hook, hook14, mid);
    const int wm = w >> 2, wn = w & 3;
    const int hd0 = (tn & 3) * 8 + wn * 2;
    const float sc = tn < 4 ? QSCALE : 1.0f;
    const unsigned dst = tn >= 8 ? WS_V + (((hd0 + (r >> 4)) * 64 + tm * 4 + wm * 2) * 64 + h * 16 + (r & 15)) * 16
                                 : (tn < 4 ? WS_Q : WS_K) + ((hd0 * 64 + tm * 4 + wm * 2) * 64 + lane) * 16;
    const unsigned dstep = tn >= 8 ? 512 : 65536;
#pragma unroll
    for (int mi = 0; mi < 2; ++mi) {
#pragma unroll
      for (int s = 0; s < 2; ++s) {
        u32x4 v;
#pragma unroll
        for (int jj = 0; jj < 4; ++jj) v[jj] = pk2(qacc[mi][8 * s + 2 * jj] * sc, qacc[mi][8 * s + 2 * jj + 1] * sc);
        st128(rws, dst + mi * 1024 + s * dstep, v);
      }
      if (mi == 0 && hasG1) { SB(); LBAR(); G1RD(wB, hB); G1MM(wA, hA); }
    }
  } else {
    constexpr int RS = 1056;
    f32x2 v[16], s2 = {0.f, 0.f}, g2 = {0.f, 0.f};
#pragma unroll
    for (int i = 0; i < 16; ++i) {
      v[i] = f32x2{bflo(xv[i >> 2][i & 3]), bfhi(xv[i >> 2][i & 3])};
      s2 += v[i];
      g2 += v[i] * f32x2{bflo(gv[i >> 2][i & 3]), bfhi(gv[i >> 2][i & 3])};
    }
    const float mean = row16_sum(s2[0] + s2[1]) * (1.0f / 512), gs = row16_sum(g2[0] + g2[1]);
    f32x2 q2 = {0.f, 0.f};
#pragma unroll
    for (int i = 0; i < 16; ++i) { v[i] -= f32x2{mean, mean}; q2 += v[i] * v[i]; }
    const float rs = __builtin_amdgcn_rsqf(row16_sum(q2[0] + q2[1]) * (1.0f / 512) + 1e-5f);
#pragma unroll
    for (int i = 0; i < 4; ++i) {
      u32x4 ov;
#pragma unroll
      for (int jj = 0; jj < 4; ++jj) { const f32x2 t = v[4 * i + jj] * f32x2{rs, rs}; ov[jj] = pk2(t[0], t[1]); }
      *(LAS u32x4*)(L + (w * 4 + g4) * RS + (lane & 15) * 16 + i * 256) = ov;
    }
    if ((lane & 15) == 0) st32(rws, WS_G + (lnb * 32 + w * 4 + g4) * 4, __float_as_uint(1.0f / (1.0f + __expf(-gs))));
    LBAR();
#pragma unroll
    for (int i = 0; i < 4; ++i) {
      const int D = w * 4 + i, d48 = D / 3;
      lptr a = L + (4 * g4 + q4) * RS + D * 32 + p4 * 8;
      const bf16x8 f = cat8(ldstr(a), ldstr(a + 16 * RS));
      st128(rws, WS_H + ((d48 * 64 + lnb) * 3 + (D - d48 * 3)) * 1024 + lane * 16, __builtin_bit_cast(u32x4, f));
      if (i == 3 && w == 7) st128(rws, WS_H + ((10 * 64 + lnb) * 3 + 2) * 1024 + lane * 16, __builtin_bit_cast(u32x4, f));
    }
    DBAR();
    if (threadIdx.x == 0) flag_st(ws, FL_H + lnb * 32, token);
    LBAR();
    LBAR(); G1RD(wA, hA);
    LBAR(); G1RD(wB, hB); G1MM(wA, hA);
  }

  bf16x8 qf;
  unsigned pmk[8];
#pragma unroll
  for (int k = 0; k < 8; ++k) { const int dlt = r - crow(2 * k, h); pmk[k] = dlt >= 1 ? 0xffffffffu : (dlt == 0 ? 0x0000ffffu : 0u); }
  int vstr = r < 16 ? 1024 : 0;
  int vp0o = r < 16 ? L_V + w * 1024 + h * 256 + r * 16 : (L_C + ((r == 16 || r == 20) ? 0 : 16));
#define PIN_ATT() asm volatile("" : "+v"(pmk[0]), "+v"(pmk[1]), "+v"(pmk[2]), "+v"(pmk[3]), "+v"(pmk[4]), "+v"(pmk[5]), "+v"(pmk[6]), "+v"(pmk[7]), "+v"(vstr), "+v"(vp0o))
  if (hasG1) {
    const unsigned pre = (1u << QDS) | (w == 7 ? 1u << KVS : 0u);
    const unsigned post = (w == 0 && hasQ ? 1u << QDS : 0u) | (w == 7 ? (1u << QDS) | (1u << (KVS - 6)) | (1u << (KVS - 4)) | (1u << (KVS - 2)) : 0u);
    auto hook = [&](int s) {
      if (RARE((pre >> s) & 1)) { if (s == QDS) asm volatile("s_waitcnt vmcnt(0)" ::: "memory"); else peek_check(pk, rws, fq_off, token); }
      CBAR(5);
      if (RARE((post >> s) & 1)) {
        if (s == QDS) { if (w == 7) *(LAS unsigned*)pk = ~token; else if (lane == 0) flag_st(ws, FL_Q + (tn * 16 + tm) * 32, token); }
        else peek_issue(lds0 + L_P, ws, fq_off);
      }
      SB();
    };
#pragma unroll 1
    for (int s = 2; s < KVS + 2; s += 2) {
      hook(s); G1RD(wA, hA); G1MM(wB, hB);
      CBAR(5); G1RD(wB, hB); G1MM(wA, hA);
    }
    qf = __builtin_bit_cast(bf16x8, ld128(rws, WS_Q + (head * 64 + qb * 8 + w) * 1024 + lane * 16));
    const int fblk = g1ft * 4 + fb;
    u32x2 bb = *(const u32x2*)(b1 + min(fblk * 16 + 4 * g4, 1372) * 2);
    unsigned ua[2];
#pragma unroll
    for (int e = 0; e < 2; ++e) {
      const int D = g1dt * 3 + (kh ? 2 : e);
      ua[e] = WS_U + ((((D >> 2) * 86 + fblk) * 2 + ((D >> 1) & 1)) * 64 + (g4 >> 1) * 32 + (D & 1) * 16 + (lane & 15)) * 16 + (g4 & 1) * 8;
      if (fblk >= 86 || D >= 32) ua[e] = WS_END;
    }
    lptr xw = L + L_X + (fb * 3 * 64 + lane) * 16;
    PIN_ATT();
#pragma unroll 1
    for (int s = KVS + 2; s < 32; s += 2) {
      CBAR(5); G1RD(wA, hA); G1MM(wB, hB);
      CBAR(5); G1RD(wB, hB); G1MM(wA, hA);
    }
    asm volatile("" : "+v"(qf), "+v"(bb), "+v"(ua[0]), "+v"(ua[1]));
    const f32x4 bv = {bflo(bb[0]), bfhi(bb[0]), bflo(bb[1]), bfhi(bb[1])};
    LBAR();
    G1MM(wB, hB);
    if (kh == 1) { *(LAS f32x4*)(xw) = acc[0]; *(LAS f32x4*)(xw + 1024) = acc[1]; }
    else *(LAS f32x4*)(xw + 2048) = acc[2];
    LBAR();
    auto fin = [&](const f32x4& a, int off, unsigned addr) {
      const f32x4 t = a + *(const LAS f32x4*)(xw + off) + bv;
      st64(rws, addr, u32x2{pk2(gelu_tanh(t[0]), gelu_tanh(t[1])), pk2(gelu_tanh(t[2]), gelu_tanh(t[3]))});
    };
    if (kh == 1) fin(acc[2], 2048, ua[0]);
    else { fin(acc[0], 0, ua[0]); fin(acc[1], 1024, ua[1]); }
  } else {
    DBAR();
    if (threadIdx.x == 0) flag_st(ws, FL_Q + (tn * 16 + tm) * 32, token);
    LBAR();
    qf = __builtin_bit_cast(bf16x8, ld128(rws, WS_Q + (head * 64 + qb * 8 + w) * 1024 + lane * 16));
    asm volatile("" : "+v"(qf));
    PIN_ATT();
    LBAR();
  }

  {
    lptr kp = L + L_K + w * 1024 + lane * 16;
    const int i_lo = qb > 0 ? 1 : 8 - w;
    const f32x16 zero = {};
    bf16x8 kf[9];
#pragma unroll
    for (int i = 0; i < 9; ++i) kf[i] = ldsr128(kp + i * 1024);
    f32x16 sb, sa, sc;
    float mx[4];
#define TMAX(S, i) (((i) >= i_lo) ? max16(S, -INFINITY) : -INFINITY)
    sa = MFMA32(kf[8], qf, zero); sc = MFMA32(kf[0], qf, zero);
    f32x16 s1 = MFMA32(kf[1], qf, zero), s2 = MFMA32(kf[2], qf, zero); SB();
    if (RARE(qb == 0)) {
#pragma unroll
      for (int i = 0; i < 16; ++i) sc[i] = -INFINITY;
      asm volatile("" : "+v"(sc));
    }
#pragma unroll
    for (int i = 0; i < 16; ++i) sb[i] = __builtin_amdgcn_inverse_ballot_w64(diag_mask(i)) ? sa[i] : sc[i];
    SB();
    sa = MFMA32(kf[3], qf, zero); sc = MFMA32(kf[4], qf, zero); SB();
    mx[0] = max16(sb, -INFINITY); mx[1] = TMAX(s1, 1); mx[2] = TMAX(s2, 2); SB();
    s1 = MFMA32(kf[5], qf, zero); s2 = MFMA32(kf[6], qf, zero); SB();
    mx[3] = TMAX(sa, 3); mx[0] = __builtin_fmaxf(mx[0], TMAX(sc, 4)); SB();
    sa = MFMA32(kf[7], qf, zero); SB();
    mx[1] = __builtin_fmaxf(mx[1], TMAX(s1, 5)); mx[2] = __builtin_fmaxf(mx[2], TMAX(s2, 6)); mx[3] = __builtin_fmaxf(mx[3], TMAX(sa, 7));
    float m = __builtin_fmaxf(__builtin_fmaxf(mx[0], mx[1]), __builtin_fmaxf(mx[2], mx[3]));
    {
      auto rr = __builtin_amdgcn_permlane32_swap(__float_as_uint(m), __float_as_uint(m), false, false);
      m = __builtin_fmaxf(__uint_as_float(rr[0]), __uint_as_float(rr[1]));
    }
    if (hasG1) {
      DBAR();
      if (threadIdx.x == 0) flag_st(ws, FL_U + (g1dt * 32 + g1ft) * 32, token);
    }
    lptr vp0 = L + vp0o;
    f32x16 cin;
#pragma unroll
    for (int i = 0; i < 16; ++i) cin[i] = -m;
    f32x16 o = {};
    sa = MFMA32(kf[1], qf, cin);
    {
      lptr vp = vp0 + 8 * vstr;
      const bf16x8 vd0 = ldsr128(vp), vd1 = ldsr128(vp + 512), vf0 = ldsr128(vp0), vf1 = ldsr128(vp0 + 512);
      SB();
      u32x4 pd[2], pf[2];
#pragma unroll
      for (int i = 0; i < 16; i += 2) {
        const unsigned pp = pk2(__builtin_amdgcn_exp2f(sb[i] - m), __builtin_amdgcn_exp2f(sb[i + 1] - m));
        pd[i >> 3][(i >> 1) & 3] = pp & pmk[i >> 1];
        pf[i >> 3][(i >> 1) & 3] = pp & ~pmk[i >> 1];
      }
      o = MFMA32(vd0, __builtin_bit_cast(bf16x8, pd[0]), o);
      o = MFMA32(vd1, __builtin_bit_cast(bf16x8, pd[1]), o);
      o = MFMA32(vf0, __builtin_bit_cast(bf16x8, pf[0]), o);
      o = MFMA32(vf1, __builtin_bit_cast(bf16x8, pf[1]), o);
    }
#define PTILE(i, SC, SN) do { SB(); \
      if (i < 7) SN = MFMA32(kf[i + 1], qf, cin); \
      if (i >= i_lo) { \
        lptr vp = vp0 + i * vstr; \
        const bf16x8 v0 = ldsr128(vp), v1 = ldsr128(vp + 512); SB(); \
        u32x4 p0, p1; \
        _Pragma("unroll") for (int e = 0; e < 8; e += 2) p0[e >> 1] = pk2(__builtin_amdgcn_exp2f(SC[e]), __builtin_amdgcn_exp2f(SC[e + 1])); \
        o = MFMA32(v0, __builtin_bit_cast(bf16x8, p0), o); \
        _Pragma("unroll") for (int e = 8; e < 16; e += 2) p1[(e >> 1) & 3] = pk2(__builtin_amdgcn_exp2f(SC[e]), __builtin_amdgcn_exp2f(SC[e + 1])); \
        o = MFMA32(v1, __builtin_bit_cast(bf16x8, p1), o); \
      } } while (0)
    PTILE(1, sa, sc); PTILE(2, sc, sa); PTILE(3, sa, sc); PTILE(4, sc, sa); PTILE(5, sa, sc); PTILE(6, sc, sa); PTILE(7, sa, sc);
    const float il = __builtin_amdgcn_rcpf(o[8]);
    u32x4 ov;
#pragma unroll
    for (int jj = 0; jj < 4; ++jj) ov[jj] = pk2(o[2 * jj] * il, o[2 * jj + 1] * il);
    const int B = qb * 8 + w;
    st128(rws, WS_O + (((B >> 1) * 32 + head) * 2 + (B & 1)) * 1024 + lane * 16, ov);
  }

  {
    const int mt = (w >> 1) & 1, nt = w & 1;
    const int tok = st * 64 + nt * 32 + r;
    const int ta2 = kh * 8192 + (8 * h + q4) * 128 + (((nt * 4 + blk * 2 + (p4 >> 1)) ^ ((q4 >> 1) << 2)) << 4) + (p4 & 1) * 8;
    const int tao = kh * 8192 + (4 * h + q4) * 128 + (((mt * 4 + blk * 2 + (p4 >> 1)) ^ ((q4 >> 1) << 2)) << 4) + (p4 & 1) * 8;
    const int fa2 = kh * 8192 + 2048 + mt * 1024 + lane * 16;
    const int fao = kh * 8192 + 2048 + nt * 1024 + lane * 16;
    f32x16 ay = {}, aa = {};
    bf16x8 pa0, pb0, pa1, pb1, qa0, qb0, qa1, qb1;
    float g = 0.f; unsigned b2v = 0;
#define RD2(A0, B0, A1, B1) do { lptr sl = L + so; NEXT_SO(); B0 = cat8(ldstr(sl + ta2), ldstr(sl + ta2 + 512)); A0 = ldsr128(sl + fa2); \
    B1 = cat8(ldstr(sl + ta2 + 4096), ldstr(sl + ta2 + 4096 + 512)); A1 = ldsr128(sl + fa2 + 4096); SB(); } while (0)
#define RDO(A0, B0, A1, B1) do { lptr sl = L + so; NEXT_SO(); A0 = cat8(ldstr(sl + tao), ldstr(sl + tao + 1024)); B0 = ldsr128(sl + fao); \
    A1 = cat8(ldstr(sl + tao + 4096), ldstr(sl + tao + 4096 + 1024)); B1 = ldsr128(sl + fao + 4096); SB(); } while (0)
#define MM2(ACC, A0, B0, A1, B1) do { ACC = MFMA32(A0, B0, ACC); ACC = MFMA32(A1, B1, ACC); SB(); } while (0)
    const unsigned pre = (1u << ODS) | (w == 7 ? 1u << 16 : 0u);
    const unsigned post = (w == 0 ? 1u << ODS : 0u) | (w == 7 ? (1u << ODS) | (1u << 10) | (1u << 12) | (1u << 14) : 0u);
    auto hook = [&](int s) {
      if (RARE((pre >> s) & 1)) { if (s == ODS) asm volatile("s_waitcnt vmcnt(0)" ::: "memory"); else peek_check(pk, rws, fo_off, token); }
      CBAR(6);
      if (RARE((post >> s) & 1)) {
        if (s == ODS) { if (w == 7) *(LAS unsigned*)pk = ~token; else if (lane == 0) flag_st(ws, FL_O + (qb * 32 + head) * 32, token); }
        else peek_issue(lds0 + L_P, ws, fo_off);
      }
      SB();
    };
    LBAR();
    RD2(pa0, pb0, pa1, pb1);
    if (kh) { pa0 = bf16x8{}; pa1 = bf16x8{}; pb0 = bf16x8{}; pb1 = bf16x8{}; }
#pragma unroll 1
    for (int s = 2; s < 22; s += 2) {
      CBAR(6); RD2(qa0, qb0, qa1, qb1); MM2(ay, pa0, pb0, pa1, pb1);
      hook(s); RD2(pa0, pb0, pa1, pb1); MM2(ay, qa0, qb0, qa1, qb1);
    }
    g = __uint_as_float(ld32(rws, WS_G + tok * 4));
    b2v = ((const unsigned short*)b2)[tok];
    CBAR(6); RD2(qa0, qb0, qa1, qb1); MM2(ay, pa0, pb0, pa1, pb1);
    CBAR(6); RDO(pa0, pb0, pa1, pb1); MM2(ay, qa0, qb0, qa1, qb1);
#pragma unroll 1
    for (int s = 23; s < 29; s += 2) {
      CBAR(6); RDO(qa0, qb0, qa1, qb1); MM2(aa, pa0, pb0, pa1, pb1);
      CBAR(6); RDO(pa0, pb0, pa1, pb1); MM2(aa, qa0, qb0, qa1, qb1);
    }
    asm volatile("" : "+v"(g), "+v"(b2v));
    if (lead && threadIdx.x == 384) st32(rws, WS_FL + FL_EPOCH * 4, token);
    CBAR(6); RDO(qa0, qb0, qa1, qb1); MM2(aa, pa0, pb0, pa1, pb1);
    MM2(aa, qa0, qb0, qa1, qb1);
    const float og = 1.0f - g;
    float v[16];
#pragma unroll
    for (int i = 0; i < 16; ++i) v[i] = ay[i] * og + aa[i] * g;
    {
      lptr ex = L + L_K + w * 2048 + lane * 16;
      f32x4 s0, s1;
#pragma unroll
      for (int jj = 0; jj < 4; ++jj) { s0[jj] = kh ? v[jj] : v[8 + jj]; s1[jj] = kh ? v[4 + jj] : v[12 + jj]; }
      *(LAS f32x4*)(ex) = s0; *(LAS f32x4*)(ex + 1024) = s1;
    }
    LBAR();
    {
      lptr ex = L + L_K + (w ^ 4) * 2048 + lane * 16;
      const f32x4 r0 = *(const LAS f32x4*)(ex), r1 = *(const LAS f32x4*)(ex + 1024);
      const float bo = bflo(b2v) * og;
      float f[8];
#pragma unroll
      for (int jj = 0; jj < 4; ++jj) { f[jj] = (kh ? v[8 + jj] : v[jj]) + r0[jj] + bo; f[4 + jj] = (kh ? v[12 + jj] : v[4 + jj]) + r1[jj] + bo; }
      const unsigned a0 = pk2(f[0], f[1]), a1 = pk2(f[2], f[3]), c0 = pk2(f[4], f[5]), c1 = pk2(f[6], f[7]);
      auto x0 = __builtin_amdgcn_permlane32_swap(a0, c0, false, false);
      auto x1 = __builtin_amdgcn_permlane32_swap(a1, c1, false, false);
      const u32x4 ov = {x0[0], x1[0], x0[1], x1[1]};
      __builtin_amdgcn_raw_buffer_store_b128(ov, __builtin_amdgcn_make_buffer_rsrc(out, 0, 2048 * 1024, 0x00020000), tok * 1024 + (dt * 64 + mt * 32 + kh * 16 + h * 8) * 2, 0, 0);
    }
  }
}

extern "C" void kernel_launch(void* const* d_in, const int* in_sizes, int n_in, void* d_out, int out_size, void* d_ws, size_t ws_size,
                              hipStream_t stream) {
  (void)hipFuncSetAttribute((const void*)fused_kernel, hipFuncAttributeMaxDynamicSharedMemorySize, LDS_BYTES);
  fused_kernel<<<256, 768, LDS_BYTES, stream>>>((const char*)d_in[0], (const char*)d_in[1], (const char*)d_in[2], (const char*)d_in[4],
                                                (const char*)d_in[5], (const char*)d_in[6], (const char*)d_in[7], (const char*)d_in[8],
                                                (char*)d_ws, (char*)d_out);
}
```
